```python
import torch

def _softmax(x, axis=-1):
    return torch.softmax(x, dim=axis)
def _gelu(x):
    return torch.nn.functional.gelu(x, approximate="tanh")
def _silu(x):
    return torch.nn.functional.silu(x)
def _sigmoid(x):
    return torch.sigmoid(x)
def _layernorm(x, axis=-1, eps=1e-5):
    m = x.mean(dim=axis, keepdim=True)
    v = x.var(dim=axis, keepdim=True, unbiased=False)
    return (x - m) / torch.sqrt(v + eps)
def _scatter_add(tgt, idx, src, axis=0):
    return tgt.clone().index_add_(axis, idx, src)
def _segment_sum(src, seg, n):
    out = torch.zeros((n,) + src.shape[1:], dtype=src.dtype)
    return out.index_add_(0, seg, src)
def _pad(x, pw):
    flat = tuple(v for pair in reversed(pw) for v in pair)
    return torch.nn.functional.pad(x, flat)

kernel_name = 'recurrent_scan__sliding_block_cached__dense_activation_relu__prompt'

def setup_inputs():

    _w = (lambda a: (lambda t: t.to(torch.bfloat16) if t.is_floating_point() else t)(torch.as_tensor(a)))
    def _A_setup():
        B, Sq, D = 1, 2048, 448
        Hq, Hkv, Sk, Dh = 16, 8, 2048, 256
        assert Hq % Hkv == 0, (Hq, Hkv)
        return {
            "x": _w((lambda *s: torch.randn(*s, dtype=torch.bfloat16) * 0.05)(B, Sq, D)),
            "k_cache": _w((lambda *s: torch.randn(*s, dtype=torch.bfloat16) * 0.8)(B, Hkv, Sk, Dh)),
            "v_cache": _w((lambda *s: torch.randn(*s, dtype=torch.bfloat16) * 0.2)(B, Hkv, Sk, Dh)),
            "wq": _w((lambda *s: torch.randn(*s, dtype=torch.bfloat16) * 0.8)(D, Hq * Dh) * (D ** -0.5)),
            "wo": _w((lambda *s: torch.randn(*s, dtype=torch.bfloat16) * 0.2)(Hq * Dh, D) * ((Hq * Dh) ** -0.5)),
        }
    _A_in = _A_setup()

    def _B_setup():
        B, S, D, Dff = 1, 2048, 448, 1792
        return {
            "x": _w((lambda *s: torch.randn(*s, dtype=torch.bfloat16) * 0.05)(B, S, D)),
            "w1": _w((lambda *s: torch.randn(*s, dtype=torch.bfloat16) * 0.1)(D, Dff)),
            "w2": _w((lambda *s: torch.randn(*s, dtype=torch.bfloat16) * 0.1)(Dff, D)),
        }
    _B_in = _B_setup()

    return {
        "A_x": _A_in["x"],
        "A_k_cache": _A_in["k_cache"],
        "A_v_cache": _A_in["v_cache"],
        "A_wq": _A_in["wq"],
        "A_wo": _A_in["wo"],
        "B_x": _B_in["x"],
        "B_w1": _B_in["w1"],
        "B_w2": _B_in["w2"],
    }

def reference(A_x, A_k_cache, A_v_cache, A_wq, A_wo, B_x, B_w1, B_w2):
    def _A_ref(x, k_cache, v_cache, wq, wo):
        B, Sq, D = x.shape
        Hq, Dh = 16, 256
        Hkv, Sk = k_cache.shape[1], k_cache.shape[2]
        W = min(4096, max(256, Sk // 4))
        rep = Hq // Hkv
        q = (lambda x, *d: x.permute(*d))((x @ wq).reshape(B, Sq, Hq, Dh), 0, 2, 1, 3)
        k = (lambda x, n, axis: torch.repeat_interleave(x, n, dim=axis))(k_cache[:, :, Sk - W:, :], rep, axis=1)
        v = (lambda x, n, axis: torch.repeat_interleave(x, n, dim=axis))(v_cache[:, :, Sk - W:, :], rep, axis=1)
        scores = torch.einsum("bhqd,bhkd->bhqk", q, k) * (Dh ** -0.5)
        p = _softmax(scores, axis=-1)
        o = torch.einsum("bhqk,bhkd->bhqd", p, v)
        return (lambda x, *d: x.permute(*d))(o, 0, 2, 1, 3).reshape(B, Sq, Hq * Dh) @ wo

    def _B_ref(x, w1, w2):
        h = torch.einsum("bsd,df->bsf", x, w1)
        zero = torch.zeros_like(h)
        h = torch.maximum(h, zero)
        return torch.einsum("bsf,fd->bsd", h, w2)

    x = A_x
    xn = _layernorm(x, axis=-1)
    s = _A_ref(xn, A_k_cache, A_v_cache, A_wq, A_wo)
    g = _silu(_B_ref(xn, B_w1, B_w2))
    return x + s * g
```

```cpp
#include <hip/hip_runtime.h>
#include <hip/hip_bf16.h>
#include <stdint.h>
typedef unsigned short u16;
__device__ __forceinline__ float bf2f(u16 v){ return __uint_as_float(((unsigned)v)<<16); }
__device__ __forceinline__ u16 f2bf(float f){ unsigned u=__float_as_uint(f); u += 0x7fff + ((u>>16)&1); return (u16)(u>>16); }
__device__ __forceinline__ float rbf(float f){ return bf2f(f2bf(f)); }

__global__ void k_ln(const u16* x, u16* xn){
  int r=blockIdx.x; int t=threadIdx.x;
  float v[7]; float s=0;
  for(int i=0;i<7;i++){ v[i]=bf2f(x[r*448+t+64*i]); s+=v[i]; }
  for(int o=32;o>0;o>>=1) s+=__shfl_xor(s,o);
  float m=s/448.f; float q=0;
  for(int i=0;i<7;i++){ float d=v[i]-m; q+=d*d; }
  for(int o=32;o>0;o>>=1) q+=__shfl_xor(q,o);
  float rs=rsqrtf(q/448.f+1e-5f);
  for(int i=0;i<7;i++) xn[r*448+t+64*i]=f2bf((v[i]-m)*rs);
}
template<int RELU>
__global__ void k_gemm(const u16* A,const u16* B,u16* C,int M,int N,int K){
  __shared__ float As[16][65], Bs[16][65];
  int tx=threadIdx.x&15, ty=threadIdx.x>>4; int m0=blockIdx.y*64,n0=blockIdx.x*64;
  float acc[4][4]={};
  for(int k0=0;k0<K;k0+=16){
    for(int i=threadIdx.x;i<1024;i+=256){ int mm=i>>4,kk=i&15; As[kk][mm]=bf2f(A[(size_t)(m0+mm)*K+k0+kk]); int kb=i>>6,nn=i&63; Bs[kb][nn]=bf2f(B[(size_t)(k0+kb)*N+n0+nn]); }
    __syncthreads();
    for(int kk=0;kk<16;kk++){ float a[4],b[4]; for(int i=0;i<4;i++){a[i]=As[kk][ty*4+i]; b[i]=Bs[kk][tx*4+i];}
      for(int i=0;i<4;i++)for(int j=0;j<4;j++)acc[i][j]+=a[i]*b[j]; }
    __syncthreads();
  }
  for(int i=0;i<4;i++)for(int j=0;j<4;j++){ float v=acc[i][j]; if(RELU) v=fmaxf(v,0.f); C[(size_t)(m0+ty*4+i)*N+n0+tx*4+j]=f2bf(v);}
}
__global__ void k_attn(const u16* q,const u16* kc,const u16* vc,u16* o){
  int r=blockIdx.x,h=blockIdx.y,t=threadIdx.x; __shared__ float qs[256], p[512], red[8];
  qs[t]=bf2f(q[(size_t)r*4096+h*256+t]); __syncthreads();
  const u16* K=kc+((size_t)(h>>1)*2048+1536)*256; const u16* V=vc+((size_t)(h>>1)*2048+1536)*256;
  float sc[2]; for(int j=0;j<2;j++){ int key=t+256*j; float s=0; for(int d=0;d<256;d++) s+=qs[d]*bf2f(K[(size_t)key*256+d]); sc[j]=s*0.0625f; }
  float m=fmaxf(sc[0],sc[1]); for(int of=32;of>0;of>>=1) m=fmaxf(m,__shfl_xor(m,of));
  if((t&63)==0) red[t>>6]=m; __syncthreads(); m=fmaxf(fmaxf(red[0],red[1]),fmaxf(red[2],red[3])); __syncthreads();
  float e0=expf(sc[0]-m),e1=expf(sc[1]-m); float s=e0+e1; for(int of=32;of>0;of>>=1) s+=__shfl_xor(s,of);
  if((t&63)==0) red[4+(t>>6)]=s; __syncthreads(); s=red[4]+red[5]+red[6]+red[7];
  p[t]=rbf(e0/s); p[t+256]=rbf(e1/s); __syncthreads();
  float a=0; for(int k=0;k<512;k++) a+=p[k]*bf2f(V[(size_t)k*256+t]);
  o[(size_t)r*4096+h*256+t]=f2bf(a);
}
__global__ void k_fin(const u16* x,const u16* s,const u16* y,u16* out,int n){
  int i=blockIdx.x*256+threadIdx.x; if(i>=n) return; float yy=bf2f(y[i]); float g=rbf(yy/(1.f+expf(-yy))); out[i]=f2bf(bf2f(x[i])+rbf(bf2f(s[i])*g));
}
extern "C" void kernel_launch(void* const* d_in, const int* in_sizes, int n_in, void* d_out, int out_size, void* d_ws, size_t ws_size, hipStream_t stream){
  const u16* x=(const u16*)d_in[0]; const u16* kc=(const u16*)d_in[1]; const u16* vc=(const u16*)d_in[2]; const u16* wq=(const u16*)d_in[3]; const u16* wo=(const u16*)d_in[4];
  const u16* w1=(const u16*)d_in[6]; const u16* w2=(const u16*)d_in[7];
  u16* ws=(u16*)d_ws; u16* xn=ws; u16* q=xn+2048*448; u16* o=q+2048*4096; u16* h=o+2048*4096; u16* s=h+2048*1792; u16* y=s+2048*448;
  k_ln<<<2048,64,0,stream>>>(x,xn);
  k_gemm<0><<<dim3(64,32),256,0,stream>>>(xn,wq,q,2048,4096,448);
  k_gemm<1><<<dim3(28,32),256,0,stream>>>(xn,w1,h,2048,1792,448);
  k_attn<<<dim3(2048,16),256,0,stream>>>(q,kc,vc,o);
  k_gemm<0><<<dim3(7,32),256,0,stream>>>(o,wo,s,2048,448,4096);
  k_gemm<0><<<dim3(7,32),256,0,stream>>>(h,w2,y,2048,448,1792);
  k_fin<<<(2048*448+255)/256,256,0,stream>>>(x,s,y,(u16*)d_out,2048*448);
}
```

```cpp
#include <hip/hip_runtime.h>

__global__ __launch_bounds__(256, 1) void fused_block_kernel(
    const void* x, unsigned n_x, unsigned f_x,
    const void* wq, unsigned n_wq, unsigned f_wq,
    const void* w1, unsigned n_w1, unsigned f_w1,
    void* ws, unsigned n_ws, unsigned f_ws,
    const void* kc, unsigned n_kc, unsigned f_kc,
    const void* vc, unsigned n_vc, unsigned f_vc,
    const void* w2, unsigned n_w2, unsigned f_w2,
    const void* wo, unsigned n_wo, unsigned f_wo,
    void* out, unsigned n_out, unsigned f_out) {
  unsigned tid = threadIdx.x;
  asm volatile(
"s_load_dwordx16 s[8:23], %[karg], 0x0\n"
"s_getpc_b64 s[28:29]\n"
"L_pc_%=:\n"
"v_and_b32 v7, 63, v0\n"
"v_lshrrev_b32 v1, 6, v0\n"
"s_lshr_b32 s73, %[bid], 3\n"
"v_readfirstlane_b32 s60, v1\n"
"s_mov_b32 s30, L_end_%=-L_pc_%=\n"
"s_mov_b32 s31, 0x20000\n"
"s_lshl_b32 s74, s73, 2\n"
"s_add_u32 s74, s74, s60\n"
"s_and_b32 s74, s74, 15\n"
"s_lshl_b32 s74, s74, 12\n"
"v_lshlrev_b32 v45, 6, v7\n"
"buffer_load_dword v59, v45, s[28:31], s74 offen\n"
"s_and_b32 s72, %[bid], 7\n"
"v_and_b32 v32, 15, v7\n"
"v_lshrrev_b32 v33, 4, v7\n"
"v_mul_u32_u24 v34, 896, v32\n"
"v_lshl_add_u32 v34, v33, 4, v34\n"
"s_lshl_b32 s86, s73, 2\n"
"s_add_u32 s86, s86, s60\n"
"s_mul_i32 s76, s86, 14336\n"
"s_waitcnt lgkmcnt(0)\n"
"buffer_load_dwordx4 v[192:195], v34, s[8:11], s76 offen offset:0\n"
"buffer_load_dwordx4 v[196:199], v34, s[8:11], s76 offen offset:64\n"
"buffer_load_dwordx4 v[200:203], v34, s[8:11], s76 offen offset:128\n"
"buffer_load_dwordx4 v[204:207], v34, s[8:11], s76 offen offset:192\n"
"buffer_load_dwordx4 v[208:211], v34, s[8:11], s76 offen offset:256\n"
"buffer_load_dwordx4 v[212:215], v34, s[8:11], s76 offen offset:320\n"
"buffer_load_dwordx4 v[216:219], v34, s[8:11], s76 offen offset:384\n"
"buffer_load_dwordx4 v[220:223], v34, s[8:11], s76 offen offset:448\n"
"buffer_load_dwordx4 v[224:227], v34, s[8:11], s76 offen offset:512\n"
"buffer_load_dwordx4 v[228:231], v34, s[8:11], s76 offen offset:576\n"
"buffer_load_dwordx4 v[232:235], v34, s[8:11], s76 offen offset:640\n"
"buffer_load_dwordx4 v[236:239], v34, s[8:11], s76 offen offset:704\n"
"buffer_load_dwordx4 v[240:243], v34, s[8:11], s76 offen offset:768\n"
"buffer_load_dwordx4 v[244:247], v34, s[8:11], s76 offen offset:832\n"
"s_load_dwordx16 s[36:51], %[karg], 0x40\n"
"s_load_dwordx4 s[24:27], %[karg], 0x80\n"
"s_mul_i32 s62, s72, 448\n"
"s_and_b32 s62, s62, 0xffffff80\n"
"s_lshl_b32 s63, s72, 10\n"
"s_and_b32 s86, s86, 127\n"
"s_mov_b32 s80, s62\n"
"s_mov_b32 s81, s63\n"
"v_lshrrev_b32 v42, 3, v7\n"
"v_and_b32 v43, 7, v7\n"
"v_lshlrev_b32 v44, 13, v42\n"
"v_lshl_add_u32 v44, v43, 7, v44\n"
"v_lshrrev_b32 v42, 2, v7\n"
"v_and_b32 v43, 3, v7\n"
"v_mul_u32_u24 v46, 3584, v42\n"
"v_lshl_add_u32 v46, v43, 7, v46\n"
"v_mov_b32 v47, 0x40000000\n"
"s_mul_i32 s85, s86, 4\n"
"s_min_u32 s85, s85, 444\n"
"s_add_u32 s85, s85, 0\n"
"v_cmp_gt_u32 vcc, 32, v7\n"
"s_mul_i32 s87, s85, 8192\n"
"s_nop 0\n"
"v_cndmask_b32 v58, v47, v44, vcc\n"
"s_add_u32 s87, s87, s81\n"
"buffer_load_dword v59, v58, s[12:15], s87 offen\n"
"v_cmp_gt_u32 vcc, 16, v7\n"
"s_mul_i32 s87, s85, 3584\n"
"s_nop 0\n"
"v_cndmask_b32 v58, v47, v46, vcc\n"
"s_add_u32 s87, s87, s80\n"
"buffer_load_dword v59, v58, s[16:19], s87 offen\n"
"s_lshl_b32 s61, s60, 12\n"
"v_lshrrev_b32 v35, 3, v7\n"
"v_and_b32 v36, 7, v7\n"
"v_and_b32 v0, 6, v35\n"
"v_xor_b32 v36, v36, v0\n"
"v_lshrrev_b32 v37, 2, v35\n"
"v_and_b32 v0, 3, v35\n"
"v_lshl_add_u32 v37, v37, 3, v0\n"
"v_mul_u32_u24 v38, 3584, v37\n"
"v_lshl_add_u32 v16, v36, 4, v38\n"
"s_and_b32 s82, s72, 1\n"
"s_mul_i32 s82, s82, 28\n"
"s_lshl_b32 s83, s60, 3\n"
"s_add_u32 s82, s82, s83\n"
"v_add_u32 v0, s82, v36\n"
"v_and_b32 v0, 28, v0\n"
"v_cmp_eq_u32 vcc, 28, v0\n"
"v_mov_b32 v0, 0x40000000\n"
"s_nop 0\n"
"v_cndmask_b32 v16, v16, v0, vcc\n"
"v_add_u32 v17, 56320, v16\n"
"v_add_u32 v18, 12288, v16\n"
"v_add_u32 v19, 68608, v16\n"
"v_lshlrev_b32 v38, 13, v37\n"
"v_lshl_add_u32 v20, v36, 4, v38\n"
"v_add_u32 v21, 130048, v20\n"
"v_add_u32 v22, 30720, v20\n"
"v_add_u32 v23, 160768, v20\n"
"s_and_b32 s74, s60, 1\n"
"s_lshr_b32 s75, s60, 1\n"
"s_lshl_b32 s66, s60, 7\n"
"s_add_u32 s62, s62, s66\n"
"s_lshl_b32 s66, s75, 9\n"
"s_add_u32 s63, s63, s66\n"
"s_lshl_b32 s66, s74, 7\n"
"s_add_u32 s63, s63, s66\n"
"v_lshlrev_b32 v1, 4, v7\n"
"v_lshrrev_b32 v38, 2, v7\n"
"v_and_b32 v38, 7, v38\n"
"v_lshrrev_b32 v39, 5, v7\n"
"v_lshlrev_b32 v39, 10, v39\n"
"v_lshl_add_u32 v39, v38, 7, v39\n"
"v_and_b32 v0, 3, v7\n"
"v_lshl_add_u32 v39, v0, 3, v39\n"
"v_lshrrev_b32 v38, 1, v38\n"
"s_add_u32 s66, s61, 65536\n"
"v_xor_b32 v0, 0, v38\n"
"v_lshl_add_u32 v0, v0, 5, v39\n"
"v_add_u32 v8, s66, v0\n"
"v_add_u32 v12, 65536, v8\n"
"v_xor_b32 v0, 1, v38\n"
"v_lshl_add_u32 v0, v0, 5, v39\n"
"v_add_u32 v9, s66, v0\n"
"v_add_u32 v13, 65536, v9\n"
"v_xor_b32 v0, 2, v38\n"
"v_lshl_add_u32 v0, v0, 5, v39\n"
"v_add_u32 v10, s66, v0\n"
"v_add_u32 v14, 65536, v10\n"
"v_xor_b32 v0, 3, v38\n"
"v_lshl_add_u32 v0, v0, 5, v39\n"
"v_add_u32 v11, s66, v0\n"
"v_add_u32 v15, 65536, v11\n"
"v_xor_b32 v0, s60, v38\n"
"v_lshl_add_u32 v3, v0, 5, v39\n"
"s_lshl_b32 s67, s60, 10\n"
"v_add_u32 v4, s67, v1\n"
"v_lshlrev_b32 v39, 2, v33\n"
"v_sub_u32 v39, v32, v39\n"
"v_cmp_eq_u32 vcc, 0, v39\n"
"v_mov_b32 v40, 0x3f803f80\n"
"s_nop 0\n"
"v_cndmask_b32 v52, 0, 1.0, vcc\n"
"v_cmp_eq_u32 vcc, 1, v39\n"
"v_mov_b32 v41, 0x3f803f80\n"
"s_nop 0\n"
"v_cndmask_b32 v53, 0, 1.0, vcc\n"
"v_cmp_eq_u32 vcc, 2, v39\n"
"v_mov_b32 v42, 0x3f803f80\n"
"s_nop 0\n"
"v_cndmask_b32 v54, 0, 1.0, vcc\n"
"v_cmp_eq_u32 vcc, 3, v39\n"
"v_mov_b32 v43, 0x3f803f80\n"
"s_nop 0\n"
"v_cndmask_b32 v55, 0, 1.0, vcc\n"
"v_mov_b32 v56, 1.0\n"
"s_waitcnt vmcnt(15)\n"
"v_mfma_f32_16x16x32_bf16 v[48:51], v[40:43], v[192:195], 0\n"
"v_mfma_f32_16x16x32_bf16 v[60:63], v[192:195], v[192:195], 0\n"
"s_waitcnt vmcnt(14)\n"
"v_mfma_f32_16x16x32_bf16 v[48:51], v[40:43], v[196:199], v[48:51]\n"
"v_mfma_f32_16x16x32_bf16 v[60:63], v[196:199], v[196:199], v[60:63]\n"
"s_waitcnt vmcnt(13)\n"
"v_mfma_f32_16x16x32_bf16 v[48:51], v[40:43], v[200:203], v[48:51]\n"
"v_mfma_f32_16x16x32_bf16 v[60:63], v[200:203], v[200:203], v[60:63]\n"
"s_waitcnt vmcnt(12)\n"
"v_mfma_f32_16x16x32_bf16 v[48:51], v[40:43], v[204:207], v[48:51]\n"
"v_mfma_f32_16x16x32_bf16 v[60:63], v[204:207], v[204:207], v[60:63]\n"
"s_waitcnt vmcnt(11)\n"
"v_mfma_f32_16x16x32_bf16 v[48:51], v[40:43], v[208:211], v[48:51]\n"
"v_mfma_f32_16x16x32_bf16 v[60:63], v[208:211], v[208:211], v[60:63]\n"
"s_waitcnt vmcnt(10)\n"
"v_mfma_f32_16x16x32_bf16 v[48:51], v[40:43], v[212:215], v[48:51]\n"
"v_mfma_f32_16x16x32_bf16 v[60:63], v[212:215], v[212:215], v[60:63]\n"
"s_waitcnt vmcnt(9)\n"
"v_mfma_f32_16x16x32_bf16 v[48:51], v[40:43], v[216:219], v[48:51]\n"
"v_mfma_f32_16x16x32_bf16 v[60:63], v[216:219], v[216:219], v[60:63]\n"
"s_waitcnt vmcnt(8)\n"
"v_mfma_f32_16x16x32_bf16 v[48:51], v[40:43], v[220:223], v[48:51]\n"
"v_mfma_f32_16x16x32_bf16 v[60:63], v[220:223], v[220:223], v[60:63]\n"
"s_waitcnt vmcnt(7)\n"
"v_mfma_f32_16x16x32_bf16 v[48:51], v[40:43], v[224:227], v[48:51]\n"
"v_mfma_f32_16x16x32_bf16 v[60:63], v[224:227], v[224:227], v[60:63]\n"
"s_waitcnt vmcnt(6)\n"
"v_mfma_f32_16x16x32_bf16 v[48:51], v[40:43], v[228:231], v[48:51]\n"
"v_mfma_f32_16x16x32_bf16 v[60:63], v[228:231], v[228:231], v[60:63]\n"
"s_waitcnt vmcnt(5)\n"
"v_mfma_f32_16x16x32_bf16 v[48:51], v[40:43], v[232:235], v[48:51]\n"
"v_mfma_f32_16x16x32_bf16 v[60:63], v[232:235], v[232:235], v[60:63]\n"
"s_waitcnt vmcnt(4)\n"
"v_mfma_f32_16x16x32_bf16 v[48:51], v[40:43], v[236:239], v[48:51]\n"
"v_mfma_f32_16x16x32_bf16 v[60:63], v[236:239], v[236:239], v[60:63]\n"
"s_waitcnt vmcnt(3)\n"
"v_mfma_f32_16x16x32_bf16 v[48:51], v[40:43], v[240:243], v[48:51]\n"
"v_mfma_f32_16x16x32_bf16 v[60:63], v[240:243], v[240:243], v[60:63]\n"
"s_waitcnt vmcnt(2)\n"
"v_mfma_f32_16x16x32_bf16 v[48:51], v[40:43], v[244:247], v[48:51]\n"
"v_mfma_f32_16x16x32_bf16 v[60:63], v[244:247], v[244:247], v[60:63]\n"
"s_nop 7\n"
"v_mul_f32 v57, v60, v52\n"
"v_fmac_f32 v57, v61, v53\n"
"v_fmac_f32 v57, v62, v54\n"
"v_fmac_f32 v57, v63, v55\n"
"s_nop 1\n"
"v_mfma_f32_16x16x4_f32 v[60:63], v56, v57, 0\n"
"v_mul_f32 v48, 0x3b124925, v48\n"
"v_mul_f32 v49, v48, v48\n"
"v_mov_b32 v50, 0x3b124925\n"
"s_add_u32 m0, s61, 147456\n"
"s_nop 0\n"
"buffer_load_dwordx4 v16, s[16:19], s62 offen offset:0 lds\n"
"s_nop 3\n"
"v_fma_f32 v49, v60, v50, -v49\n"
"v_max_f32 v49, 0, v49\n"
"v_add_f32 v49, 0x3727c5ac, v49\n"
"v_rsq_f32 v49, v49\n"
"s_nop 0\n"
"v_mul_f32 v5, 0x3db8aa3b, v49\n"
"v_mul_f32 v6, v48, v5\n"
"v_sub_f32 v6, 0, v6\n"
"v_lshlrev_b32 v24, 16, v192\n"
"v_and_b32 v25, 0xffff0000, v192\n"
"v_fma_f32 v24, v24, v5, v6\n"
"v_fma_f32 v25, v25, v5, v6\n"
"v_cvt_pk_bf16_f32 v192, v24, v25\n"
"v_lshlrev_b32 v26, 16, v193\n"
"v_and_b32 v27, 0xffff0000, v193\n"
"v_fma_f32 v26, v26, v5, v6\n"
"v_fma_f32 v27, v27, v5, v6\n"
"v_cvt_pk_bf16_f32 v193, v26, v27\n"
"v_lshlrev_b32 v24, 16, v194\n"
"v_and_b32 v25, 0xffff0000, v194\n"
"v_fma_f32 v24, v24, v5, v6\n"
"v_fma_f32 v25, v25, v5, v6\n"
"v_cvt_pk_bf16_f32 v194, v24, v25\n"
"v_lshlrev_b32 v26, 16, v195\n"
"v_and_b32 v27, 0xffff0000, v195\n"
"v_fma_f32 v26, v26, v5, v6\n"
"v_fma_f32 v27, v27, v5, v6\n"
"v_cvt_pk_bf16_f32 v195, v26, v27\n"
"ds_write_b128 v4, v[192:195] offset:0\n"
"v_lshlrev_b32 v24, 16, v196\n"
"v_and_b32 v25, 0xffff0000, v196\n"
"v_fma_f32 v24, v24, v5, v6\n"
"v_fma_f32 v25, v25, v5, v6\n"
"v_cvt_pk_bf16_f32 v196, v24, v25\n"
"v_lshlrev_b32 v26, 16, v197\n"
"v_and_b32 v27, 0xffff0000, v197\n"
"v_fma_f32 v26, v26, v5, v6\n"
"v_fma_f32 v27, v27, v5, v6\n"
"v_cvt_pk_bf16_f32 v197, v26, v27\n"
"v_lshlrev_b32 v24, 16, v198\n"
"v_and_b32 v25, 0xffff0000, v198\n"
"v_fma_f32 v24, v24, v5, v6\n"
"v_fma_f32 v25, v25, v5, v6\n"
"v_cvt_pk_bf16_f32 v198, v24, v25\n"
"v_lshlrev_b32 v26, 16, v199\n"
"v_and_b32 v27, 0xffff0000, v199\n"
"v_fma_f32 v26, v26, v5, v6\n"
"v_fma_f32 v27, v27, v5, v6\n"
"v_cvt_pk_bf16_f32 v199, v26, v27\n"
"ds_write_b128 v4, v[196:199] offset:4096\n"
"v_lshlrev_b32 v24, 16, v200\n"
"v_and_b32 v25, 0xffff0000, v200\n"
"v_fma_f32 v24, v24, v5, v6\n"
"v_fma_f32 v25, v25, v5, v6\n"
"v_cvt_pk_bf16_f32 v200, v24, v25\n"
"v_lshlrev_b32 v26, 16, v201\n"
"v_and_b32 v27, 0xffff0000, v201\n"
"v_fma_f32 v26, v26, v5, v6\n"
"v_fma_f32 v27, v27, v5, v6\n"
"v_cvt_pk_bf16_f32 v201, v26, v27\n"
"v_lshlrev_b32 v24, 16, v202\n"
"v_and_b32 v25, 0xffff0000, v202\n"
"v_fma_f32 v24, v24, v5, v6\n"
"v_fma_f32 v25, v25, v5, v6\n"
"v_cvt_pk_bf16_f32 v202, v24, v25\n"
"v_lshlrev_b32 v26, 16, v203\n"
"v_and_b32 v27, 0xffff0000, v203\n"
"v_fma_f32 v26, v26, v5, v6\n"
"v_fma_f32 v27, v27, v5, v6\n"
"v_cvt_pk_bf16_f32 v203, v26, v27\n"
"ds_write_b128 v4, v[200:203] offset:8192\n"
"v_lshlrev_b32 v24, 16, v204\n"
"v_and_b32 v25, 0xffff0000, v204\n"
"v_fma_f32 v24, v24, v5, v6\n"
"v_fma_f32 v25, v25, v5, v6\n"
"v_cvt_pk_bf16_f32 v204, v24, v25\n"
"v_lshlrev_b32 v26, 16, v205\n"
"v_and_b32 v27, 0xffff0000, v205\n"
"v_fma_f32 v26, v26, v5, v6\n"
"v_fma_f32 v27, v27, v5, v6\n"
"v_cvt_pk_bf16_f32 v205, v26, v27\n"
"v_lshlrev_b32 v24, 16, v206\n"
"v_and_b32 v25, 0xffff0000, v206\n"
"v_fma_f32 v24, v24, v5, v6\n"
"v_fma_f32 v25, v25, v5, v6\n"
"v_cvt_pk_bf16_f32 v206, v24, v25\n"
"v_lshlrev_b32 v26, 16, v207\n"
"v_and_b32 v27, 0xffff0000, v207\n"
"v_fma_f32 v26, v26, v5, v6\n"
"v_fma_f32 v27, v27, v5, v6\n"
"v_cvt_pk_bf16_f32 v207, v26, v27\n"
"ds_write_b128 v4, v[204:207] offset:12288\n"
"v_lshlrev_b32 v24, 16, v208\n"
"v_and_b32 v25, 0xffff0000, v208\n"
"v_fma_f32 v24, v24, v5, v6\n"
"v_fma_f32 v25, v25, v5, v6\n"
"v_cvt_pk_bf16_f32 v208, v24, v25\n"
"v_lshlrev_b32 v26, 16, v209\n"
"v_and_b32 v27, 0xffff0000, v209\n"
"v_fma_f32 v26, v26, v5, v6\n"
"v_fma_f32 v27, v27, v5, v6\n"
"v_cvt_pk_bf16_f32 v209, v26, v27\n"
"v_lshlrev_b32 v24, 16, v210\n"
"v_and_b32 v25, 0xffff0000, v210\n"
"v_fma_f32 v24, v24, v5, v6\n"
"v_fma_f32 v25, v25, v5, v6\n"
"v_cvt_pk_bf16_f32 v210, v24, v25\n"
"v_lshlrev_b32 v26, 16, v211\n"
"v_and_b32 v27, 0xffff0000, v211\n"
"v_fma_f32 v26, v26, v5, v6\n"
"v_fma_f32 v27, v27, v5, v6\n"
"v_cvt_pk_bf16_f32 v211, v26, v27\n"
"ds_write_b128 v4, v[208:211] offset:16384\n"
"v_lshlrev_b32 v24, 16, v212\n"
"v_and_b32 v25, 0xffff0000, v212\n"
"v_fma_f32 v24, v24, v5, v6\n"
"v_fma_f32 v25, v25, v5, v6\n"
"v_cvt_pk_bf16_f32 v212, v24, v25\n"
"v_lshlrev_b32 v26, 16, v213\n"
"v_and_b32 v27, 0xffff0000, v213\n"
"v_fma_f32 v26, v26, v5, v6\n"
"v_fma_f32 v27, v27, v5, v6\n"
"v_cvt_pk_bf16_f32 v213, v26, v27\n"
"v_lshlrev_b32 v24, 16, v214\n"
"v_and_b32 v25, 0xffff0000, v214\n"
"v_fma_f32 v24, v24, v5, v6\n"
"v_fma_f32 v25, v25, v5, v6\n"
"v_cvt_pk_bf16_f32 v214, v24, v25\n"
"v_lshlrev_b32 v26, 16, v215\n"
"v_and_b32 v27, 0xffff0000, v215\n"
"v_fma_f32 v26, v26, v5, v6\n"
"v_fma_f32 v27, v27, v5, v6\n"
"v_cvt_pk_bf16_f32 v215, v26, v27\n"
"ds_write_b128 v4, v[212:215] offset:20480\n"
"v_lshlrev_b32 v24, 16, v216\n"
"v_and_b32 v25, 0xffff0000, v216\n"
"v_fma_f32 v24, v24, v5, v6\n"
"v_fma_f32 v25, v25, v5, v6\n"
"v_cvt_pk_bf16_f32 v216, v24, v25\n"
"v_lshlrev_b32 v26, 16, v217\n"
"v_and_b32 v27, 0xffff0000, v217\n"
"v_fma_f32 v26, v26, v5, v6\n"
"v_fma_f32 v27, v27, v5, v6\n"
"v_cvt_pk_bf16_f32 v217, v26, v27\n"
"v_lshlrev_b32 v24, 16, v218\n"
"v_and_b32 v25, 0xffff0000, v218\n"
"v_fma_f32 v24, v24, v5, v6\n"
"v_fma_f32 v25, v25, v5, v6\n"
"v_cvt_pk_bf16_f32 v218, v24, v25\n"
"v_lshlrev_b32 v26, 16, v219\n"
"v_and_b32 v27, 0xffff0000, v219\n"
"v_fma_f32 v26, v26, v5, v6\n"
"v_fma_f32 v27, v27, v5, v6\n"
"v_cvt_pk_bf16_f32 v219, v26, v27\n"
"ds_write_b128 v4, v[216:219] offset:24576\n"
"v_lshlrev_b32 v24, 16, v220\n"
"v_and_b32 v25, 0xffff0000, v220\n"
"v_fma_f32 v24, v24, v5, v6\n"
"v_fma_f32 v25, v25, v5, v6\n"
"v_cvt_pk_bf16_f32 v220, v24, v25\n"
"v_lshlrev_b32 v26, 16, v221\n"
"v_and_b32 v27, 0xffff0000, v221\n"
"v_fma_f32 v26, v26, v5, v6\n"
"v_fma_f32 v27, v27, v5, v6\n"
"v_cvt_pk_bf16_f32 v221, v26, v27\n"
"v_lshlrev_b32 v24, 16, v222\n"
"v_and_b32 v25, 0xffff0000, v222\n"
"v_fma_f32 v24, v24, v5, v6\n"
"v_fma_f32 v25, v25, v5, v6\n"
"v_cvt_pk_bf16_f32 v222, v24, v25\n"
"v_lshlrev_b32 v26, 16, v223\n"
"v_and_b32 v27, 0xffff0000, v223\n"
"v_fma_f32 v26, v26, v5, v6\n"
"v_fma_f32 v27, v27, v5, v6\n"
"v_cvt_pk_bf16_f32 v223, v26, v27\n"
"ds_write_b128 v4, v[220:223] offset:28672\n"
"v_lshlrev_b32 v24, 16, v224\n"
"v_and_b32 v25, 0xffff0000, v224\n"
"v_fma_f32 v24, v24, v5, v6\n"
"v_fma_f32 v25, v25, v5, v6\n"
"v_cvt_pk_bf16_f32 v224, v24, v25\n"
"v_lshlrev_b32 v26, 16, v225\n"
"v_and_b32 v27, 0xffff0000, v225\n"
"v_fma_f32 v26, v26, v5, v6\n"
"v_fma_f32 v27, v27, v5, v6\n"
"v_cvt_pk_bf16_f32 v225, v26, v27\n"
"v_lshlrev_b32 v24, 16, v226\n"
"v_and_b32 v25, 0xffff0000, v226\n"
"v_fma_f32 v24, v24, v5, v6\n"
"v_fma_f32 v25, v25, v5, v6\n"
"v_cvt_pk_bf16_f32 v226, v24, v25\n"
"v_lshlrev_b32 v26, 16, v227\n"
"v_and_b32 v27, 0xffff0000, v227\n"
"v_fma_f32 v26, v26, v5, v6\n"
"v_fma_f32 v27, v27, v5, v6\n"
"v_cvt_pk_bf16_f32 v227, v26, v27\n"
"ds_write_b128 v4, v[224:227] offset:32768\n"
"v_lshlrev_b32 v24, 16, v228\n"
"v_and_b32 v25, 0xffff0000, v228\n"
"v_fma_f32 v24, v24, v5, v6\n"
"v_fma_f32 v25, v25, v5, v6\n"
"v_cvt_pk_bf16_f32 v228, v24, v25\n"
"v_lshlrev_b32 v26, 16, v229\n"
"v_and_b32 v27, 0xffff0000, v229\n"
"v_fma_f32 v26, v26, v5, v6\n"
"v_fma_f32 v27, v27, v5, v6\n"
"v_cvt_pk_bf16_f32 v229, v26, v27\n"
"v_lshlrev_b32 v24, 16, v230\n"
"v_and_b32 v25, 0xffff0000, v230\n"
"v_fma_f32 v24, v24, v5, v6\n"
"v_fma_f32 v25, v25, v5, v6\n"
"v_cvt_pk_bf16_f32 v230, v24, v25\n"
"v_lshlrev_b32 v26, 16, v231\n"
"v_and_b32 v27, 0xffff0000, v231\n"
"v_fma_f32 v26, v26, v5, v6\n"
"v_fma_f32 v27, v27, v5, v6\n"
"v_cvt_pk_bf16_f32 v231, v26, v27\n"
"ds_write_b128 v4, v[228:231] offset:36864\n"
"v_lshlrev_b32 v24, 16, v232\n"
"v_and_b32 v25, 0xffff0000, v232\n"
"v_fma_f32 v24, v24, v5, v6\n"
"v_fma_f32 v25, v25, v5, v6\n"
"v_cvt_pk_bf16_f32 v232, v24, v25\n"
"v_lshlrev_b32 v26, 16, v233\n"
"v_and_b32 v27, 0xffff0000, v233\n"
"v_fma_f32 v26, v26, v5, v6\n"
"v_fma_f32 v27, v27, v5, v6\n"
"v_cvt_pk_bf16_f32 v233, v26, v27\n"
"v_lshlrev_b32 v24, 16, v234\n"
"v_and_b32 v25, 0xffff0000, v234\n"
"v_fma_f32 v24, v24, v5, v6\n"
"v_fma_f32 v25, v25, v5, v6\n"
"v_cvt_pk_bf16_f32 v234, v24, v25\n"
"v_lshlrev_b32 v26, 16, v235\n"
"v_and_b32 v27, 0xffff0000, v235\n"
"v_fma_f32 v26, v26, v5, v6\n"
"v_fma_f32 v27, v27, v5, v6\n"
"v_cvt_pk_bf16_f32 v235, v26, v27\n"
"ds_write_b128 v4, v[232:235] offset:40960\n"
"v_lshlrev_b32 v24, 16, v236\n"
"v_and_b32 v25, 0xffff0000, v236\n"
"v_fma_f32 v24, v24, v5, v6\n"
"v_fma_f32 v25, v25, v5, v6\n"
"v_cvt_pk_bf16_f32 v236, v24, v25\n"
"v_lshlrev_b32 v26, 16, v237\n"
"v_and_b32 v27, 0xffff0000, v237\n"
"v_fma_f32 v26, v26, v5, v6\n"
"v_fma_f32 v27, v27, v5, v6\n"
"v_cvt_pk_bf16_f32 v237, v26, v27\n"
"v_lshlrev_b32 v24, 16, v238\n"
"v_and_b32 v25, 0xffff0000, v238\n"
"v_fma_f32 v24, v24, v5, v6\n"
"v_fma_f32 v25, v25, v5, v6\n"
"v_cvt_pk_bf16_f32 v238, v24, v25\n"
"v_lshlrev_b32 v26, 16, v239\n"
"v_and_b32 v27, 0xffff0000, v239\n"
"v_fma_f32 v26, v26, v5, v6\n"
"v_fma_f32 v27, v27, v5, v6\n"
"v_cvt_pk_bf16_f32 v239, v26, v27\n"
"ds_write_b128 v4, v[236:239] offset:45056\n"
"v_lshlrev_b32 v24, 16, v240\n"
"v_and_b32 v25, 0xffff0000, v240\n"
"v_fma_f32 v24, v24, v5, v6\n"
"v_fma_f32 v25, v25, v5, v6\n"
"v_cvt_pk_bf16_f32 v240, v24, v25\n"
"v_lshlrev_b32 v26, 16, v241\n"
"v_and_b32 v27, 0xffff0000, v241\n"
"v_fma_f32 v26, v26, v5, v6\n"
"v_fma_f32 v27, v27, v5, v6\n"
"v_cvt_pk_bf16_f32 v241, v26, v27\n"
"v_lshlrev_b32 v24, 16, v242\n"
"v_and_b32 v25, 0xffff0000, v242\n"
"v_fma_f32 v24, v24, v5, v6\n"
"v_fma_f32 v25, v25, v5, v6\n"
"v_cvt_pk_bf16_f32 v242, v24, v25\n"
"v_lshlrev_b32 v26, 16, v243\n"
"v_and_b32 v27, 0xffff0000, v243\n"
"v_fma_f32 v26, v26, v5, v6\n"
"v_fma_f32 v27, v27, v5, v6\n"
"v_cvt_pk_bf16_f32 v243, v26, v27\n"
"ds_write_b128 v4, v[240:243] offset:49152\n"
"v_lshlrev_b32 v24, 16, v244\n"
"v_and_b32 v25, 0xffff0000, v244\n"
"v_fma_f32 v24, v24, v5, v6\n"
"v_fma_f32 v25, v25, v5, v6\n"
"v_cvt_pk_bf16_f32 v244, v24, v25\n"
"v_lshlrev_b32 v26, 16, v245\n"
"v_and_b32 v27, 0xffff0000, v245\n"
"v_fma_f32 v26, v26, v5, v6\n"
"v_fma_f32 v27, v27, v5, v6\n"
"v_cvt_pk_bf16_f32 v245, v26, v27\n"
"v_lshlrev_b32 v24, 16, v246\n"
"v_and_b32 v25, 0xffff0000, v246\n"
"v_fma_f32 v24, v24, v5, v6\n"
"v_fma_f32 v25, v25, v5, v6\n"
"v_cvt_pk_bf16_f32 v246, v24, v25\n"
"v_lshlrev_b32 v26, 16, v247\n"
"v_and_b32 v27, 0xffff0000, v247\n"
"v_fma_f32 v26, v26, v5, v6\n"
"v_fma_f32 v27, v27, v5, v6\n"
"v_cvt_pk_bf16_f32 v247, v26, v27\n"
"ds_write_b128 v4, v[244:247] offset:53248\n"
"buffer_load_dwordx4 v17, s[16:19], s62 offen offset:1024 lds\n"
"buffer_load_dwordx4 v18, s[16:19], s62 offen offset:2048 lds\n"
"buffer_load_dwordx4 v19, s[16:19], s62 offen offset:3072 lds\n"
"s_add_u32 s62, s62, 114688\n"
"s_add_u32 m0, s61, 65536\n"
"s_nop 0\n"
"buffer_load_dwordx4 v20, s[12:15], s63 offen offset:0 lds\n"
"buffer_load_dwordx4 v21, s[12:15], s63 offen offset:1024 lds\n"
"buffer_load_dwordx4 v22, s[12:15], s63 offen offset:2048 lds\n"
"buffer_load_dwordx4 v23, s[12:15], s63 offen offset:3072 lds\n"
"s_add_u32 s63, s63, 262144\n"
"s_add_u32 m0, s61, 114688\n"
"s_nop 0\n"
"buffer_load_dwordx4 v16, s[16:19], s62 offen offset:0 lds\n"
"buffer_load_dwordx4 v17, s[16:19], s62 offen offset:1024 lds\n"
"buffer_load_dwordx4 v18, s[16:19], s62 offen offset:2048 lds\n"
"buffer_load_dwordx4 v19, s[16:19], s62 offen offset:3072 lds\n"
"s_add_u32 s62, s62, 114688\n"
"s_add_u32 m0, s61, 131072\n"
"s_nop 0\n"
"buffer_load_dwordx4 v20, s[12:15], s63 offen offset:0 lds\n"
"buffer_load_dwordx4 v21, s[12:15], s63 offen offset:1024 lds\n"
"buffer_load_dwordx4 v22, s[12:15], s63 offen offset:2048 lds\n"
"buffer_load_dwordx4 v23, s[12:15], s63 offen offset:3072 lds\n"
"s_add_u32 s63, s63, 262144\n"
"s_waitcnt lgkmcnt(0)\n"
"s_barrier\n"
"s_add_u32 m0, s61, 81920\n"
"ds_read_b128 a[0:3], v1 offset:0\n"
"s_waitcnt vmcnt(12)\n"
"ds_read_b64_tr_b16 a[224:225], v12 offset:16384\n"
"ds_read_b64_tr_b16 a[226:227], v12 offset:18432\n"
"ds_read_b128 a[4:7], v1 offset:1024\n"
"ds_read_b128 a[8:11], v1 offset:2048\n"
"ds_read_b128 a[12:15], v1 offset:3072\n"
"ds_read_b64_tr_b16 a[228:229], v13 offset:16384\n"
"ds_read_b64_tr_b16 a[230:231], v13 offset:18432\n"
"ds_read_b64_tr_b16 a[232:233], v14 offset:16384\n"
"ds_read_b64_tr_b16 a[234:235], v14 offset:18432\n"
"s_waitcnt lgkmcnt(7)\n"
"v_mfma_f32_16x16x32_bf16 v[64:67], a[224:227], a[0:3], 0\n"
"buffer_load_dwordx4 v16, s[16:19], s62 offen offset:0 lds\n"
"s_waitcnt lgkmcnt(6)\n"
"v_mfma_f32_16x16x32_bf16 v[68:71], a[224:227], a[4:7], 0\n"
"ds_read_b64_tr_b16 a[236:237], v15 offset:16384\n"
"ds_read_b64_tr_b16 a[238:239], v15 offset:18432\n"
"s_waitcnt lgkmcnt(7)\n"
"v_mfma_f32_16x16x32_bf16 v[72:75], a[224:227], a[8:11], 0\n"
"s_lshl_b32 s80, s74, 11\n"
"s_waitcnt lgkmcnt(6)\n"
"v_mfma_f32_16x16x32_bf16 v[76:79], a[224:227], a[12:15], 0\n"
"buffer_load_dwordx4 v17, s[16:19], s62 offen offset:1024 lds\n"
"s_waitcnt lgkmcnt(4)\n"
"v_mfma_f32_16x16x32_bf16 v[80:83], a[228:231], a[0:3], 0\n"
"s_lshl_b32 s81, s75, 14\n"
"s_add_u32 s80, s80, s81\n"
"v_mfma_f32_16x16x32_bf16 v[84:87], a[228:231], a[4:7], 0\n"
"s_waitcnt vmcnt(10)\n"
"ds_read_b64_tr_b16 a[240:241], v8 offset:0\n"
"ds_read_b64_tr_b16 a[242:243], v8 offset:2048\n"
"v_mfma_f32_16x16x32_bf16 v[88:91], a[228:231], a[8:11], 0\n"
"buffer_load_dwordx4 v18, s[16:19], s62 offen offset:2048 lds\n"
"v_mfma_f32_16x16x32_bf16 v[92:95], a[228:231], a[12:15], 0\n"
"v_add_u32 v2, s80, v1\n"
"s_waitcnt lgkmcnt(4)\n"
"v_mfma_f32_16x16x32_bf16 v[96:99], a[232:235], a[0:3], 0\n"
"ds_read_b64_tr_b16 a[244:245], v9 offset:0\n"
"s_lshl_b32 s68, s74, 13\n"
"v_mfma_f32_16x16x32_bf16 v[100:103], a[232:235], a[4:7], 0\n"
"buffer_load_dwordx4 v19, s[16:19], s62 offen offset:3072 lds\n"
"ds_read_b64_tr_b16 a[246:247], v9 offset:2048\n"
"s_add_u32 m0, s61, 98304\n"
"v_mfma_f32_16x16x32_bf16 v[104:107], a[232:235], a[8:11], 0\n"
"ds_read_b64_tr_b16 a[248:249], v10 offset:0\n"
"s_add_u32 s62, s62, 114688\n"
"v_mfma_f32_16x16x32_bf16 v[108:111], a[232:235], a[12:15], 0\n"
"ds_read_b64_tr_b16 a[250:251], v10 offset:2048\n"
"ds_read_b64_tr_b16 a[252:253], v11 offset:0\n"
"s_waitcnt lgkmcnt(7)\n"
"v_mfma_f32_16x16x32_bf16 v[112:115], a[236:239], a[0:3], 0\n"
"buffer_load_dwordx4 v20, s[12:15], s63 offen offset:0 lds\n"
"v_mfma_f32_16x16x32_bf16 v[116:119], a[236:239], a[4:7], 0\n"
"ds_read_b64_tr_b16 a[254:255], v11 offset:2048\n"
"s_waitcnt vmcnt(9)\n"
"ds_read_b64_tr_b16 a[224:225], v8 offset:49152\n"
"v_mfma_f32_16x16x32_bf16 v[120:123], a[236:239], a[8:11], 0\n"
"ds_read_b128 a[16:19], v1 offset:4096\n"
"s_sub_u32 s80, s60, 0\n"
"v_mfma_f32_16x16x32_bf16 v[124:127], a[236:239], a[12:15], 0\n"
"buffer_load_dwordx4 v21, s[12:15], s63 offen offset:1024 lds\n"
"s_waitcnt lgkmcnt(8)\n"
"v_mfma_f32_16x16x32_bf16 v[128:131], a[240:243], a[0:3], 0\n"
"ds_read_b64_tr_b16 a[226:227], v8 offset:51200\n"
"ds_read_b64_tr_b16 a[228:229], v9 offset:49152\n"
"v_mfma_f32_16x16x32_bf16 v[132:135], a[240:243], a[4:7], 0\n"
"ds_read_b128 a[20:23], v1 offset:5120\n"
"s_and_b32 s80, s80, 3\n"
"v_mfma_f32_16x16x32_bf16 v[136:139], a[240:243], a[8:11], 0\n"
"buffer_load_dwordx4 v22, s[12:15], s63 offen offset:2048 lds\n"
"v_mfma_f32_16x16x32_bf16 v[140:143], a[240:243], a[12:15], 0\n"
"ds_read_b128 a[24:27], v1 offset:6144\n"
"s_waitcnt lgkmcnt(10)\n"
"v_mfma_f32_16x16x32_bf16 v[144:147], a[244:247], a[0:3], 0\n"
"ds_read_b128 a[28:31], v1 offset:7168\n"
"s_add_u32 s68, s68, s81\n"
"v_mfma_f32_16x16x32_bf16 v[148:151], a[244:247], a[4:7], 0\n"
"buffer_load_dwordx4 v23, s[12:15], s63 offen offset:3072 lds\n"
"s_add_u32 m0, s61, 147456\n"
"v_mfma_f32_16x16x32_bf16 v[152:155], a[244:247], a[8:11], 0\n"
"ds_read_b64_tr_b16 a[230:231], v9 offset:51200\n"
"ds_read_b64_tr_b16 a[232:233], v10 offset:49152\n"
"v_mfma_f32_16x16x32_bf16 v[156:159], a[244:247], a[12:15], 0\n"
"ds_read_b64_tr_b16 a[234:235], v10 offset:51200\n"
"ds_read_b64_tr_b16 a[236:237], v11 offset:49152\n"
"s_waitcnt lgkmcnt(10)\n"
"v_mfma_f32_16x16x32_bf16 v[160:163], a[248:251], a[0:3], 0\n"
"buffer_load_dwordx4 v16, s[16:19], s62 offen offset:0 lds\n"
"v_mfma_f32_16x16x32_bf16 v[164:167], a[248:251], a[4:7], 0\n"
"ds_read_b64_tr_b16 a[238:239], v11 offset:51200\n"
"s_waitcnt vmcnt(9)\n"
"ds_read_b64_tr_b16 a[240:241], v12 offset:0\n"
"v_mfma_f32_16x16x32_bf16 v[168:171], a[248:251], a[8:11], 0\n"
"ds_read_b64_tr_b16 a[242:243], v12 offset:2048\n"
"s_add_u32 s63, s63, 262144\n"
"v_mfma_f32_16x16x32_bf16 v[172:175], a[248:251], a[12:15], 0\n"
"buffer_load_dwordx4 v17, s[16:19], s62 offen offset:1024 lds\n"
"ds_read_b64_tr_b16 a[244:245], v13 offset:0\n"
"v_mfma_f32_16x16x32_bf16 v[176:179], a[252:255], a[0:3], 0\n"
"ds_read_b64_tr_b16 a[246:247], v13 offset:2048\n"
"s_lshl_b32 s80, s80, 1\n"
"v_mfma_f32_16x16x32_bf16 v[180:183], a[252:255], a[4:7], 0\n"
"ds_read_b64_tr_b16 a[248:249], v14 offset:0\n"
"ds_read_b64_tr_b16 a[250:251], v14 offset:2048\n"
"v_mfma_f32_16x16x32_bf16 v[184:187], a[252:255], a[8:11], 0\n"
"buffer_load_dwordx4 v18, s[16:19], s62 offen offset:2048 lds\n"
"v_mfma_f32_16x16x32_bf16 v[188:191], a[252:255], a[12:15], 0\n"
"ds_read_b64_tr_b16 a[252:253], v15 offset:0\n"
"s_waitcnt lgkmcnt(12)\n"
"v_mfma_f32_16x16x32_bf16 v[64:67], a[224:227], a[16:19], v[64:67]\n"
"ds_read_b64_tr_b16 a[254:255], v15 offset:2048\n"
"s_mul_i32 s83, s73, 470016\n"
"v_mfma_f32_16x16x32_bf16 v[68:71], a[224:227], a[20:23], v[68:71]\n"
"buffer_load_dwordx4 v19, s[16:19], s62 offen offset:3072 lds\n"
"s_add_u32 m0, s61, 65536\n"
"v_mfma_f32_16x16x32_bf16 v[72:75], a[224:227], a[24:27], v[72:75]\n"
"ds_read_b128 a[32:35], v1 offset:8192\n"
"s_add_u32 s62, s62, 114688\n"
"v_mfma_f32_16x16x32_bf16 v[76:79], a[224:227], a[28:31], v[76:79]\n"
"ds_read_b128 a[36:39], v1 offset:9216\n"
"s_waitcnt lgkmcnt(9)\n"
"v_mfma_f32_16x16x32_bf16 v[80:83], a[228:231], a[16:19], v[80:83]\n"
"buffer_load_dwordx4 v20, s[12:15], s63 offen offset:0 lds\n"
"v_mfma_f32_16x16x32_bf16 v[84:87], a[228:231], a[20:23], v[84:87]\n"
"ds_read_b128 a[40:43], v1 offset:10240\n"
"s_sub_u32 s81, s72, s80\n"
"v_mfma_f32_16x16x32_bf16 v[88:91], a[228:231], a[24:27], v[88:91]\n"
"ds_read_b128 a[44:47], v1 offset:11264\n"
"s_mul_i32 s80, s80, 57\n"
"v_mfma_f32_16x16x32_bf16 v[92:95], a[228:231], a[28:31], v[92:95]\n"
"buffer_load_dwordx4 v21, s[12:15], s63 offen offset:1024 lds\n"
"s_waitcnt vmcnt(10)\n"
"ds_read_b64_tr_b16 a[224:225], v8 offset:16384\n"
"ds_read_b64_tr_b16 a[226:227], v8 offset:18432\n"
"v_mfma_f32_16x16x32_bf16 v[96:99], a[232:235], a[16:19], v[96:99]\n"
"s_and_b32 s81, s81, 7\n"
"s_cselect_b32 s82, s83, 0x40000000\n"
"v_mfma_f32_16x16x32_bf16 v[100:103], a[232:235], a[20:23], v[100:103]\n"
"ds_read_b64_tr_b16 a[228:229], v9 offset:16384\n"
"ds_read_b64_tr_b16 a[230:231], v9 offset:18432\n"
"v_mfma_f32_16x16x32_bf16 v[104:107], a[232:235], a[24:27], v[104:107]\n"
"buffer_load_dwordx4 v22, s[12:15], s63 offen offset:2048 lds\n"
"v_mfma_f32_16x16x32_bf16 v[108:111], a[232:235], a[28:31], v[108:111]\n"
"ds_read_b64_tr_b16 a[232:233], v10 offset:16384\n"
"ds_read_b64_tr_b16 a[234:235], v10 offset:18432\n"
"v_mfma_f32_16x16x32_bf16 v[112:115], a[236:239], a[16:19], v[112:115]\n"
"ds_read_b128 a[48:51], v1 offset:12288\n"
"s_add_u32 s80, s80, s81\n"
"v_mfma_f32_16x16x32_bf16 v[116:119], a[236:239], a[20:23], v[116:119]\n"
"buffer_load_dwordx4 v23, s[12:15], s63 offen offset:3072 lds\n"
"v_mfma_f32_16x16x32_bf16 v[120:123], a[236:239], a[24:27], v[120:123]\n"
"s_add_u32 m0, s61, 114688\n"
"s_add_u32 s63, s63, 262144\n"
"v_mfma_f32_16x16x32_bf16 v[124:127], a[236:239], a[28:31], v[124:127]\n"
"ds_read_b64_tr_b16 a[236:237], v11 offset:16384\n"
"s_waitcnt lgkmcnt(9)\n"
"v_mfma_f32_16x16x32_bf16 v[128:131], a[240:243], a[16:19], v[128:131]\n"
"ds_read_b64_tr_b16 a[238:239], v11 offset:18432\n"
"s_lshl_b32 s80, s80, 10\n"
"v_mfma_f32_16x16x32_bf16 v[132:135], a[240:243], a[20:23], v[132:135]\n"
"buffer_load_dwordx4 v16, s[16:19], s62 offen offset:0 lds\n"
"v_mfma_f32_16x16x32_bf16 v[136:139], a[240:243], a[24:27], v[136:139]\n"
"ds_read_b128 a[52:55], v1 offset:13312\n"
"s_add_u32 s28, s80, s82\n"
"v_mfma_f32_16x16x32_bf16 v[140:143], a[240:243], a[28:31], v[140:143]\n"
"ds_read_b128 a[56:59], v1 offset:14336\n"
"s_sub_u32 s80, s60, 1\n"
"v_mfma_f32_16x16x32_bf16 v[144:147], a[244:247], a[16:19], v[144:147]\n"
"buffer_load_dwordx4 v17, s[16:19], s62 offen offset:1024 lds\n"
"v_mfma_f32_16x16x32_bf16 v[148:151], a[244:247], a[20:23], v[148:151]\n"
"ds_read_b128 a[60:63], v1 offset:15360\n"
"s_and_b32 s80, s80, 3\n"
"v_mfma_f32_16x16x32_bf16 v[152:155], a[244:247], a[24:27], v[152:155]\n"
"ds_read_b128 a[64:67], v1 offset:16384\n"
"s_lshl_b32 s80, s80, 1\n"
"v_mfma_f32_16x16x32_bf16 v[156:159], a[244:247], a[28:31], v[156:159]\n"
"buffer_load_dwordx4 v18, s[16:19], s62 offen offset:2048 lds\n"
"s_waitcnt vmcnt(11)\n"
"ds_read_b64_tr_b16 a[240:241], v8 offset:32768\n"
"ds_read_b64_tr_b16 a[242:243], v8 offset:34816\n"
"v_mfma_f32_16x16x32_bf16 v[160:163], a[248:251], a[16:19], v[160:163]\n"
"s_sub_u32 s81, s72, s80\n"
"s_mul_i32 s80, s80, 57\n"
"v_mfma_f32_16x16x32_bf16 v[164:167], a[248:251], a[20:23], v[164:167]\n"
"ds_read_b64_tr_b16 a[244:245], v9 offset:32768\n"
"ds_read_b64_tr_b16 a[246:247], v9 offset:34816\n"
"v_mfma_f32_16x16x32_bf16 v[168:171], a[248:251], a[24:27], v[168:171]\n"
"buffer_load_dwordx4 v19, s[16:19], s62 offen offset:3072 lds\n"
"v_mfma_f32_16x16x32_bf16 v[172:175], a[248:251], a[28:31], v[172:175]\n"
"ds_read_b64_tr_b16 a[248:249], v10 offset:32768\n"
"ds_read_b64_tr_b16 a[250:251], v10 offset:34816\n"
"v_mfma_f32_16x16x32_bf16 v[176:179], a[252:255], a[16:19], v[176:179]\n"
"s_add_u32 m0, s61, 131072\n"
"s_add_u32 s62, s62, 114688\n"
"v_mfma_f32_16x16x32_bf16 v[180:183], a[252:255], a[20:23], v[180:183]\n"
"ds_read_b128 a[68:71], v1 offset:17408\n"
"s_mul_i32 s74, s72, 58368\n"
"v_mfma_f32_16x16x32_bf16 v[184:187], a[252:255], a[24:27], v[184:187]\n"
"buffer_load_dwordx4 v20, s[12:15], s63 offen offset:0 lds\n"
"v_mfma_f32_16x16x32_bf16 v[188:191], a[252:255], a[28:31], v[188:191]\n"
"ds_read_b64_tr_b16 a[252:253], v11 offset:32768\n"
"s_waitcnt lgkmcnt(9)\n"
"v_mfma_f32_16x16x32_bf16 v[64:67], a[224:227], a[32:35], v[64:67]\n"
"ds_read_b64_tr_b16 a[254:255], v11 offset:34816\n"
"s_lshl_b32 s76, s60, 5\n"
"v_mfma_f32_16x16x32_bf16 v[68:71], a[224:227], a[36:39], v[68:71]\n"
"ds_read_b128 a[72:75], v1 offset:18432\n"
"s_mul_i32 s64, s72, 200704\n"
"v_mfma_f32_16x16x32_bf16 v[72:75], a[224:227], a[40:43], v[72:75]\n"
"buffer_load_dwordx4 v21, s[12:15], s63 offen offset:1024 lds\n"
"v_mfma_f32_16x16x32_bf16 v[76:79], a[224:227], a[44:47], v[76:79]\n"
"ds_read_b128 a[76:79], v1 offset:19456\n"
"s_add_u32 s77, s64, 200704\n"
"v_mfma_f32_16x16x32_bf16 v[80:83], a[228:231], a[32:35], v[80:83]\n"
"ds_read_b128 a[80:83], v1 offset:20480\n"
"s_mul_i32 s65, s72, 458752\n"
"v_mfma_f32_16x16x32_bf16 v[84:87], a[228:231], a[36:39], v[84:87]\n"
"buffer_load_dwordx4 v22, s[12:15], s63 offen offset:2048 lds\n"
"v_mfma_f32_16x16x32_bf16 v[88:91], a[228:231], a[40:43], v[88:91]\n"
"s_waitcnt vmcnt(11)\n"
"ds_read_b64_tr_b16 a[224:225], v12 offset:16384\n"
"v_mfma_f32_16x16x32_bf16 v[92:95], a[228:231], a[44:47], v[92:95]\n"
"ds_read_b64_tr_b16 a[226:227], v12 offset:18432\n"
"ds_read_b64_tr_b16 a[228:229], v13 offset:16384\n"
"v_mfma_f32_16x16x32_bf16 v[96:99], a[232:235], a[32:35], v[96:99]\n"
"buffer_load_dwordx4 v23, s[12:15], s63 offen offset:3072 lds\n"
"v_mfma_f32_16x16x32_bf16 v[100:103], a[232:235], a[36:39], v[100:103]\n"
"ds_read_b64_tr_b16 a[230:231], v13 offset:18432\n"
"s_add_u32 m0, s61, 81920\n"
"v_mfma_f32_16x16x32_bf16 v[104:107], a[232:235], a[40:43], v[104:107]\n"
"s_add_u32 s63, s63, 262144\n"
"ds_read_b128 a[84:87], v1 offset:21504\n"
"v_mfma_f32_16x16x32_bf16 v[108:111], a[232:235], a[44:47], v[108:111]\n"
"ds_read_b64_tr_b16 a[232:233], v14 offset:16384\n"
"ds_read_b64_tr_b16 a[234:235], v14 offset:18432\n"
"v_mfma_f32_16x16x32_bf16 v[112:115], a[236:239], a[32:35], v[112:115]\n"
"buffer_load_dwordx4 v16, s[16:19], s62 offen offset:0 lds\n"
"v_mfma_f32_16x16x32_bf16 v[116:119], a[236:239], a[36:39], v[116:119]\n"
"ds_read_b128 a[88:91], v1 offset:22528\n"
"s_mul_i32 s53, s72, 1048576\n"
"v_mfma_f32_16x16x32_bf16 v[120:123], a[236:239], a[40:43], v[120:123]\n"
"ds_read_b128 a[92:95], v1 offset:23552\n"
"v_lshlrev_b32 v29, 7, v7\n"
"v_mfma_f32_16x16x32_bf16 v[124:127], a[236:239], a[44:47], v[124:127]\n"
"ds_read_b64_tr_b16 a[236:237], v15 offset:16384\n"
"s_waitcnt lgkmcnt(10)\n"
"v_mfma_f32_16x16x32_bf16 v[128:131], a[240:243], a[32:35], v[128:131]\n"
"buffer_load_dwordx4 v17, s[16:19], s62 offen offset:1024 lds\n"
"v_mfma_f32_16x16x32_bf16 v[132:135], a[240:243], a[36:39], v[132:135]\n"
"ds_read_b64_tr_b16 a[238:239], v15 offset:18432\n"
"v_mov_b32 v28, 0x40000000\n"
"v_mfma_f32_16x16x32_bf16 v[136:139], a[240:243], a[40:43], v[136:139]\n"
"ds_read_b128 a[96:99], v1 offset:24576\n"
"v_cmp_gt_u32 vcc, 13, v7\n"
"v_mfma_f32_16x16x32_bf16 v[140:143], a[240:243], a[44:47], v[140:143]\n"
"ds_read_b128 a[100:103], v1 offset:25600\n"
"s_add_u32 s53, s53, 786432\n"
"v_mfma_f32_16x16x32_bf16 v[144:147], a[244:247], a[32:35], v[144:147]\n"
"buffer_load_dwordx4 v18, s[16:19], s62 offen offset:2048 lds\n"
"v_mfma_f32_16x16x32_bf16 v[148:151], a[244:247], a[36:39], v[148:151]\n"
"ds_read_b128 a[104:107], v1 offset:26624\n"
"v_cndmask_b32 v34, v28, v29, vcc\n"
"v_mfma_f32_16x16x32_bf16 v[152:155], a[244:247], a[40:43], v[152:155]\n"
"ds_read_b128 a[108:111], v1 offset:27648\n"
"v_cmp_gt_u32 vcc, 16, v7\n"
"v_mfma_f32_16x16x32_bf16 v[156:159], a[244:247], a[44:47], v[156:159]\n"
"buffer_load_dwordx4 v19, s[16:19], s62 offen offset:3072 lds\n"
"s_waitcnt vmcnt(12)\n"
"ds_read_b64_tr_b16 a[240:241], v8 offset:0\n"
"ds_read_b64_tr_b16 a[242:243], v8 offset:2048\n"
"v_mfma_f32_16x16x32_bf16 v[160:163], a[248:251], a[32:35], v[160:163]\n"
"s_add_u32 m0, s61, 98304\n"
"s_add_u32 s62, s62, 114688\n"
"v_mfma_f32_16x16x32_bf16 v[164:167], a[248:251], a[36:39], v[164:167]\n"
"ds_read_b64_tr_b16 a[244:245], v9 offset:0\n"
"ds_read_b64_tr_b16 a[246:247], v9 offset:2048\n"
"v_mfma_f32_16x16x32_bf16 v[168:171], a[248:251], a[40:43], v[168:171]\n"
"ds_read_b128 a[112:115], v1 offset:28672\n"
"v_cndmask_b32 v35, v28, v29, vcc\n"
"v_mfma_f32_16x16x32_bf16 v[172:175], a[248:251], a[44:47], v[172:175]\n"
"buffer_load_dwordx4 v20, s[12:15], s63 offen offset:0 lds\n"
"v_mfma_f32_16x16x32_bf16 v[176:179], a[252:255], a[32:35], v[176:179]\n"
"ds_read_b64_tr_b16 a[248:249], v10 offset:0\n"
"ds_read_b64_tr_b16 a[250:251], v10 offset:2048\n"
"v_mfma_f32_16x16x32_bf16 v[180:183], a[252:255], a[36:39], v[180:183]\n"
"ds_read_b128 a[116:119], v1 offset:29696\n"
"s_lshl_b32 s52, s73, 11\n"
"v_mfma_f32_16x16x32_bf16 v[184:187], a[252:255], a[40:43], v[184:187]\n"
"ds_read_b128 a[120:123], v1 offset:30720\n"
"v_cmp_gt_u32 vcc, 16, v7\n"
"v_mfma_f32_16x16x32_bf16 v[188:191], a[252:255], a[44:47], v[188:191]\n"
"ds_read_b64_tr_b16 a[252:253], v11 offset:0\n"
"s_waitcnt lgkmcnt(11)\n"
"v_mfma_f32_16x16x32_bf16 v[64:67], a[224:227], a[48:51], v[64:67]\n"
"buffer_load_dwordx4 v21, s[12:15], s63 offen offset:1024 lds\n"
"v_mfma_f32_16x16x32_bf16 v[68:71], a[224:227], a[52:55], v[68:71]\n"
"ds_read_b64_tr_b16 a[254:255], v11 offset:2048\n"
"s_add_u32 s52, s52, 30081024\n"
"v_mfma_f32_16x16x32_bf16 v[72:75], a[224:227], a[56:59], v[72:75]\n"
"ds_read_b128 a[124:127], v1 offset:31744\n"
"v_mov_b32 v47, 1024\n"
"v_mfma_f32_16x16x32_bf16 v[76:79], a[224:227], a[60:63], v[76:79]\n"
"ds_read_b128 a[128:131], v1 offset:32768\n"
"v_lshrrev_b32 v40, 3, v7\n"
"v_mfma_f32_16x16x32_bf16 v[80:83], a[228:231], a[48:51], v[80:83]\n"
"buffer_load_dwordx4 v22, s[12:15], s63 offen offset:2048 lds\n"
"v_mfma_f32_16x16x32_bf16 v[84:87], a[228:231], a[52:55], v[84:87]\n"
"ds_read_b128 a[132:135], v1 offset:33792\n"
"v_and_b32 v41, 7, v7\n"
"v_mfma_f32_16x16x32_bf16 v[88:91], a[228:231], a[56:59], v[88:91]\n"
"ds_read_b128 a[136:139], v1 offset:34816\n"
"v_and_b32 v42, 6, v40\n"
"v_mfma_f32_16x16x32_bf16 v[92:95], a[228:231], a[60:63], v[92:95]\n"
"buffer_load_dwordx4 v23, s[12:15], s63 offen offset:3072 lds\n"
"s_waitcnt vmcnt(12)\n"
"ds_read_b64_tr_b16 a[224:225], v8 offset:49152\n"
"ds_read_b64_tr_b16 a[226:227], v8 offset:51200\n"
"v_mfma_f32_16x16x32_bf16 v[96:99], a[232:235], a[48:51], v[96:99]\n"
"s_add_u32 m0, s61, 147456\n"
"s_add_u32 s63, s63, 262144\n"
"v_mfma_f32_16x16x32_bf16 v[100:103], a[232:235], a[52:55], v[100:103]\n"
"ds_read_b64_tr_b16 a[228:229], v9 offset:49152\n"
"ds_read_b64_tr_b16 a[230:231], v9 offset:51200\n"
"v_mfma_f32_16x16x32_bf16 v[104:107], a[232:235], a[56:59], v[104:107]\n"
"ds_read_b128 a[140:143], v1 offset:35840\n"
"v_xor_b32 v41, v41, v42\n"
"v_mfma_f32_16x16x32_bf16 v[108:111], a[232:235], a[60:63], v[108:111]\n"
"buffer_load_dwordx4 v16, s[16:19], s62 offen offset:0 lds\n"
"v_mfma_f32_16x16x32_bf16 v[112:115], a[236:239], a[48:51], v[112:115]\n"
"ds_read_b64_tr_b16 a[232:233], v10 offset:49152\n"
"ds_read_b64_tr_b16 a[234:235], v10 offset:51200\n"
"v_mfma_f32_16x16x32_bf16 v[116:119], a[236:239], a[52:55], v[116:119]\n"
"ds_read_b128 a[144:147], v1 offset:36864\n"
"v_lshlrev_b32 v41, 4, v41\n"
"v_mfma_f32_16x16x32_bf16 v[120:123], a[236:239], a[56:59], v[120:123]\n"
"ds_read_b128 a[148:151], v1 offset:37888\n"
"v_mul_u32_u24 v42, 896, v40\n"
"v_mfma_f32_16x16x32_bf16 v[124:127], a[236:239], a[60:63], v[124:127]\n"
"ds_read_b64_tr_b16 a[236:237], v11 offset:49152\n"
"s_waitcnt lgkmcnt(11)\n"
"v_mfma_f32_16x16x32_bf16 v[128:131], a[240:243], a[48:51], v[128:131]\n"
"buffer_load_dwordx4 v17, s[16:19], s62 offen offset:1024 lds\n"
"v_mfma_f32_16x16x32_bf16 v[132:135], a[240:243], a[52:55], v[132:135]\n"
"ds_read_b64_tr_b16 a[238:239], v11 offset:51200\n"
"v_lshrrev_b32 v43, 5, v7\n"
"v_mfma_f32_16x16x32_bf16 v[136:139], a[240:243], a[56:59], v[136:139]\n"
"ds_read_b128 a[152:155], v1 offset:38912\n"
"v_and_b32 v45, 31, v7\n"
"v_mfma_f32_16x16x32_bf16 v[140:143], a[240:243], a[60:63], v[140:143]\n"
"ds_read_b128 a[156:159], v1 offset:39936\n"
"s_add_u32 s66, s53, s61\n"
"v_mfma_f32_16x16x32_bf16 v[144:147], a[244:247], a[48:51], v[144:147]\n"
"buffer_load_dwordx4 v18, s[16:19], s62 offen offset:2048 lds\n"
"v_mfma_f32_16x16x32_bf16 v[148:151], a[244:247], a[52:55], v[148:151]\n"
"s_and_b32 s81, s81, 7\n"
"s_cselect_b32 s82, s83, 0x40000000\n"
"v_mfma_f32_16x16x32_bf16 v[152:155], a[244:247], a[56:59], v[152:155]\n"
"s_add_u32 s80, s80, s81\n"
"s_lshl_b32 s80, s80, 10\n"
"v_mfma_f32_16x16x32_bf16 v[156:159], a[244:247], a[60:63], v[156:159]\n"
"buffer_load_dwordx4 v19, s[16:19], s62 offen offset:3072 lds\n"
"s_waitcnt vmcnt(12)\n"
"ds_read_b64_tr_b16 a[240:241], v12 offset:0\n"
"ds_read_b64_tr_b16 a[242:243], v12 offset:2048\n"
"v_mfma_f32_16x16x32_bf16 v[160:163], a[248:251], a[48:51], v[160:163]\n"
"s_add_u32 m0, s61, 65536\n"
"s_add_u32 s62, s62, 114688\n"
"v_mfma_f32_16x16x32_bf16 v[164:167], a[248:251], a[52:55], v[164:167]\n"
"ds_read_b64_tr_b16 a[244:245], v13 offset:0\n"
"ds_read_b64_tr_b16 a[246:247], v13 offset:2048\n"
"v_mfma_f32_16x16x32_bf16 v[168:171], a[248:251], a[56:59], v[168:171]\n"
"s_add_u32 s29, s80, s82\n"
"s_sub_u32 s80, s60, 2\n"
"v_mfma_f32_16x16x32_bf16 v[172:175], a[248:251], a[60:63], v[172:175]\n"
"buffer_load_dwordx4 v20, s[12:15], s63 offen offset:0 lds\n"
"v_mfma_f32_16x16x32_bf16 v[176:179], a[252:255], a[48:51], v[176:179]\n"
"ds_read_b64_tr_b16 a[248:249], v14 offset:0\n"
"ds_read_b64_tr_b16 a[250:251], v14 offset:2048\n"
"v_mfma_f32_16x16x32_bf16 v[180:183], a[252:255], a[52:55], v[180:183]\n"
"s_and_b32 s80, s80, 3\n"
"s_lshl_b32 s80, s80, 1\n"
"v_mfma_f32_16x16x32_bf16 v[184:187], a[252:255], a[56:59], v[184:187]\n"
"s_sub_u32 s81, s72, s80\n"
"s_mul_i32 s80, s80, 57\n"
"v_mfma_f32_16x16x32_bf16 v[188:191], a[252:255], a[60:63], v[188:191]\n"
"ds_read_b64_tr_b16 a[252:253], v15 offset:0\n"
"s_waitcnt lgkmcnt(7)\n"
"v_mfma_f32_16x16x32_bf16 v[64:67], a[224:227], a[64:67], v[64:67]\n"
"buffer_load_dwordx4 v21, s[12:15], s63 offen offset:1024 lds\n"
"v_mfma_f32_16x16x32_bf16 v[68:71], a[224:227], a[68:71], v[68:71]\n"
"ds_read_b64_tr_b16 a[254:255], v15 offset:2048\n"
"s_mul_i32 s85, s86, 3584\n"
"v_mfma_f32_16x16x32_bf16 v[72:75], a[224:227], a[72:75], v[72:75]\n"
"s_and_b32 s81, s81, 7\n"
"s_cselect_b32 s82, s83, 0x40000000\n"
"v_mfma_f32_16x16x32_bf16 v[76:79], a[224:227], a[76:79], v[76:79]\n"
"s_add_u32 s80, s80, s81\n"
"s_lshl_b32 s80, s80, 10\n"
"v_mfma_f32_16x16x32_bf16 v[80:83], a[228:231], a[64:67], v[80:83]\n"
"buffer_load_dwordx4 v22, s[12:15], s63 offen offset:2048 lds\n"
"v_mfma_f32_16x16x32_bf16 v[84:87], a[228:231], a[68:71], v[84:87]\n"
"ds_read_b128 a[160:163], v1 offset:40960\n"
"s_add_u32 s30, s80, s82\n"
"v_mfma_f32_16x16x32_bf16 v[88:91], a[228:231], a[72:75], v[88:91]\n"
"ds_read_b128 a[164:167], v1 offset:41984\n"
"s_sub_u32 s80, s60, 3\n"
"v_mfma_f32_16x16x32_bf16 v[92:95], a[228:231], a[76:79], v[92:95]\n"
"buffer_load_dwordx4 v23, s[12:15], s63 offen offset:3072 lds\n"
"s_waitcnt vmcnt(12)\n"
"ds_read_b64_tr_b16 a[224:225], v8 offset:16384\n"
"ds_read_b64_tr_b16 a[226:227], v8 offset:18432\n"
"v_mfma_f32_16x16x32_bf16 v[96:99], a[232:235], a[64:67], v[96:99]\n"
"s_add_u32 m0, s61, 114688\n"
"s_add_u32 s63, s63, 262144\n"
"v_mfma_f32_16x16x32_bf16 v[100:103], a[232:235], a[68:71], v[100:103]\n"
"ds_read_b64_tr_b16 a[228:229], v9 offset:16384\n"
"ds_read_b64_tr_b16 a[230:231], v9 offset:18432\n"
"v_mfma_f32_16x16x32_bf16 v[104:107], a[232:235], a[72:75], v[104:107]\n"
"ds_read_b128 a[168:171], v1 offset:43008\n"
"s_and_b32 s80, s80, 3\n"
"v_mfma_f32_16x16x32_bf16 v[108:111], a[232:235], a[76:79], v[108:111]\n"
"buffer_load_dwordx4 v16, s[16:19], s62 offen offset:0 lds\n"
"v_mfma_f32_16x16x32_bf16 v[112:115], a[236:239], a[64:67], v[112:115]\n"
"ds_read_b64_tr_b16 a[232:233], v10 offset:16384\n"
"ds_read_b64_tr_b16 a[234:235], v10 offset:18432\n"
"v_mfma_f32_16x16x32_bf16 v[116:119], a[236:239], a[68:71], v[116:119]\n"
"ds_read_b128 a[172:175], v1 offset:44032\n"
"s_lshl_b32 s80, s80, 1\n"
"v_mfma_f32_16x16x32_bf16 v[120:123], a[236:239], a[72:75], v[120:123]\n"
"s_sub_u32 s81, s72, s80\n"
"s_mul_i32 s80, s80, 57\n"
"v_mfma_f32_16x16x32_bf16 v[124:127], a[236:239], a[76:79], v[124:127]\n"
"ds_read_b64_tr_b16 a[236:237], v11 offset:16384\n"
"s_waitcnt lgkmcnt(10)\n"
"v_mfma_f32_16x16x32_bf16 v[128:131], a[240:243], a[64:67], v[128:131]\n"
"buffer_load_dwordx4 v17, s[16:19], s62 offen offset:1024 lds\n"
"v_mfma_f32_16x16x32_bf16 v[132:135], a[240:243], a[68:71], v[132:135]\n"
"ds_read_b64_tr_b16 a[238:239], v11 offset:18432\n"
"s_lshl_b32 s78, s60, 6\n"
"v_mfma_f32_16x16x32_bf16 v[136:139], a[240:243], a[72:75], v[136:139]\n"
"s_and_b32 s81, s81, 7\n"
"s_cselect_b32 s82, s83, 0x40000000\n"
"v_mfma_f32_16x16x32_bf16 v[140:143], a[240:243], a[76:79], v[140:143]\n"
"s_add_u32 s80, s80, s81\n"
"s_lshl_b32 s80, s80, 10\n"
"v_mfma_f32_16x16x32_bf16 v[144:147], a[244:247], a[64:67], v[144:147]\n"
"buffer_load_dwordx4 v18, s[16:19], s62 offen offset:2048 lds\n"
"v_mfma_f32_16x16x32_bf16 v[148:151], a[244:247], a[68:71], v[148:151]\n"
"s_add_u32 s31, s80, s82\n"
"s_sub_u32 s80, s60, 0\n"
"v_mfma_f32_16x16x32_bf16 v[152:155], a[244:247], a[72:75], v[152:155]\n"
"s_and_b32 s80, s80, 3\n"
"s_lshl_b32 s80, s80, 1\n"
"v_mfma_f32_16x16x32_bf16 v[156:159], a[244:247], a[76:79], v[156:159]\n"
"buffer_load_dwordx4 v19, s[16:19], s62 offen offset:3072 lds\n"
"s_waitcnt vmcnt(12)\n"
"ds_read_b64_tr_b16 a[240:241], v8 offset:32768\n"
"ds_read_b64_tr_b16 a[242:243], v8 offset:34816\n"
"v_mfma_f32_16x16x32_bf16 v[160:163], a[248:251], a[64:67], v[160:163]\n"
"s_add_u32 m0, s61, 131072\n"
"s_add_u32 s62, s62, 114688\n"
"v_mfma_f32_16x16x32_bf16 v[164:167], a[248:251], a[68:71], v[164:167]\n"
"ds_read_b64_tr_b16 a[244:245], v9 offset:32768\n"
"ds_read_b64_tr_b16 a[246:247], v9 offset:34816\n"
"v_mfma_f32_16x16x32_bf16 v[168:171], a[248:251], a[72:75], v[168:171]\n"
"s_add_u32 s80, s80, 1\n"
"s_sub_u32 s81, s72, s80\n"
"v_mfma_f32_16x16x32_bf16 v[172:175], a[248:251], a[76:79], v[172:175]\n"
"buffer_load_dwordx4 v20, s[12:15], s63 offen offset:0 lds\n"
"v_mfma_f32_16x16x32_bf16 v[176:179], a[252:255], a[64:67], v[176:179]\n"
"ds_read_b64_tr_b16 a[248:249], v10 offset:32768\n"
"ds_read_b64_tr_b16 a[250:251], v10 offset:34816\n"
"v_mfma_f32_16x16x32_bf16 v[180:183], a[252:255], a[68:71], v[180:183]\n"
"ds_read_b128 a[176:179], v1 offset:45056\n"
"s_mul_i32 s80, s80, 57\n"
"v_mfma_f32_16x16x32_bf16 v[184:187], a[252:255], a[72:75], v[184:187]\n"
"ds_read_b128 a[180:183], v1 offset:46080\n"
"v_mfma_f32_16x16x32_bf16 v[188:191], a[252:255], a[76:79], v[188:191]\n"
"ds_read_b64_tr_b16 a[252:253], v11 offset:32768\n"
"s_waitcnt lgkmcnt(9)\n"
"v_mfma_f32_16x16x32_bf16 v[64:67], a[224:227], a[80:83], v[64:67]\n"
"buffer_load_dwordx4 v21, s[12:15], s63 offen offset:1024 lds\n"
"v_mfma_f32_16x16x32_bf16 v[68:71], a[224:227], a[84:87], v[68:71]\n"
"ds_read_b64_tr_b16 a[254:255], v11 offset:34816\n"
"v_mfma_f32_16x16x32_bf16 v[72:75], a[224:227], a[88:91], v[72:75]\n"
"s_and_b32 s81, s81, 7\n"
"s_cselect_b32 s82, s83, 0x40000000\n"
"v_mfma_f32_16x16x32_bf16 v[76:79], a[224:227], a[92:95], v[76:79]\n"
"ds_read_b128 a[184:187], v1 offset:47104\n"
"s_add_u32 s80, s80, s81\n"
"v_mfma_f32_16x16x32_bf16 v[80:83], a[228:231], a[80:83], v[80:83]\n"
"buffer_load_dwordx4 v22, s[12:15], s63 offen offset:2048 lds\n"
"v_mfma_f32_16x16x32_bf16 v[84:87], a[228:231], a[84:87], v[84:87]\n"
"ds_read_b128 a[188:191], v1 offset:48128\n"
"s_lshl_b32 s80, s80, 10\n"
"v_mfma_f32_16x16x32_bf16 v[88:91], a[228:231], a[88:91], v[88:91]\n"
"s_add_u32 s57, s80, s82\n"
"s_sub_u32 s80, s60, 1\n"
"v_mfma_f32_16x16x32_bf16 v[92:95], a[228:231], a[92:95], v[92:95]\n"
"buffer_load_dwordx4 v23, s[12:15], s63 offen offset:3072 lds\n"
"s_waitcnt vmcnt(12)\n"
"ds_read_b64_tr_b16 a[224:225], v12 offset:16384\n"
"ds_read_b64_tr_b16 a[226:227], v12 offset:18432\n"
"v_mfma_f32_16x16x32_bf16 v[96:99], a[232:235], a[80:83], v[96:99]\n"
"s_add_u32 m0, s61, 81920\n"
"s_add_u32 s63, s63, 262144\n"
"v_mfma_f32_16x16x32_bf16 v[100:103], a[232:235], a[84:87], v[100:103]\n"
"ds_read_b64_tr_b16 a[228:229], v13 offset:16384\n"
"ds_read_b64_tr_b16 a[230:231], v13 offset:18432\n"
"v_mfma_f32_16x16x32_bf16 v[104:107], a[232:235], a[88:91], v[104:107]\n"
"s_and_b32 s80, s80, 3\n"
"s_lshl_b32 s80, s80, 1\n"
"v_mfma_f32_16x16x32_bf16 v[108:111], a[232:235], a[92:95], v[108:111]\n"
"buffer_load_dwordx4 v16, s[16:19], s62 offen offset:0 lds\n"
"v_mfma_f32_16x16x32_bf16 v[112:115], a[236:239], a[80:83], v[112:115]\n"
"ds_read_b64_tr_b16 a[232:233], v14 offset:16384\n"
"ds_read_b64_tr_b16 a[234:235], v14 offset:18432\n"
"v_mfma_f32_16x16x32_bf16 v[116:119], a[236:239], a[84:87], v[116:119]\n"
"s_add_u32 s80, s80, 1\n"
"s_sub_u32 s81, s72, s80\n"
"v_mfma_f32_16x16x32_bf16 v[120:123], a[236:239], a[88:91], v[120:123]\n"
"s_and_b32 s81, s81, 7\n"
"s_cselect_b32 s82, s83, 0x40000000\n"
"v_mfma_f32_16x16x32_bf16 v[124:127], a[236:239], a[92:95], v[124:127]\n"
"ds_read_b64_tr_b16 a[236:237], v15 offset:16384\n"
"s_waitcnt lgkmcnt(7)\n"
"v_mfma_f32_16x16x32_bf16 v[128:131], a[240:243], a[80:83], v[128:131]\n"
"buffer_load_dwordx4 v17, s[16:19], s62 offen offset:1024 lds\n"
"v_mfma_f32_16x16x32_bf16 v[132:135], a[240:243], a[84:87], v[132:135]\n"
"ds_read_b64_tr_b16 a[238:239], v15 offset:18432\n"
"s_mul_i32 s80, s80, 57\n"
"v_mfma_f32_16x16x32_bf16 v[136:139], a[240:243], a[88:91], v[136:139]\n"
"s_add_u32 s80, s80, s81\n"
"s_lshl_b32 s80, s80, 10\n"
"v_mfma_f32_16x16x32_bf16 v[140:143], a[240:243], a[92:95], v[140:143]\n"
"s_add_u32 s58, s80, s82\n"
"s_sub_u32 s80, s60, 2\n"
"v_mfma_f32_16x16x32_bf16 v[144:147], a[244:247], a[80:83], v[144:147]\n"
"buffer_load_dwordx4 v18, s[16:19], s62 offen offset:2048 lds\n"
"v_mfma_f32_16x16x32_bf16 v[148:151], a[244:247], a[84:87], v[148:151]\n"
"ds_read_b128 a[192:195], v1 offset:49152\n"
"s_and_b32 s80, s80, 3\n"
"v_mfma_f32_16x16x32_bf16 v[152:155], a[244:247], a[88:91], v[152:155]\n"
"ds_read_b128 a[196:199], v1 offset:50176\n"
"s_lshl_b32 s80, s80, 1\n"
"v_mfma_f32_16x16x32_bf16 v[156:159], a[244:247], a[92:95], v[156:159]\n"
"buffer_load_dwordx4 v19, s[16:19], s62 offen offset:3072 lds\n"
"s_waitcnt vmcnt(12)\n"
"ds_read_b64_tr_b16 a[240:241], v8 offset:0\n"
"ds_read_b64_tr_b16 a[242:243], v8 offset:2048\n"
"v_mfma_f32_16x16x32_bf16 v[160:163], a[248:251], a[80:83], v[160:163]\n"
"s_add_u32 m0, s61, 98304\n"
"s_add_u32 s62, s62, 114688\n"
"v_mfma_f32_16x16x32_bf16 v[164:167], a[248:251], a[84:87], v[164:167]\n"
"ds_read_b64_tr_b16 a[244:245], v9 offset:0\n"
"ds_read_b64_tr_b16 a[246:247], v9 offset:2048\n"
"v_mfma_f32_16x16x32_bf16 v[168:171], a[248:251], a[88:91], v[168:171]\n"
"ds_read_b128 a[200:203], v1 offset:51200\n"
"s_add_u32 s80, s80, 1\n"
"v_mfma_f32_16x16x32_bf16 v[172:175], a[248:251], a[92:95], v[172:175]\n"
"buffer_load_dwordx4 v20, s[12:15], s63 offen offset:0 lds\n"
"v_mfma_f32_16x16x32_bf16 v[176:179], a[252:255], a[80:83], v[176:179]\n"
"ds_read_b64_tr_b16 a[248:249], v10 offset:0\n"
"ds_read_b64_tr_b16 a[250:251], v10 offset:2048\n"
"v_mfma_f32_16x16x32_bf16 v[180:183], a[252:255], a[84:87], v[180:183]\n"
"ds_read_b128 a[204:207], v1 offset:52224\n"
"s_sub_u32 s81, s72, s80\n"
"v_mfma_f32_16x16x32_bf16 v[184:187], a[252:255], a[88:91], v[184:187]\n"
"s_and_b32 s81, s81, 7\n"
"s_cselect_b32 s82, s83, 0x40000000\n"
"v_mfma_f32_16x16x32_bf16 v[188:191], a[252:255], a[92:95], v[188:191]\n"
"ds_read_b64_tr_b16 a[252:253], v11 offset:0\n"
"s_waitcnt lgkmcnt(10)\n"
"v_mfma_f32_16x16x32_bf16 v[64:67], a[224:227], a[96:99], v[64:67]\n"
"buffer_load_dwordx4 v21, s[12:15], s63 offen offset:1024 lds\n"
"v_mfma_f32_16x16x32_bf16 v[68:71], a[224:227], a[100:103], v[68:71]\n"
"ds_read_b64_tr_b16 a[254:255], v11 offset:2048\n"
"s_mul_i32 s80, s80, 57\n"
"v_mfma_f32_16x16x32_bf16 v[72:75], a[224:227], a[104:107], v[72:75]\n"
"s_add_u32 s80, s80, s81\n"
"s_lshl_b32 s80, s80, 10\n"
"v_mfma_f32_16x16x32_bf16 v[76:79], a[224:227], a[108:111], v[76:79]\n"
"buffer_load_dwordx4 v22, s[12:15], s63 offen offset:2048 lds\n"
"v_mfma_f32_16x16x32_bf16 v[80:83], a[228:231], a[96:99], v[80:83]\n"
"s_add_u32 s59, s80, s82\n"
"s_sub_u32 s80, s60, 3\n"
"v_mfma_f32_16x16x32_bf16 v[84:87], a[228:231], a[100:103], v[84:87]\n"
"s_and_b32 s80, s80, 3\n"
"s_lshl_b32 s80, s80, 1\n"
"v_mfma_f32_16x16x32_bf16 v[88:91], a[228:231], a[104:107], v[88:91]\n"
"buffer_load_dwordx4 v23, s[12:15], s63 offen offset:3072 lds\n"
"v_mfma_f32_16x16x32_bf16 v[92:95], a[228:231], a[108:111], v[92:95]\n"
"s_waitcnt vmcnt(12)\n"
"ds_read_b64_tr_b16 a[224:225], v8 offset:49152\n"
"ds_read_b64_tr_b16 a[226:227], v8 offset:51200\n"
"v_mfma_f32_16x16x32_bf16 v[96:99], a[232:235], a[96:99], v[96:99]\n"
"ds_read_b64_tr_b16 a[228:229], v9 offset:49152\n"
"ds_read_b64_tr_b16 a[230:231], v9 offset:51200\n"
"v_mfma_f32_16x16x32_bf16 v[100:103], a[232:235], a[100:103], v[100:103]\n"
"s_add_u32 m0, s61, 147456\n"
"s_add_u32 s63, s63, 262144\n"
"v_mfma_f32_16x16x32_bf16 v[104:107], a[232:235], a[104:107], v[104:107]\n"
"buffer_load_dwordx4 v16, s[16:19], s62 offen offset:0 lds\n"
"v_mfma_f32_16x16x32_bf16 v[108:111], a[232:235], a[108:111], v[108:111]\n"
"ds_read_b64_tr_b16 a[232:233], v10 offset:49152\n"
"ds_read_b64_tr_b16 a[234:235], v10 offset:51200\n"
"v_mfma_f32_16x16x32_bf16 v[112:115], a[236:239], a[96:99], v[112:115]\n"
"s_add_u32 s80, s80, 1\n"
"s_sub_u32 s81, s72, s80\n"
"v_mfma_f32_16x16x32_bf16 v[116:119], a[236:239], a[100:103], v[116:119]\n"
"s_and_b32 s81, s81, 7\n"
"s_cselect_b32 s82, s83, 0x40000000\n"
"v_mfma_f32_16x16x32_bf16 v[120:123], a[236:239], a[104:107], v[120:123]\n"
"buffer_load_dwordx4 v17, s[16:19], s62 offen offset:1024 lds\n"
"v_mfma_f32_16x16x32_bf16 v[124:127], a[236:239], a[108:111], v[124:127]\n"
"ds_read_b64_tr_b16 a[236:237], v11 offset:49152\n"
"s_waitcnt lgkmcnt(7)\n"
"v_mfma_f32_16x16x32_bf16 v[128:131], a[240:243], a[96:99], v[128:131]\n"
"ds_read_b64_tr_b16 a[238:239], v11 offset:51200\n"
"s_mul_i32 s80, s80, 57\n"
"v_mfma_f32_16x16x32_bf16 v[132:135], a[240:243], a[100:103], v[132:135]\n"
"s_add_u32 s80, s80, s81\n"
"s_lshl_b32 s80, s80, 10\n"
"v_mfma_f32_16x16x32_bf16 v[136:139], a[240:243], a[104:107], v[136:139]\n"
"buffer_load_dwordx4 v18, s[16:19], s62 offen offset:2048 lds\n"
"v_mfma_f32_16x16x32_bf16 v[140:143], a[240:243], a[108:111], v[140:143]\n"
"s_add_u32 s33, s80, s82\n"
"s_lshr_b32 s80, s72, 1\n"
"v_mfma_f32_16x16x32_bf16 v[144:147], a[244:247], a[96:99], v[144:147]\n"
"s_sub_u32 s80, s60, s80\n"
"ds_read_b128 a[208:211], v1 offset:53248\n"
"v_mfma_f32_16x16x32_bf16 v[148:151], a[244:247], a[100:103], v[148:151]\n"
"buffer_load_dwordx4 v19, s[16:19], s62 offen offset:3072 lds\n"
"v_mfma_f32_16x16x32_bf16 v[152:155], a[244:247], a[104:107], v[152:155]\n"
"s_waitcnt vmcnt(12)\n"
"ds_read_b64_tr_b16 a[240:241], v12 offset:0\n"
"v_mfma_f32_16x16x32_bf16 v[156:159], a[244:247], a[108:111], v[156:159]\n"
"ds_read_b64_tr_b16 a[242:243], v12 offset:2048\n"
"ds_read_b64_tr_b16 a[244:245], v13 offset:0\n"
"v_mfma_f32_16x16x32_bf16 v[160:163], a[248:251], a[96:99], v[160:163]\n"
"s_add_u32 m0, s61, 65536\n"
"ds_read_b64_tr_b16 a[246:247], v13 offset:2048\n"
"v_mfma_f32_16x16x32_bf16 v[164:167], a[248:251], a[100:103], v[164:167]\n"
"buffer_load_dwordx4 v20, s[12:15], s63 offen offset:0 lds\n"
"v_mfma_f32_16x16x32_bf16 v[168:171], a[248:251], a[104:107], v[168:171]\n"
"s_add_u32 s62, s62, 114688\n"
"s_and_b32 s34, s80, 3\n"
"v_mfma_f32_16x16x32_bf16 v[172:175], a[248:251], a[108:111], v[172:175]\n"
"ds_read_b64_tr_b16 a[248:249], v14 offset:0\n"
"ds_read_b64_tr_b16 a[250:251], v14 offset:2048\n"
"v_mfma_f32_16x16x32_bf16 v[176:179], a[252:255], a[96:99], v[176:179]\n"
"ds_read_b128 a[212:215], v1 offset:54272\n"
"s_lshl_b32 s81, s34, 13\n"
"v_mfma_f32_16x16x32_bf16 v[180:183], a[252:255], a[100:103], v[180:183]\n"
"buffer_load_dwordx4 v21, s[12:15], s63 offen offset:1024 lds\n"
"v_mfma_f32_16x16x32_bf16 v[184:187], a[252:255], a[104:107], v[184:187]\n"
"ds_read_b128 a[216:219], v1 offset:55296\n"
"s_add_u32 s74, s74, s81\n"
"v_mfma_f32_16x16x32_bf16 v[188:191], a[252:255], a[108:111], v[188:191]\n"
"ds_read_b64_tr_b16 a[252:253], v15 offset:0\n"
"s_waitcnt lgkmcnt(9)\n"
"v_mfma_f32_16x16x32_bf16 v[64:67], a[224:227], a[112:115], v[64:67]\n"
"ds_read_b64_tr_b16 a[254:255], v15 offset:2048\n"
"s_mul_i32 s81, s73, 470016\n"
"v_mfma_f32_16x16x32_bf16 v[68:71], a[224:227], a[116:119], v[68:71]\n"
"buffer_load_dwordx4 v22, s[12:15], s63 offen offset:2048 lds\n"
"v_mfma_f32_16x16x32_bf16 v[72:75], a[224:227], a[120:123], v[72:75]\n"
"ds_read_b128 a[220:223], v1 offset:56320\n"
"s_add_u32 s74, s74, s81\n"
"v_mfma_f32_16x16x32_bf16 v[76:79], a[224:227], a[124:127], v[76:79]\n"
"s_and_b32 s81, s72, 1\n"
"s_mul_i32 s81, s81, 28672\n"
"v_mfma_f32_16x16x32_bf16 v[80:83], a[228:231], a[112:115], v[80:83]\n"
"buffer_load_dwordx4 v23, s[12:15], s63 offen offset:3072 lds\n"
"v_mfma_f32_16x16x32_bf16 v[84:87], a[228:231], a[116:119], v[84:87]\n"
"s_add_u32 m0, s61, 114688\n"
"s_add_u32 s63, s63, 262144\n"
"v_mfma_f32_16x16x32_bf16 v[88:91], a[228:231], a[120:123], v[88:91]\n"
"s_waitcnt vmcnt(12)\n"
"ds_read_b64_tr_b16 a[224:225], v8 offset:16384\n"
"ds_read_b64_tr_b16 a[226:227], v8 offset:18432\n"
"v_mfma_f32_16x16x32_bf16 v[92:95], a[228:231], a[124:127], v[92:95]\n"
"ds_read_b64_tr_b16 a[228:229], v9 offset:16384\n"
"ds_read_b64_tr_b16 a[230:231], v9 offset:18432\n"
"v_mfma_f32_16x16x32_bf16 v[96:99], a[232:235], a[112:115], v[96:99]\n"
"buffer_load_dwordx4 v16, s[16:19], s62 offen offset:0 lds\n"
"v_mfma_f32_16x16x32_bf16 v[100:103], a[232:235], a[116:119], v[100:103]\n"
"s_add_u32 s76, s76, s81\n"
"s_lshl_b32 s81, s34, 7\n"
"v_mfma_f32_16x16x32_bf16 v[104:107], a[232:235], a[120:123], v[104:107]\n"
"s_mul_i32 s82, s86, 1568\n"
"s_add_u32 s82, s82, s64\n"
"v_mfma_f32_16x16x32_bf16 v[108:111], a[232:235], a[124:127], v[108:111]\n"
"ds_read_b64_tr_b16 a[232:233], v10 offset:16384\n"
"ds_read_b64_tr_b16 a[234:235], v10 offset:18432\n"
"v_mfma_f32_16x16x32_bf16 v[112:115], a[236:239], a[112:115], v[112:115]\n"
"buffer_load_dwordx4 v17, s[16:19], s62 offen offset:1024 lds\n"
"v_mfma_f32_16x16x32_bf16 v[116:119], a[236:239], a[116:119], v[116:119]\n"
"s_mul_i32 s80, s60, 28672\n"
"s_add_u32 s76, s76, s81\n"
"v_mfma_f32_16x16x32_bf16 v[120:123], a[236:239], a[120:123], v[120:123]\n"
"s_add_u32 s64, s64, s80\n"
"s_add_u32 s65, s65, s80\n"
"v_mfma_f32_16x16x32_bf16 v[124:127], a[236:239], a[124:127], v[124:127]\n"
"buffer_load_dwordx4 v18, s[16:19], s62 offen offset:2048 lds\n"
"s_waitcnt lgkmcnt(6)\n"
"v_mfma_f32_16x16x32_bf16 v[128:131], a[240:243], a[112:115], v[128:131]\n"
"ds_read_b64_tr_b16 a[236:237], v11 offset:16384\n"
"ds_read_b64_tr_b16 a[238:239], v11 offset:18432\n"
"v_mfma_f32_16x16x32_bf16 v[132:135], a[240:243], a[116:119], v[132:135]\n"
"s_mul_i32 s81, s73, 57344\n"
"s_lshl_b32 s80, s60, 7\n"
"v_mfma_f32_16x16x32_bf16 v[136:139], a[240:243], a[120:123], v[136:139]\n"
"buffer_load_dwordx4 v19, s[16:19], s62 offen offset:3072 lds\n"
"v_mfma_f32_16x16x32_bf16 v[140:143], a[240:243], a[124:127], v[140:143]\n"
"s_add_u32 m0, s61, 131072\n"
"s_add_u32 s62, s62, 114688\n"
"v_mfma_f32_16x16x32_bf16 v[144:147], a[244:247], a[112:115], v[144:147]\n"
"s_add_u32 s67, s53, s80\n"
"s_add_u32 s76, s76, s81\n"
"v_mfma_f32_16x16x32_bf16 v[148:151], a[244:247], a[116:119], v[148:151]\n"
"s_waitcnt vmcnt(12)\n"
"ds_read_b64_tr_b16 a[240:241], v8 offset:32768\n"
"ds_read_b64_tr_b16 a[242:243], v8 offset:34816\n"
"v_mfma_f32_16x16x32_bf16 v[152:155], a[244:247], a[120:123], v[152:155]\n"
"buffer_load_dwordx4 v20, s[12:15], s63 offen offset:0 lds\n"
"v_mfma_f32_16x16x32_bf16 v[156:159], a[244:247], a[124:127], v[156:159]\n"
"ds_read_b64_tr_b16 a[244:245], v9 offset:32768\n"
"ds_read_b64_tr_b16 a[246:247], v9 offset:34816\n"
"v_mfma_f32_16x16x32_bf16 v[160:163], a[248:251], a[112:115], v[160:163]\n"
"s_and_b32 s80, s72, 1\n"
"s_mul_i32 s83, s86, 2048\n"
"v_mfma_f32_16x16x32_bf16 v[164:167], a[248:251], a[116:119], v[164:167]\n"
"s_mul_i32 s80, s80, 7\n"
"s_lshl_b32 s81, s60, 1\n"
"v_mfma_f32_16x16x32_bf16 v[168:171], a[248:251], a[120:123], v[168:171]\n"
"buffer_load_dwordx4 v21, s[12:15], s63 offen offset:1024 lds\n"
"v_mfma_f32_16x16x32_bf16 v[172:175], a[248:251], a[124:127], v[172:175]\n"
"ds_read_b64_tr_b16 a[248:249], v10 offset:32768\n"
"ds_read_b64_tr_b16 a[250:251], v10 offset:34816\n"
"v_mfma_f32_16x16x32_bf16 v[176:179], a[252:255], a[112:115], v[176:179]\n"
"s_add_u32 s83, s83, s53\n"
"s_add_u32 s80, s80, s81\n"
"v_mfma_f32_16x16x32_bf16 v[180:183], a[252:255], a[116:119], v[180:183]\n"
"s_add_u32 s69, s57, 0\n"
"s_lshl_b32 s81, s60, 6\n"
"v_mfma_f32_16x16x32_bf16 v[184:187], a[252:255], a[120:123], v[184:187]\n"
"buffer_load_dwordx4 v22, s[12:15], s63 offen offset:2048 lds\n"
"v_mfma_f32_16x16x32_bf16 v[188:191], a[252:255], a[124:127], v[188:191]\n"
"ds_read_b64_tr_b16 a[252:253], v11 offset:32768\n"
"s_waitcnt lgkmcnt(5)\n"
"v_mfma_f32_16x16x32_bf16 v[64:67], a[224:227], a[128:131], v[64:67]\n"
"ds_read_b64_tr_b16 a[254:255], v11 offset:34816\n"
"s_add_u32 s75, s74, 4096\n"
"v_mfma_f32_16x16x32_bf16 v[68:71], a[224:227], a[132:135], v[68:71]\n"
"v_mfma_f32_16x16x32_bf16 v[72:75], a[224:227], a[136:139], v[72:75]\n"
"buffer_load_dwordx4 v23, s[12:15], s63 offen offset:3072 lds\n"
"v_mfma_f32_16x16x32_bf16 v[76:79], a[224:227], a[140:143], v[76:79]\n"
"s_add_u32 m0, s61, 81920\n"
"s_add_u32 s63, s63, 262144\n"
"v_mfma_f32_16x16x32_bf16 v[80:83], a[228:231], a[128:131], v[80:83]\n"
"s_waitcnt vmcnt(12)\n"
"ds_read_b64_tr_b16 a[224:225], v12 offset:16384\n"
"v_mfma_f32_16x16x32_bf16 v[84:87], a[228:231], a[132:135], v[84:87]\n"
"ds_read_b64_tr_b16 a[226:227], v12 offset:18432\n"
"v_mfma_f32_16x16x32_bf16 v[88:91], a[228:231], a[136:139], v[88:91]\n"
"buffer_load_dwordx4 v16, s[16:19], s62 offen offset:0 lds\n"
"v_mfma_f32_16x16x32_bf16 v[92:95], a[228:231], a[140:143], v[92:95]\n"
"ds_read_b64_tr_b16 a[228:229], v13 offset:16384\n"
"ds_read_b64_tr_b16 a[230:231], v13 offset:18432\n"
"v_mfma_f32_16x16x32_bf16 v[96:99], a[232:235], a[128:131], v[96:99]\n"
"v_mfma_f32_16x16x32_bf16 v[100:103], a[232:235], a[132:135], v[100:103]\n"
"v_mfma_f32_16x16x32_bf16 v[104:107], a[232:235], a[136:139], v[104:107]\n"
"buffer_load_dwordx4 v17, s[16:19], s62 offen offset:1024 lds\n"
"v_mfma_f32_16x16x32_bf16 v[108:111], a[232:235], a[140:143], v[108:111]\n"
"ds_read_b64_tr_b16 a[232:233], v14 offset:16384\n"
"ds_read_b64_tr_b16 a[234:235], v14 offset:18432\n"
"v_mfma_f32_16x16x32_bf16 v[112:115], a[236:239], a[128:131], v[112:115]\n"
"v_mfma_f32_16x16x32_bf16 v[116:119], a[236:239], a[132:135], v[116:119]\n"
"v_mfma_f32_16x16x32_bf16 v[120:123], a[236:239], a[136:139], v[120:123]\n"
"buffer_load_dwordx4 v18, s[16:19], s62 offen offset:2048 lds\n"
"v_mfma_f32_16x16x32_bf16 v[124:127], a[236:239], a[140:143], v[124:127]\n"
"ds_read_b64_tr_b16 a[236:237], v15 offset:16384\n"
"ds_read_b64_tr_b16 a[238:239], v15 offset:18432\n"
"v_mfma_f32_16x16x32_bf16 v[128:131], a[240:243], a[128:131], v[128:131]\n"
"v_mfma_f32_16x16x32_bf16 v[132:135], a[240:243], a[132:135], v[132:135]\n"
"buffer_load_dwordx4 v19, s[16:19], s62 offen offset:3072 lds\n"
"v_mfma_f32_16x16x32_bf16 v[136:139], a[240:243], a[136:139], v[136:139]\n"
"s_add_u32 m0, s61, 98304\n"
"s_add_u32 s62, s62, 114688\n"
"v_mfma_f32_16x16x32_bf16 v[140:143], a[240:243], a[140:143], v[140:143]\n"
"s_waitcnt lgkmcnt(4)\n"
"v_mfma_f32_16x16x32_bf16 v[144:147], a[244:247], a[128:131], v[144:147]\n"
"s_waitcnt vmcnt(12)\n"
"ds_read_b64_tr_b16 a[240:241], v8 offset:0\n"
"v_mfma_f32_16x16x32_bf16 v[148:151], a[244:247], a[132:135], v[148:151]\n"
"buffer_load_dwordx4 v20, s[12:15], s63 offen offset:0 lds\n"
"v_mfma_f32_16x16x32_bf16 v[152:155], a[244:247], a[136:139], v[152:155]\n"
"ds_read_b64_tr_b16 a[242:243], v8 offset:2048\n"
"v_mfma_f32_16x16x32_bf16 v[156:159], a[244:247], a[140:143], v[156:159]\n"
"ds_read_b64_tr_b16 a[244:245], v9 offset:0\n"
"ds_read_b64_tr_b16 a[246:247], v9 offset:2048\n"
"v_mfma_f32_16x16x32_bf16 v[160:163], a[248:251], a[128:131], v[160:163]\n"
"v_mfma_f32_16x16x32_bf16 v[164:167], a[248:251], a[132:135], v[164:167]\n"
"buffer_load_dwordx4 v21, s[12:15], s63 offen offset:1024 lds\n"
"v_mfma_f32_16x16x32_bf16 v[168:171], a[248:251], a[136:139], v[168:171]\n"
"v_mfma_f32_16x16x32_bf16 v[172:175], a[248:251], a[140:143], v[172:175]\n"
"ds_read_b64_tr_b16 a[248:249], v10 offset:0\n"
"ds_read_b64_tr_b16 a[250:251], v10 offset:2048\n"
"v_mfma_f32_16x16x32_bf16 v[176:179], a[252:255], a[128:131], v[176:179]\n"
"v_mfma_f32_16x16x32_bf16 v[180:183], a[252:255], a[132:135], v[180:183]\n"
"buffer_load_dwordx4 v22, s[12:15], s63 offen offset:2048 lds\n"
"v_mfma_f32_16x16x32_bf16 v[184:187], a[252:255], a[136:139], v[184:187]\n"
"v_mfma_f32_16x16x32_bf16 v[188:191], a[252:255], a[140:143], v[188:191]\n"
"ds_read_b64_tr_b16 a[252:253], v11 offset:0\n"
"ds_read_b64_tr_b16 a[254:255], v11 offset:2048\n"
"v_mfma_f32_16x16x32_bf16 v[64:67], a[224:227], a[144:147], v[64:67]\n"
"v_mfma_f32_16x16x32_bf16 v[68:71], a[224:227], a[148:151], v[68:71]\n"
"buffer_load_dwordx4 v23, s[12:15], s63 offen offset:3072 lds\n"
"v_mfma_f32_16x16x32_bf16 v[72:75], a[224:227], a[152:155], v[72:75]\n"
"s_add_u32 m0, s61, 147456\n"
"s_add_u32 s63, s63, 262144\n"
"v_mfma_f32_16x16x32_bf16 v[76:79], a[224:227], a[156:159], v[76:79]\n"
"s_waitcnt vmcnt(12)\n"
"ds_read_b64_tr_b16 a[224:225], v8 offset:49152\n"
"v_mfma_f32_16x16x32_bf16 v[80:83], a[228:231], a[144:147], v[80:83]\n"
"ds_read_b64_tr_b16 a[226:227], v8 offset:51200\n"
"v_mfma_f32_16x16x32_bf16 v[84:87], a[228:231], a[148:151], v[84:87]\n"
"v_mfma_f32_16x16x32_bf16 v[88:91], a[228:231], a[152:155], v[88:91]\n"
"v_mfma_f32_16x16x32_bf16 v[92:95], a[228:231], a[156:159], v[92:95]\n"
"ds_read_b64_tr_b16 a[228:229], v9 offset:49152\n"
"s_waitcnt lgkmcnt(5)\n"
"v_mfma_f32_16x16x32_bf16 v[96:99], a[232:235], a[144:147], v[96:99]\n"
"buffer_load_dwordx4 v16, s[16:19], s62 offen offset:0 lds\n"
"v_mfma_f32_16x16x32_bf16 v[100:103], a[232:235], a[148:151], v[100:103]\n"
"ds_read_b64_tr_b16 a[230:231], v9 offset:51200\n"
"v_mfma_f32_16x16x32_bf16 v[104:107], a[232:235], a[152:155], v[104:107]\n"
"v_mfma_f32_16x16x32_bf16 v[108:111], a[232:235], a[156:159], v[108:111]\n"
"ds_read_b64_tr_b16 a[232:233], v10 offset:49152\n"
"ds_read_b64_tr_b16 a[234:235], v10 offset:51200\n"
"v_mfma_f32_16x16x32_bf16 v[112:115], a[236:239], a[144:147], v[112:115]\n"
"buffer_load_dwordx4 v17, s[16:19], s62 offen offset:1024 lds\n"
"v_mfma_f32_16x16x32_bf16 v[116:119], a[236:239], a[148:151], v[116:119]\n"
"v_mfma_f32_16x16x32_bf16 v[120:123], a[236:239], a[152:155], v[120:123]\n"
"v_mfma_f32_16x16x32_bf16 v[124:127], a[236:239], a[156:159], v[124:127]\n"
"buffer_load_dwordx4 v18, s[16:19], s62 offen offset:2048 lds\n"
"v_mfma_f32_16x16x32_bf16 v[128:131], a[240:243], a[144:147], v[128:131]\n"
"ds_read_b64_tr_b16 a[236:237], v11 offset:49152\n"
"ds_read_b64_tr_b16 a[238:239], v11 offset:51200\n"
"v_mfma_f32_16x16x32_bf16 v[132:135], a[240:243], a[148:151], v[132:135]\n"
"v_mfma_f32_16x16x32_bf16 v[136:139], a[240:243], a[152:155], v[136:139]\n"
"v_mfma_f32_16x16x32_bf16 v[140:143], a[240:243], a[156:159], v[140:143]\n"
"buffer_load_dwordx4 v19, s[16:19], s62 offen offset:3072 lds\n"
"v_mfma_f32_16x16x32_bf16 v[144:147], a[244:247], a[144:147], v[144:147]\n"
"s_add_u32 m0, s61, 65536\n"
"s_add_u32 s62, s62, 114688\n"
"v_mfma_f32_16x16x32_bf16 v[148:151], a[244:247], a[148:151], v[148:151]\n"
"s_waitcnt vmcnt(12)\n"
"ds_read_b64_tr_b16 a[240:241], v12 offset:0\n"
"v_mfma_f32_16x16x32_bf16 v[152:155], a[244:247], a[152:155], v[152:155]\n"
"ds_read_b64_tr_b16 a[242:243], v12 offset:2048\n"
"v_mfma_f32_16x16x32_bf16 v[156:159], a[244:247], a[156:159], v[156:159]\n"
"s_waitcnt lgkmcnt(4)\n"
"buffer_load_dwordx4 v20, s[12:15], s63 offen offset:0 lds\n"
"v_mfma_f32_16x16x32_bf16 v[160:163], a[248:251], a[144:147], v[160:163]\n"
"ds_read_b64_tr_b16 a[244:245], v13 offset:0\n"
"ds_read_b64_tr_b16 a[246:247], v13 offset:2048\n"
"v_mfma_f32_16x16x32_bf16 v[164:167], a[248:251], a[148:151], v[164:167]\n"
"v_mfma_f32_16x16x32_bf16 v[168:171], a[248:251], a[152:155], v[168:171]\n"
"buffer_load_dwordx4 v21, s[12:15], s63 offen offset:1024 lds\n"
"v_mfma_f32_16x16x32_bf16 v[172:175], a[248:251], a[156:159], v[172:175]\n"
"ds_read_b64_tr_b16 a[248:249], v14 offset:0\n"
"ds_read_b64_tr_b16 a[250:251], v14 offset:2048\n"
"v_mfma_f32_16x16x32_bf16 v[176:179], a[252:255], a[144:147], v[176:179]\n"
"v_mfma_f32_16x16x32_bf16 v[180:183], a[252:255], a[148:151], v[180:183]\n"
"buffer_load_dwordx4 v22, s[12:15], s63 offen offset:2048 lds\n"
"v_mfma_f32_16x16x32_bf16 v[184:187], a[252:255], a[152:155], v[184:187]\n"
"v_mfma_f32_16x16x32_bf16 v[188:191], a[252:255], a[156:159], v[188:191]\n"
"ds_read_b64_tr_b16 a[252:253], v15 offset:0\n"
"ds_read_b64_tr_b16 a[254:255], v15 offset:2048\n"
"v_mfma_f32_16x16x32_bf16 v[64:67], a[224:227], a[160:163], v[64:67]\n"
"buffer_load_dwordx4 v23, s[12:15], s63 offen offset:3072 lds\n"
"v_mfma_f32_16x16x32_bf16 v[68:71], a[224:227], a[164:167], v[68:71]\n"
"s_add_u32 m0, s61, 114688\n"
"s_add_u32 s63, s63, 262144\n"
"v_mfma_f32_16x16x32_bf16 v[72:75], a[224:227], a[168:171], v[72:75]\n"
"v_mfma_f32_16x16x32_bf16 v[76:79], a[224:227], a[172:175], v[76:79]\n"
"s_waitcnt vmcnt(12)\n"
"ds_read_b64_tr_b16 a[224:225], v8 offset:16384\n"
"v_mfma_f32_16x16x32_bf16 v[80:83], a[228:231], a[160:163], v[80:83]\n"
"s_waitcnt lgkmcnt(5)\n"
"buffer_load_dwordx4 v16, s[16:19], s62 offen offset:0 lds\n"
"v_mfma_f32_16x16x32_bf16 v[84:87], a[228:231], a[164:167], v[84:87]\n"
"ds_read_b64_tr_b16 a[226:227], v8 offset:18432\n"
"v_add_u32 v16, v42, v41\n"
"v_mfma_f32_16x16x32_bf16 v[88:91], a[228:231], a[168:171], v[88:91]\n"
"v_lshlrev_b32 v42, 9, v40\n"
"v_and_b32 v40, 15, v7\n"
"v_mfma_f32_16x16x32_bf16 v[92:95], a[228:231], a[172:175], v[92:95]\n"
"ds_read_b64_tr_b16 a[228:229], v9 offset:16384\n"
"ds_read_b64_tr_b16 a[230:231], v9 offset:18432\n"
"v_mfma_f32_16x16x32_bf16 v[96:99], a[232:235], a[160:163], v[96:99]\n"
"buffer_load_dwordx4 v17, s[16:19], s62 offen offset:1024 lds\n"
"v_mfma_f32_16x16x32_bf16 v[100:103], a[232:235], a[164:167], v[100:103]\n"
"v_add_u32 v17, 6144, v16\n"
"v_mfma_f32_16x16x32_bf16 v[104:107], a[232:235], a[168:171], v[104:107]\n"
"v_mfma_f32_16x16x32_bf16 v[108:111], a[232:235], a[172:175], v[108:111]\n"
"buffer_load_dwordx4 v18, s[16:19], s62 offen offset:2048 lds\n"
"v_mfma_f32_16x16x32_bf16 v[112:115], a[236:239], a[160:163], v[112:115]\n"
"ds_read_b64_tr_b16 a[232:233], v10 offset:16384\n"
"ds_read_b64_tr_b16 a[234:235], v10 offset:18432\n"
"v_mfma_f32_16x16x32_bf16 v[116:119], a[236:239], a[164:167], v[116:119]\n"
"v_add_u32 v18, 12288, v16\n"
"v_mfma_f32_16x16x32_bf16 v[120:123], a[236:239], a[168:171], v[120:123]\n"
"buffer_load_dwordx4 v19, s[16:19], s62 offen offset:3072 lds\n"
"v_mfma_f32_16x16x32_bf16 v[124:127], a[236:239], a[172:175], v[124:127]\n"
"s_add_u32 m0, s61, 131072\n"
"ds_read_b64_tr_b16 a[236:237], v11 offset:16384\n"
"v_mfma_f32_16x16x32_bf16 v[128:131], a[240:243], a[160:163], v[128:131]\n"
"ds_read_b64_tr_b16 a[238:239], v11 offset:18432\n"
"v_add_u32 v19, 18432, v16\n"
"v_mfma_f32_16x16x32_bf16 v[132:135], a[240:243], a[164:167], v[132:135]\n"
"v_mfma_f32_16x16x32_bf16 v[136:139], a[240:243], a[168:171], v[136:139]\n"
"s_waitcnt lgkmcnt(4)\n"
"buffer_load_dwordx4 v20, s[12:15], s63 offen offset:0 lds\n"
"v_mfma_f32_16x16x32_bf16 v[140:143], a[240:243], a[172:175], v[140:143]\n"
"s_waitcnt vmcnt(13)\n"
"ds_read_b64_tr_b16 a[240:241], v8 offset:32768\n"
"v_mfma_f32_16x16x32_bf16 v[144:147], a[244:247], a[160:163], v[144:147]\n"
"ds_read_b64_tr_b16 a[242:243], v8 offset:34816\n"
"v_mfma_f32_16x16x32_bf16 v[148:151], a[244:247], a[164:167], v[148:151]\n"
"buffer_load_dwordx4 v21, s[12:15], s63 offen offset:1024 lds\n"
"v_mfma_f32_16x16x32_bf16 v[152:155], a[244:247], a[168:171], v[152:155]\n"
"v_mfma_f32_16x16x32_bf16 v[156:159], a[244:247], a[172:175], v[156:159]\n"
"ds_read_b64_tr_b16 a[244:245], v9 offset:32768\n"
"ds_read_b64_tr_b16 a[246:247], v9 offset:34816\n"
"v_mfma_f32_16x16x32_bf16 v[160:163], a[248:251], a[160:163], v[160:163]\n"
"buffer_load_dwordx4 v22, s[12:15], s63 offen offset:2048 lds\n"
"v_mfma_f32_16x16x32_bf16 v[164:167], a[248:251], a[164:167], v[164:167]\n"
"v_mfma_f32_16x16x32_bf16 v[168:171], a[248:251], a[168:171], v[168:171]\n"
"v_mfma_f32_16x16x32_bf16 v[172:175], a[248:251], a[172:175], v[172:175]\n"
"buffer_load_dwordx4 v23, s[12:15], s63 offen offset:3072 lds\n"
"v_mfma_f32_16x16x32_bf16 v[176:179], a[252:255], a[160:163], v[176:179]\n"
"s_sub_u32 s63, s63, 3407616\n"
"s_add_u32 m0, s61, 81920\n"
"v_mfma_f32_16x16x32_bf16 v[180:183], a[252:255], a[164:167], v[180:183]\n"
"ds_read_b64_tr_b16 a[248:249], v10 offset:32768\n"
"ds_read_b64_tr_b16 a[250:251], v10 offset:34816\n"
"v_mfma_f32_16x16x32_bf16 v[184:187], a[252:255], a[168:171], v[184:187]\n"
"v_mfma_f32_16x16x32_bf16 v[188:191], a[252:255], a[172:175], v[188:191]\n"
"s_waitcnt lgkmcnt(4)\n"
"buffer_load_dwordx4 v20, s[12:15], s63 offen offset:0 lds\n"
"v_mfma_f32_16x16x32_bf16 v[64:67], a[224:227], a[176:179], v[64:67]\n"
"ds_read_b64_tr_b16 a[252:253], v11 offset:32768\n"
"ds_read_b64_tr_b16 a[254:255], v11 offset:34816\n"
"v_mfma_f32_16x16x32_bf16 v[68:71], a[224:227], a[180:183], v[68:71]\n"
"v_mfma_f32_16x16x32_bf16 v[72:75], a[224:227], a[184:187], v[72:75]\n"
"buffer_load_dwordx4 v21, s[12:15], s63 offen offset:1024 lds\n"
"v_mfma_f32_16x16x32_bf16 v[76:79], a[224:227], a[188:191], v[76:79]\n"
"s_waitcnt vmcnt(14)\n"
"ds_read_b64_tr_b16 a[224:225], v12 offset:16384\n"
"v_mfma_f32_16x16x32_bf16 v[80:83], a[228:231], a[176:179], v[80:83]\n"
"ds_read_b64_tr_b16 a[226:227], v12 offset:18432\n"
"v_mfma_f32_16x16x32_bf16 v[84:87], a[228:231], a[180:183], v[84:87]\n"
"v_mfma_f32_16x16x32_bf16 v[88:91], a[228:231], a[184:187], v[88:91]\n"
"buffer_load_dwordx4 v22, s[12:15], s63 offen offset:2048 lds\n"
"v_mfma_f32_16x16x32_bf16 v[92:95], a[228:231], a[188:191], v[92:95]\n"
"ds_read_b64_tr_b16 a[228:229], v13 offset:16384\n"
"ds_read_b64_tr_b16 a[230:231], v13 offset:18432\n"
"v_mfma_f32_16x16x32_bf16 v[96:99], a[232:235], a[176:179], v[96:99]\n"
"v_mfma_f32_16x16x32_bf16 v[100:103], a[232:235], a[180:183], v[100:103]\n"
"buffer_load_dwordx4 v23, s[12:15], s63 offen offset:3072 lds\n"
"v_mfma_f32_16x16x32_bf16 v[104:107], a[232:235], a[184:187], v[104:107]\n"
"s_add_u32 s63, s63, 262144\n"
"s_add_u32 m0, s61, 98304\n"
"v_mfma_f32_16x16x32_bf16 v[108:111], a[232:235], a[188:191], v[108:111]\n"
"ds_read_b64_tr_b16 a[232:233], v14 offset:16384\n"
"ds_read_b64_tr_b16 a[234:235], v14 offset:18432\n"
"v_mfma_f32_16x16x32_bf16 v[112:115], a[236:239], a[176:179], v[112:115]\n"
"v_mfma_f32_16x16x32_bf16 v[116:119], a[236:239], a[180:183], v[116:119]\n"
"v_mfma_f32_16x16x32_bf16 v[120:123], a[236:239], a[184:187], v[120:123]\n"
"s_waitcnt lgkmcnt(4)\n"
"buffer_load_dwordx4 v20, s[12:15], s63 offen offset:0 lds\n"
"v_mfma_f32_16x16x32_bf16 v[124:127], a[236:239], a[188:191], v[124:127]\n"
"ds_read_b64_tr_b16 a[236:237], v15 offset:16384\n"
"ds_read_b64_tr_b16 a[238:239], v15 offset:18432\n"
"v_mfma_f32_16x16x32_bf16 v[128:131], a[240:243], a[176:179], v[128:131]\n"
"v_mfma_f32_16x16x32_bf16 v[132:135], a[240:243], a[180:183], v[132:135]\n"
"buffer_load_dwordx4 v21, s[12:15], s63 offen offset:1024 lds\n"
"v_mfma_f32_16x16x32_bf16 v[136:139], a[240:243], a[184:187], v[136:139]\n"
"v_mfma_f32_16x16x32_bf16 v[140:143], a[240:243], a[188:191], v[140:143]\n"
"s_waitcnt vmcnt(14)\n"
"ds_read_b64_tr_b16 a[240:241], v8 offset:0\n"
"v_mfma_f32_16x16x32_bf16 v[144:147], a[244:247], a[176:179], v[144:147]\n"
"buffer_load_dwordx4 v22, s[12:15], s63 offen offset:2048 lds\n"
"v_mfma_f32_16x16x32_bf16 v[148:151], a[244:247], a[180:183], v[148:151]\n"
"ds_read_b64_tr_b16 a[242:243], v8 offset:2048\n"
"v_mfma_f32_16x16x32_bf16 v[152:155], a[244:247], a[184:187], v[152:155]\n"
"v_mfma_f32_16x16x32_bf16 v[156:159], a[244:247], a[188:191], v[156:159]\n"
"buffer_load_dwordx4 v23, s[12:15], s63 offen offset:3072 lds\n"
"ds_read_b64_tr_b16 a[244:245], v9 offset:0\n"
"ds_read_b64_tr_b16 a[246:247], v9 offset:2048\n"
"v_mfma_f32_16x16x32_bf16 v[160:163], a[248:251], a[176:179], v[160:163]\n"
"s_add_u32 s63, s63, 262144\n"
"s_add_u32 m0, s61, 147456\n"
"v_mfma_f32_16x16x32_bf16 v[164:167], a[248:251], a[180:183], v[164:167]\n"
"v_mfma_f32_16x16x32_bf16 v[168:171], a[248:251], a[184:187], v[168:171]\n"
"s_waitcnt lgkmcnt(4)\n"
"buffer_load_dwordx4 v20, s[12:15], s63 offen offset:0 lds\n"
"v_mfma_f32_16x16x32_bf16 v[172:175], a[248:251], a[188:191], v[172:175]\n"
"ds_read_b64_tr_b16 a[248:249], v10 offset:0\n"
"ds_read_b64_tr_b16 a[250:251], v10 offset:2048\n"
"v_mfma_f32_16x16x32_bf16 v[176:179], a[252:255], a[176:179], v[176:179]\n"
"v_mfma_f32_16x16x32_bf16 v[180:183], a[252:255], a[180:183], v[180:183]\n"
"buffer_load_dwordx4 v21, s[12:15], s63 offen offset:1024 lds\n"
"v_mfma_f32_16x16x32_bf16 v[184:187], a[252:255], a[184:187], v[184:187]\n"
"v_mfma_f32_16x16x32_bf16 v[188:191], a[252:255], a[188:191], v[188:191]\n"
"ds_read_b64_tr_b16 a[252:253], v11 offset:0\n"
"ds_read_b64_tr_b16 a[254:255], v11 offset:2048\n"
"v_mfma_f32_16x16x32_bf16 v[64:67], a[224:227], a[192:195], v[64:67]\n"
"buffer_load_dwordx4 v22, s[12:15], s63 offen offset:2048 lds\n"
"v_mfma_f32_16x16x32_bf16 v[68:71], a[224:227], a[196:199], v[68:71]\n"
"v_mfma_f32_16x16x32_bf16 v[72:75], a[224:227], a[200:203], v[72:75]\n"
"v_mfma_f32_16x16x32_bf16 v[76:79], a[224:227], a[204:207], v[76:79]\n"
"buffer_load_dwordx4 v23, s[12:15], s63 offen offset:3072 lds\n"
"s_add_u32 s63, s63, 262144\n"
"s_add_u32 m0, s61, 65536\n"
"s_waitcnt vmcnt(16)\n"
"ds_read_b64_tr_b16 a[224:225], v8 offset:49152\n"
"ds_read_b64_tr_b16 a[226:227], v8 offset:51200\n"
"v_mfma_f32_16x16x32_bf16 v[80:83], a[228:231], a[192:195], v[80:83]\n"
"v_mfma_f32_16x16x32_bf16 v[84:87], a[228:231], a[196:199], v[84:87]\n"
"v_mfma_f32_16x16x32_bf16 v[88:91], a[228:231], a[200:203], v[88:91]\n"
"v_mfma_f32_16x16x32_bf16 v[92:95], a[228:231], a[204:207], v[92:95]\n"
"ds_read_b64_tr_b16 a[228:229], v9 offset:49152\n"
"ds_read_b64_tr_b16 a[230:231], v9 offset:51200\n"
"v_mfma_f32_16x16x32_bf16 v[96:99], a[232:235], a[192:195], v[96:99]\n"
"v_mfma_f32_16x16x32_bf16 v[100:103], a[232:235], a[196:199], v[100:103]\n"
"s_waitcnt lgkmcnt(4)\n"
"buffer_load_dwordx4 v20, s[12:15], s63 offen offset:0 lds\n"
"v_mfma_f32_16x16x32_bf16 v[104:107], a[232:235], a[200:203], v[104:107]\n"
"v_mfma_f32_16x16x32_bf16 v[108:111], a[232:235], a[204:207], v[108:111]\n"
"ds_read_b64_tr_b16 a[232:233], v10 offset:49152\n"
"ds_read_b64_tr_b16 a[234:235], v10 offset:51200\n"
"v_mfma_f32_16x16x32_bf16 v[112:115], a[236:239], a[192:195], v[112:115]\n"
"buffer_load_dwordx4 v21, s[12:15], s63 offen offset:1024 lds\n"
"v_mfma_f32_16x16x32_bf16 v[116:119], a[236:239], a[196:199], v[116:119]\n"
"v_mfma_f32_16x16x32_bf16 v[120:123], a[236:239], a[200:203], v[120:123]\n"
"v_mfma_f32_16x16x32_bf16 v[124:127], a[236:239], a[204:207], v[124:127]\n"
"buffer_load_dwordx4 v22, s[12:15], s63 offen offset:2048 lds\n"
"ds_read_b64_tr_b16 a[236:237], v11 offset:49152\n"
"ds_read_b64_tr_b16 a[238:239], v11 offset:51200\n"
"v_mfma_f32_16x16x32_bf16 v[128:131], a[240:243], a[192:195], v[128:131]\n"
"v_mfma_f32_16x16x32_bf16 v[132:135], a[240:243], a[196:199], v[132:135]\n"
"v_mfma_f32_16x16x32_bf16 v[136:139], a[240:243], a[200:203], v[136:139]\n"
"buffer_load_dwordx4 v23, s[12:15], s63 offen offset:3072 lds\n"
"s_add_u32 s63, s63, 262144\n"
"s_add_u32 m0, s61, 114688\n"
"v_mfma_f32_16x16x32_bf16 v[140:143], a[240:243], a[204:207], v[140:143]\n"
"s_waitcnt vmcnt(16)\n"
"ds_read_b64_tr_b16 a[240:241], v12 offset:0\n"
"ds_read_b64_tr_b16 a[242:243], v12 offset:2048\n"
"v_mfma_f32_16x16x32_bf16 v[144:147], a[244:247], a[192:195], v[144:147]\n"
"v_mfma_f32_16x16x32_bf16 v[148:151], a[244:247], a[196:199], v[148:151]\n"
"v_mfma_f32_16x16x32_bf16 v[152:155], a[244:247], a[200:203], v[152:155]\n"
"v_mfma_f32_16x16x32_bf16 v[156:159], a[244:247], a[204:207], v[156:159]\n"
"ds_read_b64_tr_b16 a[244:245], v13 offset:0\n"
"ds_read_b64_tr_b16 a[246:247], v13 offset:2048\n"
"v_mfma_f32_16x16x32_bf16 v[160:163], a[248:251], a[192:195], v[160:163]\n"
"v_mfma_f32_16x16x32_bf16 v[164:167], a[248:251], a[196:199], v[164:167]\n"
"s_waitcnt lgkmcnt(4)\n"
"buffer_load_dwordx4 v20, s[12:15], s63 offen offset:0 lds\n"
"v_mfma_f32_16x16x32_bf16 v[168:171], a[248:251], a[200:203], v[168:171]\n"
"v_mfma_f32_16x16x32_bf16 v[172:175], a[248:251], a[204:207], v[172:175]\n"
"ds_read_b64_tr_b16 a[248:249], v14 offset:0\n"
"ds_read_b64_tr_b16 a[250:251], v14 offset:2048\n"
"v_mfma_f32_16x16x32_bf16 v[176:179], a[252:255], a[192:195], v[176:179]\n"
"buffer_load_dwordx4 v21, s[12:15], s63 offen offset:1024 lds\n"
"v_mfma_f32_16x16x32_bf16 v[180:183], a[252:255], a[196:199], v[180:183]\n"
"v_mfma_f32_16x16x32_bf16 v[184:187], a[252:255], a[200:203], v[184:187]\n"
"v_mfma_f32_16x16x32_bf16 v[188:191], a[252:255], a[204:207], v[188:191]\n"
"buffer_load_dwordx4 v22, s[12:15], s63 offen offset:2048 lds\n"
"ds_read_b64_tr_b16 a[252:253], v15 offset:0\n"
"ds_read_b64_tr_b16 a[254:255], v15 offset:2048\n"
"v_mfma_f32_16x16x32_bf16 v[64:67], a[224:227], a[208:211], v[64:67]\n"
"v_mfma_f32_16x16x32_bf16 v[68:71], a[224:227], a[212:215], v[68:71]\n"
"v_mfma_f32_16x16x32_bf16 v[72:75], a[224:227], a[216:219], v[72:75]\n"
"buffer_load_dwordx4 v23, s[12:15], s63 offen offset:3072 lds\n"
"s_add_u32 s63, s63, 262144\n"
"s_add_u32 m0, s61, 131072\n"
"v_mfma_f32_16x16x32_bf16 v[76:79], a[224:227], a[220:223], v[76:79]\n"
"s_waitcnt vmcnt(16)\n"
"ds_read_b64_tr_b16 a[224:225], v8 offset:16384\n"
"v_max_f32 v64, 0, v64\n"
"v_max_f32 v65, 0, v65\n"
"v_cvt_pk_bf16_f32 v64, v64, v65\n"
"ds_read_b64_tr_b16 a[226:227], v8 offset:18432\n"
"v_mfma_f32_16x16x32_bf16 v[80:83], a[228:231], a[208:211], v[80:83]\n"
"v_max_f32 v68, 0, v68\n"
"v_max_f32 v70, 0, v70\n"
"v_mfma_f32_16x16x32_bf16 v[84:87], a[228:231], a[212:215], v[84:87]\n"
"v_max_f32 v69, 0, v69\n"
"v_max_f32 v71, 0, v71\n"
"v_cvt_pk_bf16_f32 v68, v68, v69\n"
"v_mfma_f32_16x16x32_bf16 v[88:91], a[228:231], a[216:219], v[88:91]\n"
"v_max_f32 v72, 0, v72\n"
"v_max_f32 v74, 0, v74\n"
"v_mfma_f32_16x16x32_bf16 v[92:95], a[228:231], a[220:223], v[92:95]\n"
"ds_read_b64_tr_b16 a[228:229], v9 offset:16384\n"
"ds_read_b64_tr_b16 a[230:231], v9 offset:18432\n"
"v_max_f32 v73, 0, v73\n"
"v_max_f32 v75, 0, v75\n"
"v_cvt_pk_bf16_f32 v72, v72, v73\n"
"v_mfma_f32_16x16x32_bf16 v[96:99], a[232:235], a[208:211], v[96:99]\n"
"v_max_f32 v76, 0, v76\n"
"v_max_f32 v78, 0, v78\n"
"v_max_f32 v79, 0, v79\n"
"v_mfma_f32_16x16x32_bf16 v[100:103], a[232:235], a[212:215], v[100:103]\n"
"s_waitcnt lgkmcnt(4)\n"
"buffer_load_dwordx4 v20, s[12:15], s63 offen offset:0 lds\n"
"v_max_f32 v77, 0, v77\n"
"v_cvt_pk_bf16_f32 v76, v76, v77\n"
"v_mfma_f32_16x16x32_bf16 v[104:107], a[232:235], a[216:219], v[104:107]\n"
"v_max_f32 v80, 0, v80\n"
"v_max_f32 v66, 0, v66\n"
"v_mfma_f32_16x16x32_bf16 v[108:111], a[232:235], a[220:223], v[108:111]\n"
"ds_read_b64_tr_b16 a[232:233], v10 offset:16384\n"
"ds_read_b64_tr_b16 a[234:235], v10 offset:18432\n"
"v_mfma_f32_16x16x32_bf16 v[112:115], a[236:239], a[208:211], v[112:115]\n"
"buffer_load_dwordx4 v21, s[12:15], s63 offen offset:1024 lds\n"
"v_max_f32 v81, 0, v81\n"
"v_max_f32 v67, 0, v67\n"
"v_cvt_pk_bf16_f32 v65, v66, v67\n"
"v_mfma_f32_16x16x32_bf16 v[116:119], a[236:239], a[212:215], v[116:119]\n"
"v_max_f32 v82, 0, v82\n"
"v_max_f32 v83, 0, v83\n"
"v_mfma_f32_16x16x32_bf16 v[120:123], a[236:239], a[216:219], v[120:123]\n"
"v_max_f32 v84, 0, v84\n"
"v_max_f32 v85, 0, v85\n"
"v_mfma_f32_16x16x32_bf16 v[124:127], a[236:239], a[220:223], v[124:127]\n"
"buffer_load_dwordx4 v22, s[12:15], s63 offen offset:2048 lds\n"
"ds_read_b64_tr_b16 a[236:237], v11 offset:16384\n"
"ds_read_b64_tr_b16 a[238:239], v11 offset:18432\n"
"v_mfma_f32_16x16x32_bf16 v[128:131], a[240:243], a[208:211], v[128:131]\n"
"v_max_f32 v86, 0, v86\n"
"v_cvt_pk_bf16_f32 v69, v70, v71\n"
"v_max_f32 v96, 0, v96\n"
"v_mfma_f32_16x16x32_bf16 v[132:135], a[240:243], a[212:215], v[132:135]\n"
"v_max_f32 v87, 0, v87\n"
"v_max_f32 v88, 0, v88\n"
"v_max_f32 v97, 0, v97\n"
"v_mfma_f32_16x16x32_bf16 v[136:139], a[240:243], a[216:219], v[136:139]\n"
"buffer_load_dwordx4 v23, s[12:15], s63 offen offset:3072 lds\n"
"s_add_u32 s63, s63, 262144\n"
"s_add_u32 m0, s61, 81920\n"
"v_max_f32 v89, 0, v89\n"
"v_cvt_pk_bf16_f32 v73, v74, v75\n"
"v_mfma_f32_16x16x32_bf16 v[140:143], a[240:243], a[220:223], v[140:143]\n"
"v_max_f32 v90, 0, v90\n"
"v_max_f32 v91, 0, v91\n"
"v_max_f32 v98, 0, v98\n"
"v_cvt_pk_bf16_f32 v128, v128, v129\n"
"s_waitcnt vmcnt(16)\n"
"ds_read_b64_tr_b16 a[240:241], v8 offset:32768\n"
"ds_read_b64_tr_b16 a[242:243], v8 offset:34816\n"
"v_mfma_f32_16x16x32_bf16 v[144:147], a[244:247], a[208:211], v[144:147]\n"
"v_max_f32 v92, 0, v92\n"
"v_max_f32 v93, 0, v93\n"
"v_max_f32 v99, 0, v99\n"
"v_cvt_pk_bf16_f32 v132, v132, v133\n"
"v_mfma_f32_16x16x32_bf16 v[148:151], a[244:247], a[212:215], v[148:151]\n"
"v_max_f32 v94, 0, v94\n"
"v_max_f32 v95, 0, v95\n"
"v_cvt_pk_bf16_f32 v96, v96, v97\n"
"v_cvt_pk_bf16_f32 v129, v130, v131\n"
"v_mfma_f32_16x16x32_bf16 v[152:155], a[244:247], a[216:219], v[152:155]\n"
"v_cvt_pk_bf16_f32 v77, v78, v79\n"
"v_max_f32 v112, 0, v112\n"
"v_cvt_pk_bf16_f32 v133, v134, v135\n"
"v_cvt_pk_bf16_f32 v136, v136, v137\n"
"v_mfma_f32_16x16x32_bf16 v[156:159], a[244:247], a[220:223], v[156:159]\n"
"ds_read_b64_tr_b16 a[244:245], v9 offset:32768\n"
"ds_read_b64_tr_b16 a[246:247], v9 offset:34816\n"
"v_max_f32 v113, 0, v113\n"
"v_max_f32 v114, 0, v114\n"
"v_cvt_pk_bf16_f32 v130, v144, v145\n"
"v_cvt_pk_bf16_f32 v131, v146, v147\n"
"v_cvt_pk_bf16_f32 v137, v138, v139\n"
"v_mfma_f32_16x16x32_bf16 v[160:163], a[248:251], a[208:211], v[160:163]\n"
"v_max_f32 v115, 0, v115\n"
"v_permlane32_swap_b32 v128, v130\n"
"v_mfma_f32_16x16x32_bf16 v[164:167], a[248:251], a[212:215], v[164:167]\n"
"s_waitcnt lgkmcnt(4)\n"
"buffer_load_dwordx4 v20, s[12:15], s63 offen offset:0 lds\n"
"v_cvt_pk_bf16_f32 v97, v98, v99\n"
"v_permlane32_swap_b32 v129, v131\n"
"v_mfma_f32_16x16x32_bf16 v[168:171], a[248:251], a[216:219], v[168:171]\n"
"v_max_f32 v100, 0, v100\n"
"v_cvt_pk_bf16_f32 v134, v148, v149\n"
"v_mfma_f32_16x16x32_bf16 v[172:175], a[248:251], a[220:223], v[172:175]\n"
"ds_read_b64_tr_b16 a[248:249], v10 offset:32768\n"
"ds_read_b64_tr_b16 a[250:251], v10 offset:34816\n"
"v_max_f32 v101, 0, v101\n"
"v_permlane16_swap_b32 v128, v130\n"
"v_cvt_pk_bf16_f32 v135, v150, v151\n"
"v_mfma_f32_16x16x32_bf16 v[176:179], a[252:255], a[208:211], v[176:179]\n"
"buffer_load_dwordx4 v21, s[12:15], s63 offen offset:1024 lds\n"
"v_permlane16_swap_b32 v129, v131\n"
"v_permlane32_swap_b32 v132, v134\n"
"v_cvt_pk_bf16_f32 v138, v152, v153\n"
"v_mfma_f32_16x16x32_bf16 v[180:183], a[252:255], a[212:215], v[180:183]\n"
"s_barrier\n"
"v_max_f32 v102, 0, v102\n"
"v_max_f32 v103, 0, v103\n"
"v_permlane32_swap_b32 v133, v135\n"
"v_cvt_pk_bf16_f32 v140, v140, v141\n"
"v_mfma_f32_16x16x32_bf16 v[184:187], a[252:255], a[216:219], v[184:187]\n"
"v_permlane16_swap_b32 v132, v134\n"
"v_cvt_pk_bf16_f32 v139, v154, v155\n"
"v_cvt_pk_bf16_f32 v160, v160, v161\n"
"v_mfma_f32_16x16x32_bf16 v[188:191], a[252:255], a[220:223], v[188:191]\n"
"buffer_load_dwordx4 v22, s[12:15], s63 offen offset:2048 lds\n"
"ds_read_b64_tr_b16 a[252:253], v11 offset:32768\n"
"ds_read_b64_tr_b16 a[254:255], v11 offset:34816\n"
"v_permlane16_swap_b32 v133, v135\n"
"v_cvt_pk_bf16_f32 v161, v162, v163\n"
"v_mfma_f32_16x16x32_bf16 v[192:195], a[224:227], a[0:3], 0\n"
"v_cvt_pk_bf16_f32 v100, v100, v101\n"
"v_permlane32_swap_b32 v136, v138\n"
"v_mfma_f32_16x16x32_bf16 v[196:199], a[224:227], a[4:7], 0\n"
"v_cvt_pk_bf16_f32 v141, v142, v143\n"
"v_max_f32 v104, 0, v104\n"
"v_mfma_f32_16x16x32_bf16 v[200:203], a[224:227], a[8:11], 0\n"
"buffer_load_dwordx4 v23, s[12:15], s63 offen offset:3072 lds\n"
"s_add_u32 s63, s63, 262144\n"
"s_add_u32 m0, s61, 98304\n"
"v_permlane32_swap_b32 v137, v139\n"
"v_cvt_pk_bf16_f32 v162, v176, v177\n"
"v_mfma_f32_16x16x32_bf16 v[204:207], a[224:227], a[12:15], 0\n"
"v_permlane16_swap_b32 v136, v138\n"
"v_cvt_pk_bf16_f32 v142, v156, v157\n"
"v_mfma_f32_16x16x32_bf16 v[208:211], a[228:231], a[0:3], 0\n"
"v_permlane16_swap_b32 v137, v139\n"
"s_waitcnt vmcnt(16)\n"
"ds_read_b64_tr_b16 a[224:225], v12 offset:16384\n"
"v_mfma_f32_16x16x32_bf16 v[212:215], a[228:231], a[4:7], 0\n"
"v_cvt_pk_bf16_f32 v66, v80, v81\n"
"v_max_f32 v105, 0, v105\n"
"ds_read_b64_tr_b16 a[226:227], v12 offset:18432\n"
"v_mfma_f32_16x16x32_bf16 v[216:219], a[228:231], a[8:11], 0\n"
"v_permlane32_swap_b32 v140, v142\n"
"v_mfma_f32_16x16x32_bf16 v[220:223], a[228:231], a[12:15], 0\n"
"ds_read_b64_tr_b16 a[228:229], v13 offset:16384\n"
"ds_read_b64_tr_b16 a[230:231], v13 offset:18432\n"
"v_cvt_pk_bf16_f32 v143, v158, v159\n"
"v_mfma_f32_16x16x32_bf16 v[224:227], a[232:235], a[0:3], 0\n"
"v_cvt_pk_bf16_f32 v67, v82, v83\n"
"v_permlane32_swap_b32 v141, v143\n"
"v_mfma_f32_16x16x32_bf16 v[228:231], a[232:235], a[4:7], 0\n"
"s_waitcnt lgkmcnt(4)\n"
"buffer_load_dwordx4 v20, s[12:15], s63 offen offset:0 lds\n"
"v_cvt_pk_bf16_f32 v163, v178, v179\n"
"v_mfma_f32_16x16x32_bf16 v[232:235], a[232:235], a[8:11], 0\n"
"v_cvt_pk_bf16_f32 v70, v84, v85\n"
"v_cvt_pk_bf16_f32 v71, v86, v87\n"
"v_mfma_f32_16x16x32_bf16 v[236:239], a[232:235], a[12:15], 0\n"
"ds_read_b64_tr_b16 a[232:233], v14 offset:16384\n"
"ds_read_b64_tr_b16 a[234:235], v14 offset:18432\n"
"v_cvt_pk_bf16_f32 v164, v164, v165\n"
"v_cvt_pk_bf16_f32 v165, v166, v167\n"
"v_mfma_f32_16x16x32_bf16 v[240:243], a[236:239], a[0:3], 0\n"
"buffer_load_dwordx4 v21, s[12:15], s63 offen offset:1024 lds\n"
"v_cvt_pk_bf16_f32 v74, v88, v89\n"
"v_cvt_pk_bf16_f32 v172, v172, v173\n"
"v_mfma_f32_16x16x32_bf16 v[244:247], a[236:239], a[4:7], 0\n"
"v_cvt_pk_bf16_f32 v75, v90, v91\n"
"v_cvt_pk_bf16_f32 v168, v168, v169\n"
"v_cvt_pk_bf16_f32 v169, v170, v171\n"
"v_mfma_f32_16x16x32_bf16 v[248:251], a[236:239], a[8:11], 0\n"
"v_cvt_pk_bf16_f32 v78, v92, v93\n"
"v_cvt_pk_bf16_f32 v79, v94, v95\n"
"v_mfma_f32_16x16x32_bf16 v[252:255], a[236:239], a[12:15], 0\n"
"buffer_load_dwordx4 v22, s[12:15], s63 offen offset:2048 lds\n"
"ds_read_b64_tr_b16 a[236:237], v15 offset:16384\n"
"ds_read_b64_tr_b16 a[238:239], v15 offset:18432\n"
"v_cvt_pk_bf16_f32 v98, v112, v113\n"
"v_cvt_pk_bf16_f32 v99, v114, v115\n"
"v_mfma_f32_16x16x32_bf16 v[192:195], a[240:243], a[16:19], v[192:195]\n"
"v_max_f32 v116, 0, v116\n"
"v_max_f32 v117, 0, v117\n"
"v_mfma_f32_16x16x32_bf16 v[196:199], a[240:243], a[20:23], v[196:199]\n"
"v_max_f32 v118, 0, v118\n"
"v_cvt_pk_bf16_f32 v101, v102, v103\n"
"v_mfma_f32_16x16x32_bf16 v[200:203], a[240:243], a[24:27], v[200:203]\n"
"buffer_load_dwordx4 v23, s[12:15], s63 offen offset:3072 lds\n"
"s_add_u32 s63, s63, 262144\n"
"s_add_u32 m0, s61, 147456\n"
"v_max_f32 v119, 0, v119\n"
"v_max_f32 v106, 0, v106\n"
"v_mfma_f32_16x16x32_bf16 v[204:207], a[240:243], a[28:31], v[204:207]\n"
"v_max_f32 v107, 0, v107\n"
"v_cvt_pk_bf16_f32 v104, v104, v105\n"
"v_mfma_f32_16x16x32_bf16 v[208:211], a[244:247], a[16:19], v[208:211]\n"
"v_permlane32_swap_b32 v160, v162\n"
"v_mfma_f32_16x16x32_bf16 v[212:215], a[244:247], a[20:23], v[212:215]\n"
"buffer_load_dword v59, v34, s[44:47], s82 offen\n"
"s_add_u32 s82, s80, 0\n"
"s_and_b32 s82, s82, 7\n"
"s_lshl_b32 s82, s82, 12\n"
"v_add_u32 v32, s82, v1\n"
"s_add_u32 s82, s80, 1\n"
"s_and_b32 s82, s82, 7\n"
"v_mfma_f32_16x16x32_bf16 v[216:219], a[244:247], a[24:27], v[216:219]\n"
"ds_write_b128 v32, v[64:67] offset:0\n"
"v_permlane16_swap_b32 v140, v142\n"
"v_mfma_f32_16x16x32_bf16 v[220:223], a[244:247], a[28:31], v[220:223]\n"
"s_waitcnt vmcnt(17)\n"
"ds_read_b64_tr_b16 a[240:241], v8 offset:0\n"
"v_permlane16_swap_b32 v141, v143\n"
"ds_read_b64_tr_b16 a[242:243], v8 offset:2048\n"
"v_mfma_f32_16x16x32_bf16 v[224:227], a[248:251], a[16:19], v[224:227]\n"
"buffer_load_dword v59, v35, s[36:39], s83 offen\n"
"s_mul_i32 s83, s86, 2048\n"
"v_cndmask_b32 v35, v28, v29, vcc\n"
"v_add_u32 v28, s68, v1\n"
"ds_write_b128 v28, v[128:131] offset:32768\n"
"s_add_u32 s83, s83, s53\n"
"v_mfma_f32_16x16x32_bf16 v[228:231], a[248:251], a[20:23], v[228:231]\n"
"ds_read_b64_tr_b16 a[244:245], v9 offset:0\n"
"ds_read_b64_tr_b16 a[246:247], v9 offset:2048\n"
"s_lshl_b32 s82, s82, 12\n"
"v_permlane32_swap_b32 v161, v163\n"
"v_mfma_f32_16x16x32_bf16 v[232:235], a[248:251], a[24:27], v[232:235]\n"
"v_cvt_pk_bf16_f32 v166, v180, v181\n"
"v_cvt_pk_bf16_f32 v167, v182, v183\n"
"v_mfma_f32_16x16x32_bf16 v[236:239], a[248:251], a[28:31], v[236:239]\n"
"s_waitcnt lgkmcnt(6)\n"
"buffer_load_dwordx4 v20, s[12:15], s63 offen offset:0 lds\n"
"v_mfma_f32_16x16x32_bf16 v[240:243], a[252:255], a[16:19], v[240:243]\n"
"ds_write_b128 v28, v[132:135] offset:33792\n"
"v_cvt_pk_bf16_f32 v173, v174, v175\n"
"v_mfma_f32_16x16x32_bf16 v[244:247], a[252:255], a[20:23], v[244:247]\n"
"ds_read_b64_tr_b16 a[248:249], v10 offset:0\n"
"v_max_f32 v108, 0, v108\n"
"v_max_f32 v109, 0, v109\n"
"ds_read_b64_tr_b16 a[250:251], v10 offset:2048\n"
"v_mfma_f32_16x16x32_bf16 v[248:251], a[252:255], a[24:27], v[248:251]\n"
"buffer_load_dwordx4 v21, s[12:15], s63 offen offset:1024 lds\n"
"v_add_u32 v33, s82, v1\n"
"v_mfma_f32_16x16x32_bf16 v[252:255], a[252:255], a[28:31], v[252:255]\n"
"v_cvt_pk_bf16_f32 v170, v184, v185\n"
"v_cvt_pk_bf16_f32 v171, v186, v187\n"
"v_mfma_f32_16x16x32_bf16 v[192:195], a[224:227], a[32:35], v[192:195]\n"
"ds_write_b128 v28, v[136:139] offset:34816\n"
"v_cvt_pk_bf16_f32 v102, v116, v117\n"
"v_mfma_f32_16x16x32_bf16 v[196:199], a[224:227], a[36:39], v[196:199]\n"
"buffer_load_dwordx2 v[30:31], v47, s[20:23], s52 offen sc1\n"
"v_cvt_pk_bf16_f32 v103, v118, v119\n"
"v_max_f32 v120, 0, v120\n"
"v_mfma_f32_16x16x32_bf16 v[200:203], a[224:227], a[40:43], v[200:203]\n"
"ds_write_b128 v32, v[68:71] offset:1024\n"
"v_max_f32 v121, 0, v121\n"
"v_cvt_pk_bf16_f32 v105, v106, v107\n"
"v_mfma_f32_16x16x32_bf16 v[204:207], a[224:227], a[44:47], v[204:207]\n"
"ds_read_b64_tr_b16 a[252:253], v11 offset:0\n"
"v_max_f32 v122, 0, v122\n"
"v_max_f32 v123, 0, v123\n"
"ds_read_b64_tr_b16 a[254:255], v11 offset:2048\n"
"v_mfma_f32_16x16x32_bf16 v[208:211], a[228:231], a[32:35], v[208:211]\n"
"buffer_load_dword v59, v35, s[40:43], s83 offen\n"
"v_permlane16_swap_b32 v160, v162\n"
"v_mfma_f32_16x16x32_bf16 v[212:215], a[228:231], a[36:39], v[212:215]\n"
"ds_write_b128 v32, v[72:75] offset:2048\n"
"v_cvt_pk_bf16_f32 v174, v188, v189\n"
"v_mfma_f32_16x16x32_bf16 v[216:219], a[228:231], a[40:43], v[216:219]\n"
"v_permlane16_swap_b32 v161, v163\n"
"s_waitcnt vmcnt(18)\n"
"ds_read_b64_tr_b16 a[224:225], v8 offset:49152\n"
"v_mfma_f32_16x16x32_bf16 v[220:223], a[228:231], a[44:47], v[220:223]\n"
"buffer_load_dwordx4 v22, s[12:15], s63 offen offset:2048 lds\n"
"v_permlane32_swap_b32 v164, v166\n"
"ds_read_b64_tr_b16 a[226:227], v8 offset:51200\n"
"v_mfma_f32_16x16x32_bf16 v[224:227], a[232:235], a[32:35], v[224:227]\n"
"v_permlane32_swap_b32 v165, v167\n"
"v_mfma_f32_16x16x32_bf16 v[228:231], a[232:235], a[36:39], v[228:231]\n"
"ds_write_b128 v32, v[76:79] offset:3072\n"
"v_cvt_pk_bf16_f32 v175, v190, v191\n"
"v_mfma_f32_16x16x32_bf16 v[232:235], a[232:235], a[40:43], v[232:235]\n"
"buffer_load_dwordx4 v23, s[12:15], s63 offen offset:3072 lds\n"
"v_max_f32 v110, 0, v110\n"
"v_max_f32 v111, 0, v111\n"
"v_mfma_f32_16x16x32_bf16 v[236:239], a[232:235], a[44:47], v[236:239]\n"
"ds_read_b64_tr_b16 a[228:229], v9 offset:49152\n"
"v_cvt_pk_bf16_f32 v108, v108, v109\n"
"s_add_u32 s63, s63, 262144\n"
"ds_read_b64_tr_b16 a[230:231], v9 offset:51200\n"
"v_mfma_f32_16x16x32_bf16 v[240:243], a[236:239], a[32:35], v[240:243]\n"
"v_permlane32_swap_b32 v168, v170\n"
"v_mfma_f32_16x16x32_bf16 v[244:247], a[236:239], a[36:39], v[244:247]\n"
"s_add_u32 m0, s61, 65536\n"
"ds_write_b128 v33, v[96:99] offset:0\n"
"v_permlane32_swap_b32 v169, v171\n"
"v_mfma_f32_16x16x32_bf16 v[248:251], a[236:239], a[40:43], v[248:251]\n"
"ds_read_b64_tr_b16 a[232:233], v10 offset:49152\n"
"v_cvt_pk_bf16_f32 v106, v120, v121\n"
"v_mfma_f32_16x16x32_bf16 v[252:255], a[236:239], a[44:47], v[252:255]\n"
"ds_write_b128 v28, v[140:143] offset:35840\n"
"s_waitcnt lgkmcnt(8)\n"
"v_mfma_f32_16x16x32_bf16 v[192:195], a[240:243], a[48:51], v[192:195]\n"
"buffer_load_dwordx4 v20, s[12:15], s63 offen offset:0 lds\n"
"v_cvt_pk_bf16_f32 v107, v122, v123\n"
"v_mfma_f32_16x16x32_bf16 v[196:199], a[240:243], a[52:55], v[196:199]\n"
"ds_read_b64_tr_b16 a[234:235], v10 offset:51200\n"
"v_permlane16_swap_b32 v164, v166\n"
"v_mfma_f32_16x16x32_bf16 v[200:203], a[240:243], a[56:59], v[200:203]\n"
"v_permlane16_swap_b32 v165, v167\n"
"ds_read_b64_tr_b16 a[236:237], v11 offset:49152\n"
"v_mfma_f32_16x16x32_bf16 v[204:207], a[240:243], a[60:63], v[204:207]\n"
"ds_write_b128 v33, v[100:103] offset:1024\n"
"v_max_f32 v124, 0, v124\n"
"v_max_f32 v125, 0, v125\n"
"v_mfma_f32_16x16x32_bf16 v[208:211], a[244:247], a[48:51], v[208:211]\n"
"buffer_load_dwordx4 v21, s[12:15], s63 offen offset:1024 lds\n"
"v_max_f32 v126, 0, v126\n"
"v_max_f32 v127, 0, v127\n"
"v_mfma_f32_16x16x32_bf16 v[212:215], a[244:247], a[52:55], v[212:215]\n"
"ds_read_b64_tr_b16 a[238:239], v11 offset:51200\n"
"v_permlane32_swap_b32 v172, v174\n"
"v_mfma_f32_16x16x32_bf16 v[216:219], a[244:247], a[56:59], v[216:219]\n"
"v_permlane32_swap_b32 v173, v175\n"
"s_waitcnt vmcnt(18)\n"
"ds_read_b64_tr_b16 a[240:241], v12 offset:0\n"
"v_mfma_f32_16x16x32_bf16 v[220:223], a[244:247], a[60:63], v[220:223]\n"
"ds_write_b128 v28, v[160:163] offset:36864\n"
"v_cvt_pk_bf16_f32 v109, v110, v111\n"
"v_mfma_f32_16x16x32_bf16 v[224:227], a[248:251], a[48:51], v[224:227]\n"
"buffer_load_dwordx4 v22, s[12:15], s63 offen offset:2048 lds\n"
"v_mfma_f32_16x16x32_bf16 v[228:231], a[248:251], a[52:55], v[228:231]\n"
"ds_read_b64_tr_b16 a[242:243], v12 offset:2048\n"
"v_permlane16_swap_b32 v168, v170\n"
"v_mfma_f32_16x16x32_bf16 v[232:235], a[248:251], a[56:59], v[232:235]\n"
"v_permlane16_swap_b32 v169, v171\n"
"ds_read_b64_tr_b16 a[244:245], v13 offset:0\n"
"v_mfma_f32_16x16x32_bf16 v[236:239], a[248:251], a[60:63], v[236:239]\n"
"ds_write_b128 v33, v[104:107] offset:2048\n"
"v_cvt_pk_bf16_f32 v110, v124, v125\n"
"v_mfma_f32_16x16x32_bf16 v[240:243], a[252:255], a[48:51], v[240:243]\n"
"buffer_load_dwordx4 v23, s[12:15], s63 offen offset:3072 lds\n"
"v_cvt_pk_bf16_f32 v111, v126, v127\n"
"v_mfma_f32_16x16x32_bf16 v[244:247], a[252:255], a[52:55], v[244:247]\n"
"ds_read_b64_tr_b16 a[246:247], v13 offset:2048\n"
"v_permlane16_swap_b32 v172, v174\n"
"v_mfma_f32_16x16x32_bf16 v[248:251], a[252:255], a[56:59], v[248:251]\n"
"v_permlane16_swap_b32 v173, v175\n"
"ds_read_b64_tr_b16 a[248:249], v14 offset:0\n"
"v_mfma_f32_16x16x32_bf16 v[252:255], a[252:255], a[60:63], v[252:255]\n"
"s_add_u32 m0, s61, 114688\n"
"ds_write_b128 v28, v[164:167] offset:37888\n"
"s_waitcnt lgkmcnt(8)\n"
"v_mfma_f32_16x16x32_bf16 v[192:195], a[224:227], a[64:67], v[192:195]\n"
"ds_read_b64_tr_b16 a[250:251], v14 offset:2048\n"
"s_add_u32 s63, s63, 262144\n"
"v_mfma_f32_16x16x32_bf16 v[196:199], a[224:227], a[68:71], v[196:199]\n"
"buffer_load_dwordx4 v20, s[12:15], s63 offen offset:0 lds\n"
"ds_read_b64_tr_b16 a[252:253], v15 offset:0\n"
"v_mfma_f32_16x16x32_bf16 v[200:203], a[224:227], a[72:75], v[200:203]\n"
"ds_read_b64_tr_b16 a[254:255], v15 offset:2048\n"
"s_lshl_b32 s80, s60, 3\n"
"v_mfma_f32_16x16x32_bf16 v[204:207], a[224:227], a[76:79], v[204:207]\n"
"ds_write_b128 v28, v[168:171] offset:38912\n"
"v_add_u32 v44, s80, v43\n"
"v_mfma_f32_16x16x32_bf16 v[208:211], a[228:231], a[64:67], v[208:211]\n"
"v_add_u32 v46, 0, v44\n"
"v_and_b32 v46, 15, v46\n"
"v_mfma_f32_16x16x32_bf16 v[212:215], a[228:231], a[68:71], v[212:215]\n"
"buffer_load_dwordx4 v21, s[12:15], s63 offen offset:1024 lds\n"
"ds_write_b128 v33, v[108:111] offset:3072\n"
"ds_write_b128 v28, v[172:175] offset:39936\n"
"v_mfma_f32_16x16x32_bf16 v[216:219], a[228:231], a[72:75], v[216:219]\n"
"v_lshlrev_b32 v43, 9, v43\n"
"v_xor_b32 v46, v45, v46\n"
"v_mfma_f32_16x16x32_bf16 v[220:223], a[228:231], a[76:79], v[220:223]\n"
"v_lshl_add_u32 v24, v46, 4, v43\n"
"v_add_u32 v46, 2, v44\n"
"v_mfma_f32_16x16x32_bf16 v[224:227], a[232:235], a[64:67], v[224:227]\n"
"v_and_b32 v46, 15, v46\n"
"v_xor_b32 v46, v45, v46\n"
"v_mfma_f32_16x16x32_bf16 v[228:231], a[232:235], a[68:71], v[228:231]\n"
"buffer_load_dwordx4 v22, s[12:15], s63 offen offset:2048 lds\n"
"v_mfma_f32_16x16x32_bf16 v[232:235], a[232:235], a[72:75], v[232:235]\n"
"s_waitcnt vmcnt(19)\n"
"ds_read_b64_tr_b16 a[224:225], v8 offset:16384\n"
"v_mfma_f32_16x16x32_bf16 v[236:239], a[232:235], a[76:79], v[236:239]\n"
"ds_read_b64_tr_b16 a[226:227], v8 offset:18432\n"
"ds_read_b64_tr_b16 a[228:229], v9 offset:16384\n"
"v_mfma_f32_16x16x32_bf16 v[240:243], a[236:239], a[64:67], v[240:243]\n"
"ds_read_b64_tr_b16 a[230:231], v9 offset:18432\n"
"v_lshl_add_u32 v25, v46, 4, v43\n"
"v_mfma_f32_16x16x32_bf16 v[244:247], a[236:239], a[68:71], v[244:247]\n"
"buffer_load_dwordx4 v23, s[12:15], s63 offen offset:3072 lds\n"
"v_mfma_f32_16x16x32_bf16 v[248:251], a[236:239], a[72:75], v[248:251]\n"
"ds_read_b64_tr_b16 a[232:233], v10 offset:16384\n"
"s_add_u32 s63, s63, 262144\n"
"v_mfma_f32_16x16x32_bf16 v[252:255], a[236:239], a[76:79], v[252:255]\n"
"s_waitcnt lgkmcnt(5)\n"
"s_barrier\n"
"ds_read_b64_tr_b16 a[234:235], v10 offset:18432\n"
"v_mfma_f32_16x16x32_bf16 v[192:195], a[240:243], a[80:83], v[192:195]\n"
"s_add_u32 m0, s61, 131072\n"
"ds_read_b64_tr_b16 a[236:237], v11 offset:16384\n"
"v_mfma_f32_16x16x32_bf16 v[196:199], a[240:243], a[84:87], v[196:199]\n"
"buffer_load_dwordx4 v20, s[12:15], s63 offen offset:0 lds\n"
"v_mfma_f32_16x16x32_bf16 v[200:203], a[240:243], a[88:91], v[200:203]\n"
"ds_read_b64_tr_b16 a[238:239], v11 offset:18432\n"
"v_add_u32 v46, 4, v44\n"
"v_mfma_f32_16x16x32_bf16 v[204:207], a[240:243], a[92:95], v[204:207]\n"
"s_waitcnt vmcnt(17)\n"
"ds_read_b64_tr_b16 a[240:241], v8 offset:32768\n"
"v_mfma_f32_16x16x32_bf16 v[208:211], a[244:247], a[80:83], v[208:211]\n"
"ds_read_b64_tr_b16 a[242:243], v8 offset:34816\n"
"v_and_b32 v46, 15, v46\n"
"v_mfma_f32_16x16x32_bf16 v[212:215], a[244:247], a[84:87], v[212:215]\n"
"buffer_load_dwordx4 v21, s[12:15], s63 offen offset:1024 lds\n"
"v_mfma_f32_16x16x32_bf16 v[216:219], a[244:247], a[88:91], v[216:219]\n"
"ds_read_b128 a[0:3], v1 offset:0\n"
"v_xor_b32 v46, v45, v46\n"
"v_mfma_f32_16x16x32_bf16 v[220:223], a[244:247], a[92:95], v[220:223]\n"
"ds_read_b64_tr_b16 a[244:245], v9 offset:32768\n"
"ds_read_b64_tr_b16 a[246:247], v9 offset:34816\n"
"v_mfma_f32_16x16x32_bf16 v[224:227], a[248:251], a[80:83], v[224:227]\n"
"ds_read_b128 a[4:7], v1 offset:1024\n"
"v_lshl_add_u32 v26, v46, 4, v43\n"
"v_mfma_f32_16x16x32_bf16 v[228:231], a[248:251], a[84:87], v[228:231]\n"
"buffer_load_dwordx4 v22, s[12:15], s63 offen offset:2048 lds\n"
"v_mfma_f32_16x16x32_bf16 v[232:235], a[248:251], a[88:91], v[232:235]\n"
"ds_read_b128 a[8:11], v1 offset:2048\n"
"v_add_u32 v46, 6, v44\n"
"v_mfma_f32_16x16x32_bf16 v[236:239], a[248:251], a[92:95], v[236:239]\n"
"ds_read_b64_tr_b16 a[248:249], v10 offset:32768\n"
"ds_read_b64_tr_b16 a[250:251], v10 offset:34816\n"
"v_mfma_f32_16x16x32_bf16 v[240:243], a[252:255], a[80:83], v[240:243]\n"
"ds_read_b128 a[12:15], v1 offset:3072\n"
"v_and_b32 v46, 15, v46\n"
"v_mfma_f32_16x16x32_bf16 v[244:247], a[252:255], a[84:87], v[244:247]\n"
"buffer_load_dwordx4 v23, s[12:15], s63 offen offset:3072 lds\n"
"v_mfma_f32_16x16x32_bf16 v[248:251], a[252:255], a[88:91], v[248:251]\n"
"s_add_u32 s63, s63, 262144\n"
"s_add_u32 m0, s61, 81920\n"
"v_mfma_f32_16x16x32_bf16 v[252:255], a[252:255], a[92:95], v[252:255]\n"
"ds_read_b64_tr_b16 a[252:253], v11 offset:32768\n"
"ds_read_b64_tr_b16 a[254:255], v11 offset:34816\n"
"s_waitcnt lgkmcnt(10)\n"
"v_mfma_f32_16x16x32_bf16 v[192:195], a[224:227], a[96:99], v[192:195]\n"
"ds_read_b128 a[16:19], v1 offset:4096\n"
"v_xor_b32 v46, v45, v46\n"
"v_mfma_f32_16x16x32_bf16 v[196:199], a[224:227], a[100:103], v[196:199]\n"
"buffer_load_dwordx4 v20, s[12:15], s63 offen offset:0 lds\n"
"v_mfma_f32_16x16x32_bf16 v[200:203], a[224:227], a[104:107], v[200:203]\n"
"ds_read_b128 a[20:23], v1 offset:5120\n"
"v_lshl_add_u32 v27, v46, 4, v43\n"
"v_mfma_f32_16x16x32_bf16 v[204:207], a[224:227], a[108:111], v[204:207]\n"
"s_waitcnt vmcnt(13)\n"
"ds_read_b64_tr_b16 a[224:225], v12 offset:16384\n"
"v_mfma_f32_16x16x32_bf16 v[208:211], a[228:231], a[96:99], v[208:211]\n"
"ds_read_b64_tr_b16 a[226:227], v12 offset:18432\n"
"v_readfirstlane_b32 s70, v30\n"
"v_mfma_f32_16x16x32_bf16 v[212:215], a[228:231], a[100:103], v[212:215]\n"
"buffer_load_dwordx4 v21, s[12:15], s63 offen offset:1024 lds\n"
"v_mfma_f32_16x16x32_bf16 v[216:219], a[228:231], a[104:107], v[216:219]\n"
"ds_read_b128 a[24:27], v1 offset:6144\n"
"v_readfirstlane_b32 s71, v31\n"
"v_mfma_f32_16x16x32_bf16 v[220:223], a[228:231], a[108:111], v[220:223]\n"
"ds_read_b64_tr_b16 a[228:229], v13 offset:16384\n"
"ds_read_b64_tr_b16 a[230:231], v13 offset:18432\n"
"v_mfma_f32_16x16x32_bf16 v[224:227], a[232:235], a[96:99], v[224:227]\n"
"buffer_load_dwordx4 v22, s[12:15], s63 offen offset:2048 lds\n"
"v_mfma_f32_16x16x32_bf16 v[228:231], a[232:235], a[100:103], v[228:231]\n"
"ds_read_b128 a[28:31], v1 offset:7168\n"
"v_mov_b32 v30, v3\n"
"v_mfma_f32_16x16x32_bf16 v[232:235], a[232:235], a[104:107], v[232:235]\n"
"ds_read_b128 a[32:35], v1 offset:8192\n"
"v_add_u32 v31, 65536, v30\n"
"v_mfma_f32_16x16x32_bf16 v[236:239], a[232:235], a[108:111], v[236:239]\n"
"buffer_load_dwordx4 v23, s[12:15], s63 offen offset:3072 lds\n"
"s_add_u32 s63, s63, 262144\n"
"s_add_u32 m0, s61, 98304\n"
"ds_read_b64_tr_b16 a[232:233], v14 offset:16384\n"
"ds_read_b64_tr_b16 a[234:235], v14 offset:18432\n"
"v_mfma_f32_16x16x32_bf16 v[240:243], a[236:239], a[96:99], v[240:243]\n"
"v_add_u32 v3, 65536, v31\n"
"s_add_u32 s68, s28, 0\n"
"v_mfma_f32_16x16x32_bf16 v[244:247], a[236:239], a[100:103], v[244:247]\n"
"ds_read_b128 a[36:39], v1 offset:9216\n"
"s_sub_u32 s80, 0, s61\n"
"v_mfma_f32_16x16x32_bf16 v[248:251], a[236:239], a[104:107], v[248:251]\n"
"s_waitcnt lgkmcnt(8)\n"
"buffer_load_dwordx4 v20, s[12:15], s63 offen offset:0 lds\n"
"v_mfma_f32_16x16x32_bf16 v[252:255], a[236:239], a[108:111], v[252:255]\n"
"ds_read_b64_tr_b16 a[236:237], v15 offset:16384\n"
"ds_read_b64_tr_b16 a[238:239], v15 offset:18432\n"
"v_mfma_f32_16x16x32_bf16 v[192:195], a[240:243], a[112:115], v[192:195]\n"
"ds_read_b128 a[40:43], v1 offset:10240\n"
"v_add_u32 v20, v42, v41\n"
"v_mfma_f32_16x16x32_bf16 v[196:199], a[240:243], a[116:119], v[196:199]\n"
"buffer_load_dwordx4 v21, s[12:15], s63 offen offset:1024 lds\n"
"v_mfma_f32_16x16x32_bf16 v[200:203], a[240:243], a[120:123], v[200:203]\n"
"ds_read_b128 a[44:47], v1 offset:11264\n"
"v_lshrrev_b32 v41, 4, v7\n"
"v_mfma_f32_16x16x32_bf16 v[204:207], a[240:243], a[124:127], v[204:207]\n"
"s_waitcnt vmcnt(14)\n"
"ds_read_b64_tr_b16 a[240:241], v8 offset:0\n"
"v_mfma_f32_16x16x32_bf16 v[208:211], a[244:247], a[112:115], v[208:211]\n"
"buffer_load_dwordx4 v22, s[12:15], s63 offen offset:2048 lds\n"
"v_mfma_f32_16x16x32_bf16 v[212:215], a[244:247], a[116:119], v[212:215]\n"
"ds_read_b64_tr_b16 a[242:243], v8 offset:2048\n"
"v_xor_b32 v41, v41, v40\n"
"v_mfma_f32_16x16x32_bf16 v[216:219], a[244:247], a[120:123], v[216:219]\n"
"ds_read_b128 a[48:51], v1 offset:12288\n"
"v_and_b32 v42, 12, v40\n"
"v_mfma_f32_16x16x32_bf16 v[220:223], a[244:247], a[124:127], v[220:223]\n"
"buffer_load_dwordx4 v23, s[12:15], s63 offen offset:3072 lds\n"
"s_add_u32 m0, s61, 147456\n"
"v_mfma_f32_16x16x32_bf16 v[224:227], a[248:251], a[112:115], v[224:227]\n"
"ds_read_b64_tr_b16 a[244:245], v9 offset:0\n"
"ds_read_b64_tr_b16 a[246:247], v9 offset:2048\n"
"v_mfma_f32_16x16x32_bf16 v[228:231], a[248:251], a[116:119], v[228:231]\n"
"ds_read_b128 a[52:55], v1 offset:13312\n"
"v_and_b32 v41, 3, v41\n"
"v_mfma_f32_16x16x32_bf16 v[232:235], a[248:251], a[120:123], v[232:235]\n"
"s_waitcnt lgkmcnt(7)\n"
"buffer_load_dwordx4 v16, s[44:47], s64 offen offset:0 lds\n"
"v_mfma_f32_16x16x32_bf16 v[236:239], a[248:251], a[124:127], v[236:239]\n"
"ds_read_b64_tr_b16 a[248:249], v10 offset:0\n"
"ds_read_b64_tr_b16 a[250:251], v10 offset:2048\n"
"v_mfma_f32_16x16x32_bf16 v[240:243], a[252:255], a[112:115], v[240:243]\n"
"ds_read_b128 a[56:59], v1 offset:14336\n"
"v_lshlrev_b32 v40, 9, v40\n"
"v_mfma_f32_16x16x32_bf16 v[244:247], a[252:255], a[116:119], v[244:247]\n"
"buffer_load_dwordx4 v17, s[44:47], s64 offen offset:1024 lds\n"
"v_mfma_f32_16x16x32_bf16 v[248:251], a[252:255], a[120:123], v[248:251]\n"
"ds_read_b128 a[60:63], v1 offset:15360\n"
"v_xor_b32 v43, 0, v42\n"
"v_mfma_f32_16x16x32_bf16 v[252:255], a[252:255], a[124:127], v[252:255]\n"
"ds_read_b64_tr_b16 a[252:253], v11 offset:0\n"
"ds_read_b64_tr_b16 a[254:255], v11 offset:2048\n"
"v_mfma_f32_16x16x32_bf16 v[192:195], a[224:227], a[128:131], v[192:195]\n"
"ds_read_b128 a[64:67], v1 offset:16384\n"
"v_add_u32 v40, 114688, v40\n"
"v_mfma_f32_16x16x32_bf16 v[196:199], a[224:227], a[132:135], v[196:199]\n"
"buffer_load_dwordx4 v18, s[44:47], s64 offen offset:2048 lds\n"
"v_mfma_f32_16x16x32_bf16 v[200:203], a[224:227], a[136:139], v[200:203]\n"
"ds_read_b128 a[68:71], v1 offset:17408\n"
"v_or_b32 v43, v43, v41\n"
"v_mfma_f32_16x16x32_bf16 v[204:207], a[224:227], a[140:143], v[204:207]\n"
"ds_read_b128 a[72:75], v1 offset:18432\n"
"v_add_u32 v21, 3072, v20\n"
"v_mfma_f32_16x16x32_bf16 v[208:211], a[228:231], a[128:131], v[208:211]\n"
"ds_read_b128 a[76:79], v1 offset:19456\n"
"v_add_u32 v22, 6144, v20\n"
"v_mfma_f32_16x16x32_bf16 v[212:215], a[228:231], a[132:135], v[212:215]\n"
"buffer_load_dwordx4 v19, s[44:47], s64 offen offset:3072 lds\n"
"v_mfma_f32_16x16x32_bf16 v[216:219], a[228:231], a[136:139], v[216:219]\n"
"s_waitcnt vmcnt(16)\n"
"ds_read_b64_tr_b16 a[224:225], v8 offset:49152\n"
"v_mfma_f32_16x16x32_bf16 v[220:223], a[228:231], a[140:143], v[220:223]\n"
"ds_read_b128 a[80:83], v1 offset:20480\n"
"s_add_u32 s64, s64, 114688\n"
"v_mfma_f32_16x16x32_bf16 v[224:227], a[232:235], a[128:131], v[224:227]\n"
"s_add_u32 m0, s61, 65536\n"
"ds_read_b128 a[84:87], v1 offset:21504\n"
"v_mfma_f32_16x16x32_bf16 v[228:231], a[232:235], a[132:135], v[228:231]\n"
"ds_read_b64_tr_b16 a[226:227], v8 offset:51200\n"
"ds_read_b64_tr_b16 a[228:229], v9 offset:49152\n"
"v_mfma_f32_16x16x32_bf16 v[232:235], a[232:235], a[136:139], v[232:235]\n"
"ds_read_b128 a[88:91], v1 offset:22528\n"
"v_add_u32 v23, 9216, v20\n"
"v_mfma_f32_16x16x32_bf16 v[236:239], a[232:235], a[140:143], v[236:239]\n"
"ds_read_b128 a[92:95], v1 offset:23552\n"
"s_mul_i32 s83, s72, 458752\n"
"v_mfma_f32_16x16x32_bf16 v[240:243], a[236:239], a[128:131], v[240:243]\n"
"ds_read_b128 a[96:99], v1 offset:24576\n"
"s_add_u32 s85, s85, s83\n"
"v_mfma_f32_16x16x32_bf16 v[244:247], a[236:239], a[132:135], v[244:247]\n"
"ds_read_b128 a[100:103], v1 offset:25600\n"
"v_mfma_f32_16x16x32_bf16 v[248:251], a[236:239], a[136:139], v[248:251]\n"
"ds_read_b128 a[104:107], v1 offset:26624\n"
"v_mfma_f32_16x16x32_bf16 v[252:255], a[236:239], a[140:143], v[252:255]\n"
"ds_read_b128 a[108:111], v1 offset:27648\n"
"s_waitcnt lgkmcnt(11)\n"
"v_mfma_f32_16x16x32_bf16 v[192:195], a[240:243], a[144:147], v[192:195]\n"
"ds_read_b128 a[112:115], v2 offset:32768\n"
"v_mfma_f32_16x16x32_bf16 v[196:199], a[240:243], a[148:151], v[196:199]\n"
"ds_read_b64_tr_b16 a[230:231], v9 offset:51200\n"
"ds_read_b64_tr_b16 a[232:233], v10 offset:49152\n"
"v_mfma_f32_16x16x32_bf16 v[200:203], a[240:243], a[152:155], v[200:203]\n"
"ds_read_b128 a[116:119], v2 offset:33792\n"
"v_mfma_f32_16x16x32_bf16 v[204:207], a[240:243], a[156:159], v[204:207]\n"
"ds_read_b128 a[120:123], v2 offset:36864\n"
"v_mfma_f32_16x16x32_bf16 v[208:211], a[244:247], a[144:147], v[208:211]\n"
"ds_read_b128 a[124:127], v2 offset:37888\n"
"v_mfma_f32_16x16x32_bf16 v[212:215], a[244:247], a[148:151], v[212:215]\n"
"ds_read_b128 a[128:131], v2 offset:40960\n"
"ds_read_b128 a[132:135], v2 offset:41984\n"
"ds_read_b128 a[136:139], v2 offset:45056\n"
"ds_read_b128 a[140:143], v2 offset:46080\n"
"s_cmp_ge_u32 s64, s77\n"
"s_cselect_b32 s84, 200576, 0\n"
"v_mfma_f32_16x16x32_bf16 v[216:219], a[244:247], a[152:155], v[216:219]\n"
"ds_read_b64_tr_b16 a[234:235], v10 offset:51200\n"
"s_sub_u32 s64, s64, s84\n"
"v_mfma_f32_16x16x32_bf16 v[220:223], a[244:247], a[156:159], v[220:223]\n"
"buffer_load_dwordx4 v16, s[44:47], s64 offen offset:0 lds\n"
"v_mfma_f32_16x16x32_bf16 v[224:227], a[248:251], a[144:147], v[224:227]\n"
"ds_read_b64_tr_b16 a[236:237], v11 offset:49152\n"
"ds_read_b64_tr_b16 a[238:239], v11 offset:51200\n"
"v_mfma_f32_16x16x32_bf16 v[228:231], a[248:251], a[148:151], v[228:231]\n"
"s_waitcnt vmcnt(13)\n"
"ds_read_b64_tr_b16 a[240:241], v12 offset:0\n"
"ds_read_b64_tr_b16 a[242:243], v12 offset:2048\n"
"v_mfma_f32_16x16x32_bf16 v[232:235], a[248:251], a[152:155], v[232:235]\n"
"ds_read_b64_tr_b16 a[244:245], v13 offset:0\n"
"v_lshl_add_u32 v12, v43, 4, v40\n"
"v_mfma_f32_16x16x32_bf16 v[236:239], a[248:251], a[156:159], v[236:239]\n"
"buffer_load_dwordx4 v17, s[44:47], s64 offen offset:1024 lds\n"
"ds_read_b64_tr_b16 a[246:247], v13 offset:2048\n"
"v_mfma_f32_16x16x32_bf16 v[240:243], a[252:255], a[144:147], v[240:243]\n"
"v_xor_b32 v43, 4, v42\n"
"v_or_b32 v43, v43, v41\n"
"v_mfma_f32_16x16x32_bf16 v[244:247], a[252:255], a[148:151], v[244:247]\n"
"ds_read_b64_tr_b16 a[248:249], v14 offset:0\n"
"ds_read_b64_tr_b16 a[250:251], v14 offset:2048\n"
"v_mfma_f32_16x16x32_bf16 v[248:251], a[252:255], a[152:155], v[248:251]\n"
"v_lshl_add_u32 v13, v43, 4, v40\n"
"v_xor_b32 v43, 8, v42\n"
"v_mfma_f32_16x16x32_bf16 v[252:255], a[252:255], a[156:159], v[252:255]\n"
"buffer_load_dwordx4 v18, s[44:47], s64 offen offset:2048 lds\n"
"s_waitcnt lgkmcnt(9)\n"
"s_barrier\n"
"v_mfma_f32_16x16x32_bf16 v[192:195], a[224:227], a[160:163], v[192:195]\n"
"ds_read_b64_tr_b16 a[252:253], v15 offset:0\n"
"ds_read_b64_tr_b16 a[254:255], v15 offset:2048\n"
"v_mfma_f32_16x16x32_bf16 v[196:199], a[224:227], a[164:167], v[196:199]\n"
"v_or_b32 v43, v43, v41\n"
"v_lshl_add_u32 v14, v43, 4, v40\n"
"v_mfma_f32_16x16x32_bf16 v[200:203], a[224:227], a[168:171], v[200:203]\n"
"buffer_load_dwordx4 v19, s[44:47], s64 offen offset:3072 lds\n"
"v_mfma_f32_16x16x32_bf16 v[204:207], a[224:227], a[172:175], v[204:207]\n"
"s_add_u32 m0, s61, 114688\n"
"s_add_u32 s64, s64, 114688\n"
"v_mfma_f32_16x16x32_bf16 v[208:211], a[228:231], a[160:163], v[208:211]\n"
"s_waitcnt vmcnt(12)\n"
"ds_read_b64_tr_b16 a[224:225], v8 offset:16384\n"
"v_mfma_f32_16x16x32_bf16 v[212:215], a[228:231], a[164:167], v[212:215]\n"
"ds_read_b64_tr_b16 a[226:227], v8 offset:18432\n"
"v_xor_b32 v43, 12, v42\n"
"v_mfma_f32_16x16x32_bf16 v[216:219], a[228:231], a[168:171], v[216:219]\n"
"s_waitcnt lgkmcnt(6)\n"
"buffer_load_dwordx4 v24, s[36:39], s66 offen offset:0 lds\n"
"v_mfma_f32_16x16x32_bf16 v[220:223], a[228:231], a[172:175], v[220:223]\n"
"ds_read_b64_tr_b16 a[228:229], v9 offset:16384\n"
"ds_read_b64_tr_b16 a[230:231], v9 offset:18432\n"
"v_mfma_f32_16x16x32_bf16 v[224:227], a[232:235], a[160:163], v[224:227]\n"
"s_cmp_ge_u32 s64, s77\n"
"s_cselect_b32 s84, 200576, 0\n"
"v_mfma_f32_16x16x32_bf16 v[228:231], a[232:235], a[164:167], v[228:231]\n"
"buffer_load_dwordx4 v25, s[36:39], s66 offen offset:1024 lds\n"
"v_mfma_f32_16x16x32_bf16 v[232:235], a[232:235], a[168:171], v[232:235]\n"
"s_sub_u32 s64, s64, s84\n"
"v_or_b32 v43, v43, v41\n"
"v_mfma_f32_16x16x32_bf16 v[236:239], a[232:235], a[172:175], v[236:239]\n"
"ds_read_b64_tr_b16 a[232:233], v10 offset:16384\n"
"ds_read_b64_tr_b16 a[234:235], v10 offset:18432\n"
"v_mfma_f32_16x16x32_bf16 v[240:243], a[236:239], a[160:163], v[240:243]\n"
"buffer_load_dwordx4 v26, s[36:39], s66 offen offset:2048 lds\n"
"v_mfma_f32_16x16x32_bf16 v[244:247], a[236:239], a[164:167], v[244:247]\n"
"v_lshl_add_u32 v15, v43, 4, v40\n"
"v_mfma_f32_16x16x32_bf16 v[248:251], a[236:239], a[168:171], v[248:251]\n"
"s_cmp_ge_u32 s28, 0x40000000\n"
"s_cselect_b64 vcc, -1, 0\n"
"v_mfma_f32_16x16x32_bf16 v[252:255], a[236:239], a[172:175], v[252:255]\n"
"buffer_load_dwordx4 v27, s[36:39], s66 offen offset:3072 lds\n"
"v_mfma_f32_16x16x32_bf16 v[192:195], a[240:243], a[176:179], v[192:195]\n"
"s_add_u32 m0, s61, 32768\n"
"ds_read_b64_tr_b16 a[236:237], v11 offset:16384\n"
"v_mfma_f32_16x16x32_bf16 v[196:199], a[240:243], a[180:183], v[196:199]\n"
"ds_read_b64_tr_b16 a[238:239], v11 offset:18432\n"
"s_add_u32 s66, s66, 16384\n"
"v_mfma_f32_16x16x32_bf16 v[200:203], a[240:243], a[184:187], v[200:203]\n"
"buffer_load_dwordx4 v16, s[44:47], s64 offen offset:0 lds\n"
"v_mfma_f32_16x16x32_bf16 v[204:207], a[240:243], a[188:191], v[204:207]\n"
"s_waitcnt vmcnt(13)\n"
"ds_read_b64_tr_b16 a[240:241], v8 offset:32768\n"
"v_mfma_f32_16x16x32_bf16 v[208:211], a[244:247], a[176:179], v[208:211]\n"
"ds_read_b64_tr_b16 a[242:243], v8 offset:34816\n"
"v_add_u32 v8, s80, v8\n"
"v_mfma_f32_16x16x32_bf16 v[212:215], a[244:247], a[180:183], v[212:215]\n"
"buffer_load_dwordx4 v17, s[44:47], s64 offen offset:1024 lds\n"
"v_mfma_f32_16x16x32_bf16 v[216:219], a[244:247], a[184:187], v[216:219]\n"
"s_add_u32 s70, s70, 1\n"
"s_addc_u32 s71, s71, 0\n"
"v_mfma_f32_16x16x32_bf16 v[220:223], a[244:247], a[188:191], v[220:223]\n"
"ds_read_b64_tr_b16 a[244:245], v9 offset:32768\n"
"s_waitcnt lgkmcnt(5)\n"
"v_mfma_f32_16x16x32_bf16 v[224:227], a[248:251], a[176:179], v[224:227]\n"
"buffer_load_dwordx4 v18, s[44:47], s64 offen offset:2048 lds\n"
"v_mfma_f32_16x16x32_bf16 v[228:231], a[248:251], a[180:183], v[228:231]\n"
"ds_read_b64_tr_b16 a[246:247], v9 offset:34816\n"
"v_add_u32 v9, s80, v9\n"
"v_mfma_f32_16x16x32_bf16 v[232:235], a[248:251], a[184:187], v[232:235]\n"
"s_cmp_eq_u32 s34, 3\n"
"s_cselect_b32 s83, 0x40000000, 0\n"
"v_mfma_f32_16x16x32_bf16 v[236:239], a[248:251], a[188:191], v[236:239]\n"
"buffer_load_dwordx4 v19, s[44:47], s64 offen offset:3072 lds\n"
"s_add_u32 s64, s64, 114688\n"
"s_cmp_ge_u32 s64, s77\n"
"s_cselect_b32 s84, 200576, 0\n"
"s_sub_u32 s64, s64, s84\n"
"s_add_u32 m0, s61, 49152\n"
"v_mfma_f32_16x16x32_bf16 v[240:243], a[252:255], a[176:179], v[240:243]\n"
"ds_read_b64_tr_b16 a[248:249], v10 offset:32768\n"
"ds_read_b64_tr_b16 a[250:251], v10 offset:34816\n"
"v_mfma_f32_16x16x32_bf16 v[244:247], a[252:255], a[180:183], v[244:247]\n"
"v_add_u32 v10, s80, v10\n"
"s_add_u32 s83, s83, 32768\n"
"v_mfma_f32_16x16x32_bf16 v[248:251], a[252:255], a[184:187], v[248:251]\n"
"buffer_load_dwordx4 v16, s[44:47], s64 offen offset:0 lds\n"
"v_mfma_f32_16x16x32_bf16 v[252:255], a[252:255], a[188:191], v[252:255]\n"
"ds_read_b64_tr_b16 a[252:253], v11 offset:32768\n"
"ds_read_b64_tr_b16 a[254:255], v11 offset:34816\n"
"v_mfma_f32_16x16x32_bf16 v[192:195], a[224:227], a[192:195], v[192:195]\n"
"v_add_u32 v11, s80, v11\n"
"s_lshl_b32 s80, s72, 3\n"
"v_mfma_f32_16x16x32_bf16 v[196:199], a[224:227], a[196:199], v[196:199]\n"
"buffer_load_dwordx4 v17, s[44:47], s64 offen offset:1024 lds\n"
"v_mfma_f32_16x16x32_bf16 v[200:203], a[224:227], a[200:203], v[200:203]\n"
"s_add_u32 s78, s78, s80\n"
"s_add_u32 s78, s78, s52\n"
"v_mfma_f32_16x16x32_bf16 v[204:207], a[224:227], a[204:207], v[204:207]\n"
"v_mfma_f32_16x16x32_bf16 v[208:211], a[228:231], a[192:195], v[208:211]\n"
"buffer_load_dwordx4 v18, s[44:47], s64 offen offset:2048 lds\n"
"v_mfma_f32_16x16x32_bf16 v[212:215], a[228:231], a[196:199], v[212:215]\n"
"v_mfma_f32_16x16x32_bf16 v[216:219], a[228:231], a[200:203], v[216:219]\n"
"v_mfma_f32_16x16x32_bf16 v[220:223], a[228:231], a[204:207], v[220:223]\n"
"buffer_load_dwordx4 v19, s[44:47], s64 offen offset:3072 lds\n"
"v_mfma_f32_16x16x32_bf16 v[224:227], a[232:235], a[192:195], v[224:227]\n"
"s_add_u32 m0, s61, 131072\n"
"s_add_u32 s64, s64, 114688\n"
"v_mfma_f32_16x16x32_bf16 v[228:231], a[232:235], a[196:199], v[228:231]\n"
"s_cmp_ge_u32 s64, s77\n"
"s_cselect_b32 s84, 200576, 0\n"
"v_mfma_f32_16x16x32_bf16 v[232:235], a[232:235], a[200:203], v[232:235]\n"
"s_sub_u32 s64, s64, s84\n"
"v_mfma_f32_16x16x32_bf16 v[236:239], a[232:235], a[204:207], v[236:239]\n"
"buffer_load_dwordx4 v24, s[36:39], s66 offen offset:0 lds\n"
"s_waitcnt lgkmcnt(0)\n"
"v_mfma_f32_16x16x32_bf16 v[240:243], a[236:239], a[192:195], v[240:243]\n"
"v_mfma_f32_16x16x32_bf16 v[244:247], a[236:239], a[196:199], v[244:247]\n"
"v_mfma_f32_16x16x32_bf16 v[248:251], a[236:239], a[200:203], v[248:251]\n"
"buffer_load_dwordx4 v25, s[36:39], s66 offen offset:1024 lds\n"
"v_mfma_f32_16x16x32_bf16 v[252:255], a[236:239], a[204:207], v[252:255]\n"
"v_mfma_f32_16x16x32_bf16 v[192:195], a[240:243], a[208:211], v[192:195]\n"
"v_mfma_f32_16x16x32_bf16 v[196:199], a[240:243], a[212:215], v[196:199]\n"
"buffer_load_dwordx4 v26, s[36:39], s66 offen offset:2048 lds\n"
"v_mfma_f32_16x16x32_bf16 v[200:203], a[240:243], a[216:219], v[200:203]\n"
"v_mfma_f32_16x16x32_bf16 v[204:207], a[240:243], a[220:223], v[204:207]\n"
"v_mfma_f32_16x16x32_bf16 v[208:211], a[244:247], a[208:211], v[208:211]\n"
"buffer_load_dwordx4 v27, s[36:39], s66 offen offset:3072 lds\n"
"s_add_u32 m0, s61, 81920\n"
"v_mfma_f32_16x16x32_bf16 v[212:215], a[244:247], a[212:215], v[212:215]\n"
"v_cvt_pk_bf16_f32 v192, v192, v193\n"
"v_cvt_pk_bf16_f32 v193, v194, v195\n"
"v_cvt_pk_bf16_f32 v196, v196, v197\n"
"s_waitcnt vmcnt(16)\n"
"s_barrier\n"
"ds_read_b64_tr_b16 a[224:225], v3 offset:16384\n"
"ds_read_b64_tr_b16 a[226:227], v3 offset:18432\n"
"v_cvt_pk_bf16_f32 v197, v198, v199\n"
"v_cvt_pk_bf16_f32 v200, v200, v201\n"
"v_mfma_f32_16x16x32_bf16 v[216:219], a[244:247], a[216:219], v[216:219]\n"
"ds_read_b64_tr_b16 a[228:229], v3 offset:20480\n"
"ds_read_b64_tr_b16 a[230:231], v3 offset:22528\n"
"v_cvt_pk_bf16_f32 v201, v202, v203\n"
"v_cvt_pk_bf16_f32 v204, v204, v205\n"
"v_cvt_pk_bf16_f32 v205, v206, v207\n"
"v_mfma_f32_16x16x32_bf16 v[220:223], a[244:247], a[220:223], v[220:223]\n"
"buffer_load_dwordx4 v20, s[40:43], s67 offen offset:0 lds\n"
"ds_read_b64_tr_b16 a[232:233], v3 offset:24576\n"
"ds_read_b64_tr_b16 a[234:235], v3 offset:26624\n"
"v_cvt_pk_bf16_f32 v194, v208, v209\n"
"v_cvt_pk_bf16_f32 v195, v210, v211\n"
"v_mfma_f32_16x16x32_bf16 v[224:227], a[248:251], a[208:211], v[224:227]\n"
"ds_read_b64_tr_b16 a[236:237], v3 offset:28672\n"
"ds_read_b64_tr_b16 a[238:239], v3 offset:30720\n"
"v_permlane32_swap_b32 v192, v194\n"
"v_permlane32_swap_b32 v193, v195\n"
"v_cvt_pk_bf16_f32 v198, v212, v213\n"
"v_permlane16_swap_b32 v192, v194\n"
"v_permlane16_swap_b32 v193, v195\n"
"v_cvt_pk_bf16_f32 v199, v214, v215\n"
"v_permlane32_swap_b32 v196, v198\n"
"v_mfma_f32_16x16x32_bf16 v[228:231], a[248:251], a[212:215], v[228:231]\n"
"ds_read_b64_tr_b16 a[240:241], v31 offset:0\n"
"ds_read_b64_tr_b16 a[242:243], v31 offset:2048\n"
"v_permlane32_swap_b32 v197, v199\n"
"v_permlane16_swap_b32 v196, v198\n"
"v_cvt_pk_bf16_f32 v202, v216, v217\n"
"v_permlane16_swap_b32 v197, v199\n"
"v_cvt_pk_bf16_f32 v203, v218, v219\n"
"v_permlane32_swap_b32 v200, v202\n"
"v_mfma_f32_16x16x32_bf16 v[232:235], a[248:251], a[216:219], v[232:235]\n"
"ds_write_b128 v28, v[192:195] offset:0\n"
"v_permlane32_swap_b32 v201, v203\n"
"v_permlane16_swap_b32 v200, v202\n"
"v_cvt_pk_bf16_f32 v206, v220, v221\n"
"v_permlane16_swap_b32 v201, v203\n"
"v_cvt_pk_bf16_f32 v207, v222, v223\n"
"v_permlane32_swap_b32 v204, v206\n"
"v_mfma_f32_16x16x32_bf16 v[236:239], a[248:251], a[220:223], v[236:239]\n"
"buffer_load_dwordx4 v21, s[40:43], s67 offen offset:1024 lds\n"
"v_permlane32_swap_b32 v205, v207\n"
"v_permlane16_swap_b32 v204, v206\n"
"v_mfma_f32_16x16x32_bf16 v[240:243], a[252:255], a[208:211], v[240:243]\n"
"ds_read_b64_tr_b16 a[244:245], v31 offset:4096\n"
"v_permlane16_swap_b32 v205, v207\n"
"v_cvt_pk_bf16_f32 v228, v228, v229\n"
"v_mfma_f32_16x16x32_bf16 v[244:247], a[252:255], a[212:215], v[244:247]\n"
"ds_read_b64_tr_b16 a[246:247], v31 offset:6144\n"
"ds_read_b64_tr_b16 a[176:177], v31 offset:8192\n"
"v_cvt_pk_bf16_f32 v224, v224, v225\n"
"v_cvt_pk_bf16_f32 v225, v226, v227\n"
"v_mfma_f32_16x16x32_bf16 v[248:251], a[252:255], a[216:219], v[248:251]\n"
"ds_read_b64_tr_b16 a[178:179], v31 offset:10240\n"
"v_cvt_pk_bf16_f32 v229, v230, v231\n"
"v_cvt_pk_bf16_f32 v232, v232, v233\n"
"v_cvt_pk_bf16_f32 v233, v234, v235\n"
"v_mfma_f32_16x16x32_bf16 v[252:255], a[252:255], a[220:223], v[252:255]\n"
"ds_write_b128 v28, v[196:199] offset:1024\n"
"v_cvt_pk_bf16_f32 v226, v240, v241\n"
"v_cvt_pk_bf16_f32 v227, v242, v243\n"
"v_cvt_pk_bf16_f32 v236, v236, v237\n"
"v_permlane32_swap_b32 v224, v226\n"
"s_waitcnt lgkmcnt(14)\n"
"v_mfma_f32_16x16x32_bf16 v[48:51], a[224:227], a[0:3], 0\n"
"buffer_load_dwordx4 v22, s[40:43], s67 offen offset:2048 lds\n"
"v_permlane16_swap_b32 v224, v226\n"
"v_permlane32_swap_b32 v225, v227\n"
"v_cvt_pk_bf16_f32 v230, v244, v245\n"
"v_mfma_f32_16x16x32_bf16 v[52:55], a[224:227], a[4:7], 0\n"
"v_permlane16_swap_b32 v225, v227\n"
"v_cvt_pk_bf16_f32 v237, v238, v239\n"
"v_mfma_f32_16x16x32_bf16 v[56:59], a[224:227], a[8:11], 0\n"
"ds_write_b128 v28, v[200:203] offset:2048\n"
"v_cvt_pk_bf16_f32 v231, v246, v247\n"
"v_mfma_f32_16x16x32_bf16 v[60:63], a[224:227], a[12:15], 0\n"
"v_permlane32_swap_b32 v228, v230\n"
"v_cvt_pk_bf16_f32 v239, v254, v255\n"
"s_waitcnt lgkmcnt(7)\n"
"v_mfma_f32_16x16x32_bf16 v[48:51], a[228:231], a[16:19], v[48:51]\n"
"ds_write_b128 v28, v[204:207] offset:3072\n"
"v_permlane32_swap_b32 v229, v231\n"
"v_permlane16_swap_b32 v228, v230\n"
"v_mfma_f32_16x16x32_bf16 v[52:55], a[228:231], a[20:23], v[52:55]\n"
"buffer_load_dwordx4 v23, s[40:43], s67 offen offset:3072 lds\n"
"v_permlane16_swap_b32 v229, v231\n"
"v_cvt_pk_bf16_f32 v234, v248, v249\n"
"s_waitcnt vmcnt(16)\n"
"s_barrier\n"
"v_mfma_f32_16x16x32_bf16 v[56:59], a[228:231], a[24:27], v[56:59]\n"
"ds_read_b128 a[180:183], v12 offset:0\n"
"v_cvt_pk_bf16_f32 v235, v250, v251\n"
"v_mfma_f32_16x16x32_bf16 v[60:63], a[228:231], a[28:31], v[60:63]\n"
"ds_read_b128 a[184:187], v12 offset:8192\n"
"v_permlane32_swap_b32 v232, v234\n"
"v_mfma_f32_16x16x32_bf16 v[48:51], a[232:235], a[32:35], v[48:51]\n"
"ds_read_b128 a[188:191], v13 offset:0\n"
"v_permlane32_swap_b32 v233, v235\n"
"v_cvt_pk_bf16_f32 v238, v252, v253\n"
"v_mfma_f32_16x16x32_bf16 v[52:55], a[232:235], a[36:39], v[52:55]\n"
"s_add_u32 m0, s61, 98304\n"
"ds_read_b128 a[200:203], v14 offset:8192\n"
"v_permlane32_swap_b32 v236, v238\n"
"v_mfma_f32_16x16x32_bf16 v[56:59], a[232:235], a[40:43], v[56:59]\n"
"ds_write_b128 v28, v[224:227] offset:4096\n"
"s_add_u32 s67, s67, 16384\n"
"v_mfma_f32_16x16x32_bf16 v[60:63], a[232:235], a[44:47], v[60:63]\n"
"buffer_load_dwordx4 v20, s[40:43], s67 offen offset:0 lds\n"
"v_mfma_f32_16x16x32_bf16 v[48:51], a[236:239], a[48:51], v[48:51]\n"
"v_permlane32_swap_b32 v237, v239\n"
"v_mfma_f32_16x16x32_bf16 v[52:55], a[236:239], a[52:55], v[52:55]\n"
"ds_read_b128 a[192:195], v13 offset:8192\n"
"s_add_u32 s66, s66, 16384\n"
"v_mfma_f32_16x16x32_bf16 v[56:59], a[236:239], a[56:59], v[56:59]\n"
"ds_read_b128 a[196:199], v14 offset:0\n"
"v_permlane16_swap_b32 v232, v234\n"
"v_mfma_f32_16x16x32_bf16 v[60:63], a[236:239], a[60:63], v[60:63]\n"
"ds_read_b128 a[204:207], v15 offset:0\n"
"v_permlane16_swap_b32 v233, v235\n"
"v_mfma_f32_16x16x32_bf16 v[48:51], a[240:243], a[64:67], v[48:51]\n"
"buffer_load_dwordx4 v21, s[40:43], s67 offen offset:1024 lds\n"
"ds_read_b128 a[208:211], v15 offset:8192\n"
"v_permlane16_swap_b32 v236, v238\n"
"v_mfma_f32_16x16x32_bf16 v[52:55], a[240:243], a[68:71], v[52:55]\n"
"v_permlane16_swap_b32 v237, v239\n"
"v_mfma_f32_16x16x32_bf16 v[56:59], a[240:243], a[72:75], v[56:59]\n"
"ds_write_b128 v28, v[228:231] offset:5120\n"
"v_mov_b32 v240, 0x3f803f80\n"
"v_mfma_f32_16x16x32_bf16 v[60:63], a[240:243], a[76:79], v[60:63]\n"
"ds_write_b128 v28, v[232:235] offset:6144\n"
"ds_write_b128 v28, v[236:239] offset:7168\n"
"s_waitcnt lgkmcnt(8)\n"
"v_mfma_f32_16x16x32_bf16 v[48:51], a[244:247], a[80:83], v[48:51]\n"
"v_mov_b32 v241, 0x3f803f80\n"
"v_mov_b32 v242, 0x3f803f80\n"
"v_mfma_f32_16x16x32_bf16 v[52:55], a[244:247], a[84:87], v[52:55]\n"
"buffer_load_dwordx4 v22, s[40:43], s67 offen offset:2048 lds\n"
"v_mfma_f32_16x16x32_bf16 v[56:59], a[244:247], a[88:91], v[56:59]\n"
"v_mov_b32 v243, 0x3f803f80\n"
"v_and_b32 v248, 15, v7\n"
"v_mfma_f32_16x16x32_bf16 v[60:63], a[244:247], a[92:95], v[60:63]\n"
"v_mul_u32_u24 v248, 896, v248\n"
"v_mov_b32 v28, 0x40000000\n"
"v_mfma_f32_16x16x32_bf16 v[48:51], a[176:179], a[96:99], v[48:51]\n"
"ds_read_b128 a[212:215], v12 offset:256\n"
"v_mov_b32 v246, s70\n"
"v_mfma_f32_16x16x32_bf16 v[52:55], a[176:179], a[100:103], v[52:55]\n"
"ds_read_b128 a[216:219], v12 offset:8448\n"
"v_mov_b32 v247, s71\n"
"v_mfma_f32_16x16x32_bf16 v[56:59], a[176:179], a[104:107], v[56:59]\n"
"buffer_load_dwordx4 v23, s[40:43], s67 offen offset:3072 lds\n"
"v_mfma_f32_16x16x32_bf16 v[60:63], a[176:179], a[108:111], v[60:63]\n"
"ds_read_b128 a[220:223], v13 offset:256\n"
"v_cvt_pk_bf16_f32 v48, v48, v49\n"
"v_mfma_f32_16x16x32_bf16 v[32:35], a[180:183], a[112:115], 0\n"
"ds_read_b128 a[224:227], v13 offset:8448\n"
"v_cvt_pk_bf16_f32 v49, v50, v51\n"
"v_mfma_f32_16x16x32_bf16 v[40:43], a[180:183], a[116:119], 0\n"
"ds_read_b128 a[228:231], v14 offset:256\n"
"s_waitcnt vmcnt(12) lgkmcnt(5)\n"
"s_barrier\n"
"v_mfma_f32_16x16x32_bf16 v[36:39], a[184:187], a[112:115], 0\n"
"s_add_u32 m0, s61, 147456\n"
"ds_read_b128 a[144:147], v2 offset:0\n"
"v_mfma_f32_16x16x32_bf16 v[44:47], a[184:187], a[116:119], 0\n"
"buffer_load_dwordx4 v24, s[36:39], s66 offen offset:0 lds\n"
"ds_read_b128 a[148:151], v2 offset:1024\n"
"v_mfma_f32_16x16x32_bf16 v[32:35], a[188:191], a[120:123], v[32:35]\n"
"ds_read_b128 a[152:155], v2 offset:4096\n"
"v_cvt_pk_bf16_f32 v50, v52, v53\n"
"v_mfma_f32_16x16x32_bf16 v[40:43], a[188:191], a[124:127], v[40:43]\n"
"ds_read_b128 a[156:159], v2 offset:5120\n"
"v_cvt_pk_bf16_f32 v56, v56, v57\n"
"v_mfma_f32_16x16x32_bf16 v[36:39], a[192:195], a[120:123], v[36:39]\n"
"ds_read_b128 a[232:235], v14 offset:8448\n"
"v_cvt_pk_bf16_f32 v51, v54, v55\n"
"v_mfma_f32_16x16x32_bf16 v[44:47], a[192:195], a[124:127], v[44:47]\n"
"ds_read_b128 a[160:163], v2 offset:8192\n"
"v_cvt_pk_bf16_f32 v57, v58, v59\n"
"v_mfma_f32_16x16x32_bf16 v[32:35], a[196:199], a[128:131], v[32:35]\n"
"buffer_store_dwordx4 v[48:51], v1, s[20:23], s68 offen sc1\n"
"ds_read_b128 a[236:239], v15 offset:256\n"
"v_mfma_f32_16x16x32_bf16 v[40:43], a[196:199], a[132:135], v[40:43]\n"
"ds_read_b128 a[164:167], v2 offset:9216\n"
"v_cvt_pk_bf16_f32 v58, v60, v61\n"
"v_mfma_f32_16x16x32_bf16 v[36:39], a[200:203], a[128:131], v[36:39]\n"
"ds_read_b128 a[172:175], v2 offset:13312\n"
"v_cvt_pk_bf16_f32 v59, v62, v63\n"
"v_mfma_f32_16x16x32_bf16 v[44:47], a[200:203], a[132:135], v[44:47]\n"
"ds_read_b128 a[240:243], v15 offset:8448\n"
"v_cndmask_b32 v252, v252, v48, vcc\n"
"v_mfma_f32_16x16x32_bf16 v[32:35], a[204:207], a[136:139], v[32:35]\n"
"ds_read_b128 a[168:171], v2 offset:12288\n"
"v_cndmask_b32 v253, v253, v49, vcc\n"
"v_mfma_f32_16x16x32_bf16 v[40:43], a[204:207], a[140:143], v[40:43]\n"
"buffer_store_dwordx4 v[56:59], v1, s[20:23], s69 offen sc1\n"
"ds_read_b64_tr_b16 a[244:245], v31 offset:12288\n"
"ds_read_b64_tr_b16 a[246:247], v31 offset:14336\n"
"v_mfma_f32_16x16x32_bf16 v[36:39], a[208:211], a[136:139], v[36:39]\n"
"ds_read_b64_tr_b16 a[176:177], v30 offset:32768\n"
"ds_read_b64_tr_b16 a[178:179], v30 offset:34816\n"
"v_mfma_f32_16x16x32_bf16 v[44:47], a[208:211], a[140:143], v[44:47]\n"
"ds_read_b64_tr_b16 a[180:181], v30 offset:36864\n"
"ds_read_b64_tr_b16 a[182:183], v30 offset:38912\n"
"s_waitcnt lgkmcnt(13)\n"
"v_mfma_f32_16x16x32_bf16 v[32:35], a[212:215], a[144:147], v[32:35]\n"
"ds_read_b64_tr_b16 a[184:185], v30 offset:40960\n"
"ds_read_b64_tr_b16 a[186:187], v30 offset:43008\n"
"v_mfma_f32_16x16x32_bf16 v[40:43], a[212:215], a[148:151], v[40:43]\n"
"ds_read_b64_tr_b16 a[188:189], v30 offset:45056\n"
"ds_read_b64_tr_b16 a[190:191], v30 offset:47104\n"
"v_mfma_f32_16x16x32_bf16 v[36:39], a[216:219], a[144:147], v[36:39]\n"
"buffer_load_dwordx4 v25, s[36:39], s66 offen offset:1024 lds\n"
"v_mfma_f32_16x16x32_bf16 v[44:47], a[216:219], a[148:151], v[44:47]\n"
"v_cndmask_b32 v254, v254, v50, vcc\n"
"v_cndmask_b32 v255, v255, v51, vcc\n"
"v_mfma_f32_16x16x32_bf16 v[32:35], a[220:223], a[152:155], v[32:35]\n"
"ds_read_b64_tr_b16 a[192:193], v30 offset:49152\n"
"ds_read_b64_tr_b16 a[194:195], v30 offset:51200\n"
"v_mfma_f32_16x16x32_bf16 v[40:43], a[220:223], a[156:159], v[40:43]\n"
"s_cmp_ge_u32 s57, 0x40000000\n"
"s_cselect_b64 vcc, -1, 0\n"
"v_mfma_f32_16x16x32_bf16 v[36:39], a[224:227], a[152:155], v[36:39]\n"
"ds_read_b64_tr_b16 a[196:197], v30 offset:53248\n"
"ds_read_b64_tr_b16 a[198:199], v30 offset:55296\n"
"v_mfma_f32_16x16x32_bf16 v[44:47], a[224:227], a[156:159], v[44:47]\n"
"v_cndmask_b32 v252, v252, v56, vcc\n"
"s_waitcnt lgkmcnt(12)\n"
"v_mfma_f32_16x16x32_bf16 v[32:35], a[228:231], a[160:163], v[32:35]\n"
"buffer_load_dwordx4 v26, s[36:39], s66 offen offset:2048 lds\n"
"v_mfma_f32_16x16x32_bf16 v[40:43], a[228:231], a[164:167], v[40:43]\n"
"v_cndmask_b32 v253, v253, v57, vcc\n"
"v_cndmask_b32 v254, v254, v58, vcc\n"
"v_mfma_f32_16x16x32_bf16 v[36:39], a[232:235], a[160:163], v[36:39]\n"
"v_cndmask_b32 v255, v255, v59, vcc\n"
"s_add_u32 s68, s29, 8192\n"
"v_mfma_f32_16x16x32_bf16 v[44:47], a[232:235], a[164:167], v[44:47]\n"
"s_add_u32 s69, s58, 8192\n"
"s_add_u32 s67, s67, 16384\n"
"v_mfma_f32_16x16x32_bf16 v[32:35], a[236:239], a[168:171], v[32:35]\n"
"s_cmp_ge_u32 s29, 0x40000000\n"
"s_cselect_b64 vcc, -1, 0\n"
"v_mfma_f32_16x16x32_bf16 v[40:43], a[236:239], a[172:175], v[40:43]\n"
"buffer_load_dwordx4 v27, s[36:39], s66 offen offset:3072 lds\n"
"v_mfma_f32_16x16x32_bf16 v[36:39], a[240:243], a[168:171], v[36:39]\n"
"s_add_u32 s66, s66, 16384\n"
"s_add_u32 m0, s61, 114688\n"
"v_mfma_f32_16x16x32_bf16 v[44:47], a[240:243], a[172:175], v[44:47]\n"
"v_max3_f32 v224, v32, v33, v34\n"
"v_and_b32 v244, 7, v7\n"
"v_mfma_f32_16x16x32_bf16 v[48:51], a[244:247], a[0:3], 0\n"
"v_max3_f32 v226, v40, v41, v42\n"
"v_lshlrev_b32 v244, 3, v244\n"
"v_mfma_f32_16x16x32_bf16 v[52:55], a[244:247], a[4:7], 0\n"
"v_max3_f32 v224, v35, v36, v224\n"
"v_max3_f32 v224, v37, v38, v224\n"
"v_max_f32 v224, v39, v224\n"
"v_mov_b32 v225, v224\n"
"v_mfma_f32_16x16x32_bf16 v[56:59], a[244:247], a[8:11], 0\n"
"v_max3_f32 v226, v43, v44, v226\n"
"v_permlane32_swap_b32 v224, v225\n"
"v_max_f32 v224, v224, v225\n"
"v_mov_b32 v225, v224\n"
"v_max3_f32 v226, v45, v46, v226\n"
"v_max_f32 v226, v47, v226\n"
"v_permlane16_swap_b32 v224, v225\n"
"v_max_f32 v224, v224, v225\n"
"v_mov_b32 v227, v226\n"
"v_mfma_f32_16x16x32_bf16 v[60:63], a[244:247], a[12:15], 0\n"
"v_sub_f32 v32, v32, v224\n"
"v_permlane32_swap_b32 v226, v227\n"
"v_max_f32 v226, v226, v227\n"
"v_mov_b32 v227, v226\n"
"s_waitcnt lgkmcnt(0)\n"
"v_mfma_f32_16x16x32_bf16 v[48:51], a[176:179], a[16:19], v[48:51]\n"
"v_permlane16_swap_b32 v226, v227\n"
"v_max_f32 v226, v226, v227\n"
"v_sub_f32 v33, v33, v224\n"
"v_mfma_f32_16x16x32_bf16 v[52:55], a[176:179], a[20:23], v[52:55]\n"
"s_waitcnt vmcnt(10)\n"
"s_barrier\n"
"ds_read_b128 a[200:203], v12 offset:16384\n"
"v_mfma_f32_16x16x32_bf16 v[56:59], a[176:179], a[24:27], v[56:59]\n"
"buffer_load_dwordx4 v24, s[36:39], s66 offen offset:0 lds\n"
"ds_read_b128 a[204:207], v12 offset:24576\n"
"v_sub_f32 v40, v40, v226\n"
"v_mfma_f32_16x16x32_bf16 v[60:63], a[176:179], a[28:31], v[60:63]\n"
"ds_read_b128 a[228:231], v15 offset:24576\n"
"v_sub_f32 v41, v41, v226\n"
"v_mfma_f32_16x16x32_bf16 v[48:51], a[180:183], a[32:35], v[48:51]\n"
"ds_read_b128 a[208:211], v13 offset:16384\n"
"v_sub_f32 v208, 0, v224\n"
"v_mfma_f32_16x16x32_bf16 v[52:55], a[180:183], a[36:39], v[52:55]\n"
"ds_read_b128 a[232:235], v12 offset:16640\n"
"v_sub_f32 v209, 0, v224\n"
"v_mfma_f32_16x16x32_bf16 v[56:59], a[180:183], a[40:43], v[56:59]\n"
"buffer_load_dwordx4 v25, s[36:39], s66 offen offset:1024 lds\n"
"v_mfma_f32_16x16x32_bf16 v[60:63], a[180:183], a[44:47], v[60:63]\n"
"ds_read_b128 a[236:239], v12 offset:24832\n"
"v_sub_f32 v210, 0, v224\n"
"v_mfma_f32_16x16x32_bf16 v[48:51], a[184:187], a[48:51], v[48:51]\n"
"ds_read_b128 a[212:215], v13 offset:24576\n"
"v_exp_f32 v32, v32\n"
"v_mfma_f32_16x16x32_bf16 v[52:55], a[184:187], a[52:55], v[52:55]\n"
"ds_read_b128 a[240:243], v13 offset:16640\n"
"v_exp_f32 v33, v33\n"
"v_mfma_f32_16x16x32_bf16 v[56:59], a[184:187], a[56:59], v[56:59]\n"
"buffer_load_dwordx4 v26, s[36:39], s66 offen offset:2048 lds\n"
"ds_read_b128 a[244:247], v13 offset:24832\n"
"v_sub_f32 v211, 0, v224\n"
"v_mfma_f32_16x16x32_bf16 v[60:63], a[184:187], a[60:63], v[60:63]\n"
"ds_read_b128 a[216:219], v14 offset:16384\n"
"v_sub_f32 v34, v34, v224\n"
"v_sub_f32 v212, 0, v226\n"
"v_mfma_f32_16x16x32_bf16 v[48:51], a[188:191], a[64:67], v[48:51]\n"
"ds_read_b128 a[220:223], v14 offset:24576\n"
"v_sub_f32 v213, 0, v226\n"
"v_sub_f32 v214, 0, v226\n"
"v_mfma_f32_16x16x32_bf16 v[52:55], a[188:191], a[68:71], v[52:55]\n"
"ds_read_b128 a[224:227], v15 offset:16384\n"
"v_sub_f32 v215, 0, v226\n"
"v_mfma_f32_16x16x32_bf16 v[56:59], a[188:191], a[72:75], v[56:59]\n"
"buffer_load_dwordx4 v27, s[36:39], s66 offen offset:3072 lds\n"
"s_add_u32 m0, s61, 0\n"
"v_sub_f32 v35, v35, v224\n"
"v_mfma_f32_16x16x32_bf16 v[60:63], a[188:191], a[76:79], v[60:63]\n"
"ds_read_b128 a[176:179], v14 offset:16640\n"
"v_sub_f32 v42, v42, v226\n"
"v_mfma_f32_16x16x32_bf16 v[48:51], a[192:195], a[80:83], v[48:51]\n"
"ds_read_b128 a[180:183], v14 offset:24832\n"
"v_sub_f32 v43, v43, v226\n"
"v_mfma_f32_16x16x32_bf16 v[52:55], a[192:195], a[84:87], v[52:55]\n"
"ds_read_b128 a[184:187], v15 offset:16640\n"
"v_exp_f32 v40, v40\n"
"v_mfma_f32_16x16x32_bf16 v[56:59], a[192:195], a[88:91], v[56:59]\n"
"buffer_load_dwordx4 v16, s[44:47], s64 offen offset:0 lds\n"
"ds_read_b128 a[188:191], v15 offset:24832\n"
"v_exp_f32 v41, v41\n"
"v_mfma_f32_16x16x32_bf16 v[60:63], a[192:195], a[92:95], v[60:63]\n"
"v_sub_f32 v36, v36, v224\n"
"v_cvt_pk_bf16_f32 v32, v32, v33\n"
"v_mfma_f32_16x16x32_bf16 v[48:51], a[196:199], a[96:99], v[48:51]\n"
"ds_read_b64_tr_b16 a[192:193], v8 offset:16384\n"
"ds_read_b64_tr_b16 a[194:195], v8 offset:18432\n"
"v_mfma_f32_16x16x32_bf16 v[52:55], a[196:199], a[100:103], v[52:55]\n"
"v_exp_f32 v34, v34\n"
"v_mfma_f32_16x16x32_bf16 v[56:59], a[196:199], a[104:107], v[56:59]\n"
"buffer_load_dwordx4 v17, s[44:47], s64 offen offset:1024 lds\n"
"v_mfma_f32_16x16x32_bf16 v[60:63], a[196:199], a[108:111], v[60:63]\n"
"v_sub_f32 v37, v37, v224\n"
"s_waitcnt lgkmcnt(6)\n"
"v_mfma_f32_16x16x32_bf16 v[192:195], a[200:203], a[112:115], v[208:211]\n"
"v_exp_f32 v35, v35\n"
"v_mfma_f32_16x16x32_bf16 v[200:203], a[200:203], a[116:119], v[212:215]\n"
"ds_read_b64_tr_b16 a[200:201], v10 offset:16384\n"
"ds_read_b64_tr_b16 a[202:203], v10 offset:18432\n"
"v_mfma_f32_16x16x32_bf16 v[196:199], a[204:207], a[112:115], v[208:211]\n"
"buffer_load_dwordx4 v18, s[44:47], s64 offen offset:2048 lds\n"
"v_sub_f32 v38, v38, v224\n"
"v_mfma_f32_16x16x32_bf16 v[204:207], a[204:207], a[116:119], v[212:215]\n"
"v_sub_f32 v39, v39, v224\n"
"ds_read_b64_tr_b16 a[206:207], v11 offset:18432\n"
"v_mfma_f32_16x16x32_bf16 v[192:195], a[208:211], a[120:123], v[192:195]\n"
"v_sub_f32 v44, v44, v226\n"
"v_sub_f32 v45, v45, v226\n"
"v_mfma_f32_16x16x32_bf16 v[200:203], a[208:211], a[124:127], v[200:203]\n"
"ds_read_b64_tr_b16 a[208:209], v8 offset:20480\n"
"ds_read_b64_tr_b16 a[210:211], v8 offset:22528\n"
"v_mfma_f32_16x16x32_bf16 v[196:199], a[212:215], a[120:123], v[196:199]\n"
"buffer_load_dwordx4 v19, s[44:47], s64 offen offset:3072 lds\n"
"v_sub_f32 v46, v46, v226\n"
"v_mfma_f32_16x16x32_bf16 v[204:207], a[212:215], a[124:127], v[204:207]\n"
"v_sub_f32 v47, v47, v226\n"
"v_cvt_pk_bf16_f32 v40, v40, v41\n"
"v_mfma_f32_16x16x32_bf16 v[192:195], a[216:219], a[128:131], v[192:195]\n"
"v_exp_f32 v42, v42\n"
"v_mfma_f32_16x16x32_bf16 v[200:203], a[216:219], a[132:135], v[200:203]\n"
"v_exp_f32 v43, v43\n"
"v_mfma_f32_16x16x32_bf16 v[196:199], a[220:223], a[128:131], v[196:199]\n"
"s_add_u32 m0, s61, 16384\n"
"ds_read_b64_tr_b16 a[196:197], v9 offset:16384\n"
"ds_read_b64_tr_b16 a[216:217], v10 offset:20480\n"
"v_mfma_f32_16x16x32_bf16 v[204:207], a[220:223], a[132:135], v[204:207]\n"
"ds_read_b64_tr_b16 a[218:219], v10 offset:22528\n"
"ds_read_b64_tr_b16 a[198:199], v9 offset:18432\n"
"v_mfma_f32_16x16x32_bf16 v[192:195], a[224:227], a[136:139], v[192:195]\n"
"ds_read_b64_tr_b16 a[220:221], v11 offset:20480\n"
"ds_read_b64_tr_b16 a[222:223], v11 offset:22528\n"
"v_exp_f32 v36, v36\n"
"v_mfma_f32_16x16x32_bf16 v[200:203], a[224:227], a[140:143], v[200:203]\n"
"v_cvt_pk_bf16_f32 v33, v34, v35\n"
"ds_read_b64_tr_b16 a[204:205], v11 offset:16384\n"
"v_mfma_f32_16x16x32_bf16 v[196:199], a[228:231], a[136:139], v[196:199]\n"
"v_exp_f32 v37, v37\n"
"v_mfma_f32_16x16x32_bf16 v[204:207], a[228:231], a[140:143], v[204:207]\n"
"v_exp_f32 v38, v38\n"
"v_mfma_f32_16x16x32_bf16 v[192:195], a[232:235], a[144:147], v[192:195]\n"
"v_exp_f32 v39, v39\n"
"v_mfma_f32_16x16x32_bf16 v[200:203], a[232:235], a[148:151], v[200:203]\n"
"v_exp_f32 v44, v44\n"
"v_mfma_f32_16x16x32_bf16 v[196:199], a[236:239], a[144:147], v[196:199]\n"
"v_exp_f32 v45, v45\n"
"ds_read_b64_tr_b16 a[212:213], v9 offset:20480\n"
"v_mfma_f32_16x16x32_bf16 v[204:207], a[236:239], a[148:151], v[204:207]\n"
"v_exp_f32 v46, v46\n"
"ds_read_b64_tr_b16 a[214:215], v9 offset:22528\n"
"v_mfma_f32_16x16x32_bf16 v[192:195], a[240:243], a[152:155], v[192:195]\n"
"v_exp_f32 v47, v47\n"
"v_mfma_f32_16x16x32_bf16 v[200:203], a[240:243], a[156:159], v[200:203]\n"
"v_cvt_pk_bf16_f32 v41, v42, v43\n"
"ds_read_b64_tr_b16 a[228:229], v9 offset:24576\n"
"v_mfma_f32_16x16x32_bf16 v[196:199], a[244:247], a[152:155], v[196:199]\n"
"ds_read_b64_tr_b16 a[230:231], v9 offset:26624\n"
"ds_read_b64_tr_b16 a[232:233], v10 offset:24576\n"
"v_mfma_f32_16x16x32_bf16 v[204:207], a[244:247], a[156:159], v[204:207]\n"
"ds_read_b64_tr_b16 a[234:235], v10 offset:26624\n"
"s_waitcnt lgkmcnt(6)\n"
"v_mfma_f32_16x16x32_bf16 v[192:195], a[176:179], a[160:163], v[192:195]\n"
"ds_read_b64_tr_b16 a[224:225], v8 offset:24576\n"
"ds_read_b64_tr_b16 a[226:227], v8 offset:26624\n"
"v_mfma_f32_16x16x32_bf16 v[200:203], a[176:179], a[164:167], v[200:203]\n"
"v_cvt_pk_bf16_f32 v34, v36, v37\n"
"v_cvt_pk_bf16_f32 v35, v38, v39\n"
"v_mfma_f32_16x16x32_bf16 v[196:199], a[180:183], a[160:163], v[196:199]\n"
"v_cvt_pk_bf16_f32 v42, v44, v45\n"
"v_cvt_pk_bf16_f32 v43, v46, v47\n"
"v_mfma_f32_16x16x32_bf16 v[204:207], a[180:183], a[164:167], v[204:207]\n"
"v_cvt_pk_bf16_f32 v48, v48, v49\n"
"ds_read_b64_tr_b16 a[236:237], v11 offset:24576\n"
"v_mfma_f32_16x16x32_bf16 v[192:195], a[184:187], a[168:171], v[192:195]\n"
"ds_read_b64_tr_b16 a[238:239], v11 offset:26624\n"
"ds_read_b64_tr_b16 a[240:241], v8 offset:28672\n"
"v_mfma_f32_16x16x32_bf16 v[200:203], a[184:187], a[172:175], v[200:203]\n"
"s_add_u32 s64, s64, 114688\n"
"ds_read_b64_tr_b16 a[242:243], v8 offset:30720\n"
"v_mfma_f32_16x16x32_bf16 v[196:199], a[188:191], a[168:171], v[196:199]\n"
"v_cvt_pk_bf16_f32 v49, v50, v51\n"
"ds_read_b64_tr_b16 a[244:245], v9 offset:28672\n"
"v_mfma_f32_16x16x32_bf16 v[204:207], a[188:191], a[172:175], v[204:207]\n"
"ds_read_b64_tr_b16 a[246:247], v9 offset:30720\n"
"ds_read_b64_tr_b16 a[176:177], v10 offset:28672\n"
"v_mfma_f32_16x16x32_bf16 v[216:219], v[240:243], v[32:35], 0\n"
"ds_read_b64_tr_b16 a[178:179], v10 offset:30720\n"
"v_cvt_pk_bf16_f32 v50, v52, v53\n"
"v_mfma_f32_16x16x32_bf16 v[220:223], v[240:243], v[40:43], 0\n"
"s_cmp_ge_u32 s64, s77\n"
"s_cselect_b32 s84, 200576, 0\n"
"v_mfma_f32_16x16x32_bf16 v[64:67], a[192:195], v[32:35], 0\n"
"v_cvt_pk_bf16_f32 v51, v54, v55\n"
"ds_read_b64_tr_b16 a[180:181], v11 offset:28672\n"
"v_mfma_f32_16x16x32_bf16 v[72:75], a[192:195], v[40:43], 0\n"
"buffer_store_dwordx4 v[48:51], v1, s[20:23], s68 offen sc1\n"
"v_mfma_f32_16x16x32_bf16 v[68:71], a[196:199], v[32:35], 0\n"
"v_cvt_pk_bf16_f32 v56, v56, v57\n"
"ds_read_b64_tr_b16 a[182:183], v11 offset:30720\n"
"v_mfma_f32_16x16x32_bf16 v[76:79], a[196:199], v[40:43], 0\n"
"s_sub_u32 s64, s64, s84\n"
"v_cvt_pk_bf16_f32 v57, v58, v59\n"
"v_mfma_f32_16x16x32_bf16 v[80:83], a[200:203], v[32:35], 0\n"
"v_cvt_pk_bf16_f32 v58, v60, v61\n"
"v_cvt_pk_bf16_f32 v59, v62, v63\n"
"v_mfma_f32_16x16x32_bf16 v[88:91], a[200:203], v[40:43], 0\n"
"v_lshrrev_b32 v224, 4, v7\n"
"v_lshl_add_u32 v248, v224, 3, v248\n"
"v_mfma_f32_16x16x32_bf16 v[84:87], a[204:207], v[32:35], 0\n"
"buffer_load_dwordx4 v16, s[44:47], s64 offen offset:0 lds\n"
"v_mfma_f32_16x16x32_bf16 v[92:95], a[204:207], v[40:43], 0\n"
"v_exp_f32 v192, v192\n"
"v_mfma_f32_16x16x32_bf16 v[96:99], a[208:211], v[32:35], 0\n"
"v_exp_f32 v193, v193\n"
"v_mfma_f32_16x16x32_bf16 v[104:107], a[208:211], v[40:43], 0\n"
"s_waitcnt vmcnt(2)\n"
"s_barrier\n"
"ds_read_b128 a[184:187], v12 offset:32768\n"
"s_waitcnt lgkmcnt(3)\n"
"v_mfma_f32_16x16x32_bf16 v[100:103], a[212:215], v[32:35], 0\n"
"buffer_load_dwordx4 v17, s[44:47], s64 offen offset:1024 lds\n"
"ds_read_b128 a[188:191], v12 offset:40960\n"
"v_mfma_f32_16x16x32_bf16 v[108:111], a[212:215], v[40:43], 0\n"
"ds_read_b128 a[192:195], v13 offset:32768\n"
"v_cvt_pk_bf16_f32 v192, v192, v193\n"
"v_mfma_f32_16x16x32_bf16 v[112:115], a[216:219], v[32:35], 0\n"
"v_exp_f32 v194, v194\n"
"v_mfma_f32_16x16x32_bf16 v[120:123], a[216:219], v[40:43], 0\n"
"buffer_store_dwordx4 v[56:59], v1, s[20:23], s69 offen sc1\n"
"ds_read_b128 a[196:199], v13 offset:40960\n"
"v_mfma_f32_16x16x32_bf16 v[116:119], a[220:223], v[32:35], 0\n"
"ds_read_b128 a[200:203], v14 offset:32768\n"
"v_add_u32 v249, 14336, v248\n"
"v_mfma_f32_16x16x32_bf16 v[124:127], a[220:223], v[40:43], 0\n"
"ds_read_b128 a[204:207], v14 offset:40960\n"
"v_cndmask_b32 v252, v252, v48, vcc\n"
"v_mfma_f32_16x16x32_bf16 v[128:131], a[224:227], v[32:35], 0\n"
"ds_read_b128 a[208:211], v15 offset:32768\n"
"v_cndmask_b32 v253, v253, v49, vcc\n"
"v_mfma_f32_16x16x32_bf16 v[136:139], a[224:227], v[40:43], 0\n"
"buffer_load_dwordx4 v18, s[44:47], s64 offen offset:2048 lds\n"
"v_mfma_f32_16x16x32_bf16 v[132:135], a[228:231], v[32:35], 0\n"
"ds_read_b128 a[212:215], v15 offset:40960\n"
"v_cndmask_b32 v254, v254, v50, vcc\n"
"v_mfma_f32_16x16x32_bf16 v[140:143], a[228:231], v[40:43], 0\n"
"ds_read_b128 a[216:219], v12 offset:33024\n"
"v_cndmask_b32 v255, v255, v51, vcc\n"
"v_mfma_f32_16x16x32_bf16 v[144:147], a[232:235], v[32:35], 0\n"
"ds_read_b128 a[220:223], v12 offset:41216\n"
"s_add_u32 s66, s66, 16384\n"
"v_mfma_f32_16x16x32_bf16 v[152:155], a[232:235], v[40:43], 0\n"
"buffer_load_dwordx4 v19, s[44:47], s64 offen offset:3072 lds\n"
"s_add_u32 m0, s61, 65536\n"
"ds_read_b128 a[224:227], v13 offset:33024\n"
"v_mfma_f32_16x16x32_bf16 v[148:151], a[236:239], v[32:35], 0\n"
"v_exp_f32 v195, v195\n"
"v_mfma_f32_16x16x32_bf16 v[156:159], a[236:239], v[40:43], 0\n"
"ds_read_b128 a[228:231], v13 offset:41216\n"
"v_cvt_pk_bf16_f32 v193, v194, v195\n"
"v_mfma_f32_16x16x32_bf16 v[160:163], a[240:243], v[32:35], 0\n"
"ds_read_b128 a[232:235], v14 offset:33024\n"
"s_add_u32 s64, s64, 114688\n"
"v_mfma_f32_16x16x32_bf16 v[168:171], a[240:243], v[40:43], 0\n"
"buffer_load_dwordx4 v20, s[40:43], s67 offen offset:0 lds\n"
"v_mfma_f32_16x16x32_bf16 v[164:167], a[244:247], v[32:35], 0\n"
"ds_read_b128 a[236:239], v14 offset:41216\n"
"s_add_u32 s68, s30, 16384\n"
"v_mfma_f32_16x16x32_bf16 v[172:175], a[244:247], v[40:43], 0\n"
"ds_read_b128 a[240:243], v15 offset:33024\n"
"s_add_u32 s69, s59, 16384\n"
"v_mfma_f32_16x16x32_bf16 v[176:179], a[176:179], v[32:35], 0\n"
"ds_read_b128 a[244:247], v15 offset:41216\n"
"v_mov_b32 v224, 0x40000000\n"
"v_mfma_f32_16x16x32_bf16 v[184:187], a[176:179], v[40:43], 0\n"
"buffer_load_dwordx4 v21, s[40:43], s67 offen offset:1024 lds\n"
"ds_read_b64_tr_b16 a[176:177], v8 offset:32768\n"
"s_waitcnt lgkmcnt(8)\n"
"v_mfma_f32_16x16x32_bf16 v[180:183], a[180:183], v[32:35], 0\n"
"ds_read_b64_tr_b16 a[178:179], v8 offset:34816\n"
"v_add_u32 v244, s81, v244\n"
"v_mfma_f32_16x16x32_bf16 v[188:191], a[180:183], v[40:43], 0\n"
"ds_read_b64_tr_b16 a[180:181], v9 offset:32768\n"
"ds_read_b64_tr_b16 a[182:183], v9 offset:34816\n"
"v_mfma_f32_16x16x32_bf16 v[32:35], a[184:187], a[112:115], v[208:211]\n"
"v_exp_f32 v200, v200\n"
"v_mfma_f32_16x16x32_bf16 v[40:43], a[184:187], a[116:119], v[212:215]\n"
"buffer_load_dwordx4 v22, s[40:43], s67 offen offset:2048 lds\n"
"v_mfma_f32_16x16x32_bf16 v[36:39], a[188:191], a[112:115], v[208:211]\n"
"ds_read_b64_tr_b16 a[184:185], v10 offset:32768\n"
"ds_read_b64_tr_b16 a[186:187], v10 offset:34816\n"
"v_mfma_f32_16x16x32_bf16 v[44:47], a[188:191], a[116:119], v[212:215]\n"
"ds_read_b64_tr_b16 a[188:189], v11 offset:32768\n"
"ds_read_b64_tr_b16 a[190:191], v11 offset:34816\n"
"v_mfma_f32_16x16x32_bf16 v[32:35], a[192:195], a[120:123], v[32:35]\n"
"v_exp_f32 v201, v201\n"
"v_mfma_f32_16x16x32_bf16 v[40:43], a[192:195], a[124:127], v[40:43]\n"
"ds_read_b64_tr_b16 a[192:193], v8 offset:36864\n"
"ds_read_b64_tr_b16 a[194:195], v8 offset:38912\n"
"v_mfma_f32_16x16x32_bf16 v[36:39], a[196:199], a[120:123], v[36:39]\n"
"buffer_load_dwordx4 v23, s[40:43], s67 offen offset:3072 lds\n"
"v_mfma_f32_16x16x32_bf16 v[44:47], a[196:199], a[124:127], v[44:47]\n"
"v_exp_f32 v196, v196\n"
"v_mfma_f32_16x16x32_bf16 v[32:35], a[200:203], a[128:131], v[32:35]\n"
"v_exp_f32 v197, v197\n"
"v_mfma_f32_16x16x32_bf16 v[40:43], a[200:203], a[132:135], v[40:43]\n"
"v_exp_f32 v198, v198\n"
"v_mfma_f32_16x16x32_bf16 v[36:39], a[204:207], a[128:131], v[36:39]\n"
"s_add_u32 m0, s61, 131072\n"
"ds_read_b64_tr_b16 a[196:197], v9 offset:36864\n"
"v_mfma_f32_16x16x32_bf16 v[44:47], a[204:207], a[132:135], v[44:47]\n"
"buffer_load_dwordx2 a[248:249], v248, s[8:11], s76 offen offset:0\n"
"v_mfma_f32_16x16x32_bf16 v[32:35], a[208:211], a[136:139], v[32:35]\n"
"v_exp_f32 v199, v199\n"
"v_mfma_f32_16x16x32_bf16 v[40:43], a[208:211], a[140:143], v[40:43]\n"
"ds_read_b64_tr_b16 a[208:209], v8 offset:40960\n"
"ds_read_b64_tr_b16 a[210:211], v8 offset:43008\n"
"v_mfma_f32_16x16x32_bf16 v[36:39], a[212:215], a[136:139], v[36:39]\n"
"ds_read_b64_tr_b16 a[198:199], v9 offset:38912\n"
"v_cvt_pk_bf16_f32 v200, v200, v201\n"
"v_mfma_f32_16x16x32_bf16 v[44:47], a[212:215], a[140:143], v[44:47]\n"
"buffer_load_dwordx2 a[250:251], v249, s[8:11], s76 offen offset:0\n"
"v_mfma_f32_16x16x32_bf16 v[32:35], a[216:219], a[144:147], v[32:35]\n"
"ds_read_b64_tr_b16 a[212:213], v9 offset:40960\n"
"ds_read_b64_tr_b16 a[200:201], v10 offset:36864\n"
"v_mfma_f32_16x16x32_bf16 v[40:43], a[216:219], a[148:151], v[40:43]\n"
"ds_read_b64_tr_b16 a[214:215], v9 offset:43008\n"
"s_waitcnt lgkmcnt(7)\n"
"v_mfma_f32_16x16x32_bf16 v[36:39], a[220:223], a[144:147], v[36:39]\n"
"v_exp_f32 v202, v202\n"
"v_mfma_f32_16x16x32_bf16 v[44:47], a[220:223], a[148:151], v[44:47]\n"
"buffer_load_dwordx2 a[252:253], v248, s[8:11], s76 offen offset:512\n"
"v_mfma_f32_16x16x32_bf16 v[32:35], a[224:227], a[152:155], v[32:35]\n"
"ds_read_b64_tr_b16 a[202:203], v10 offset:38912\n"
"ds_read_b64_tr_b16 a[204:205], v11 offset:36864\n"
"v_mfma_f32_16x16x32_bf16 v[40:43], a[224:227], a[156:159], v[40:43]\n"
"v_exp_f32 v203, v203\n"
"v_mfma_f32_16x16x32_bf16 v[36:39], a[228:231], a[152:155], v[36:39]\n"
"ds_read_b64_tr_b16 a[206:207], v11 offset:38912\n"
"ds_read_b64_tr_b16 a[216:217], v10 offset:40960\n"
"v_mfma_f32_16x16x32_bf16 v[44:47], a[228:231], a[156:159], v[44:47]\n"
"buffer_load_dwordx2 a[254:255], v249, s[8:11], s76 offen offset:512\n"
"ds_read_b64_tr_b16 a[218:219], v10 offset:43008\n"
"v_mfma_f32_16x16x32_bf16 v[32:35], a[232:235], a[160:163], v[32:35]\n"
"v_cvt_pk_bf16_f32 v194, v196, v197\n"
"v_cvt_pk_bf16_f32 v195, v198, v199\n"
"v_mfma_f32_16x16x32_bf16 v[40:43], a[232:235], a[164:167], v[40:43]\n"
"v_exp_f32 v204, v204\n"
"v_mfma_f32_16x16x32_bf16 v[36:39], a[236:239], a[160:163], v[36:39]\n"
"v_exp_f32 v205, v205\n"
"v_mfma_f32_16x16x32_bf16 v[44:47], a[236:239], a[164:167], v[44:47]\n"
"v_exp_f32 v206, v206\n"
"v_mfma_f32_16x16x32_bf16 v[32:35], a[240:243], a[168:171], v[32:35]\n"
"buffer_load_dwordx4 v24, s[36:39], s66 offen offset:0 lds\n"
"v_cvt_pk_bf16_f32 v201, v202, v203\n"
"v_mfma_f32_16x16x32_bf16 v[40:43], a[240:243], a[172:175], v[40:43]\n"
"v_exp_f32 v207, v207\n"
"v_mfma_f32_16x16x32_bf16 v[36:39], a[244:247], a[168:171], v[36:39]\n"
"v_cvt_pk_bf16_f32 v202, v204, v205\n"
"v_cvt_pk_bf16_f32 v203, v206, v207\n"
"v_mfma_f32_16x16x32_bf16 v[44:47], a[244:247], a[172:175], v[44:47]\n"
"ds_read_b64_tr_b16 a[220:221], v11 offset:40960\n"
"ds_read_b64_tr_b16 a[222:223], v11 offset:43008\n"
"v_mfma_f32_16x16x32_bf16 v[216:219], v[240:243], v[192:195], v[216:219]\n"
"ds_read_b64_tr_b16 a[224:225], v8 offset:45056\n"
"ds_read_b64_tr_b16 a[226:227], v8 offset:47104\n"
"v_mfma_f32_16x16x32_bf16 v[220:223], v[240:243], v[200:203], v[220:223]\n"
"buffer_load_dwordx4 v25, s[36:39], s66 offen offset:1024 lds\n"
"v_mfma_f32_16x16x32_bf16 v[64:67], a[176:179], v[192:195], v[64:67]\n"
"ds_read_b64_tr_b16 a[228:229], v9 offset:45056\n"
"ds_read_b64_tr_b16 a[230:231], v9 offset:47104\n"
"v_mfma_f32_16x16x32_bf16 v[72:75], a[176:179], v[200:203], v[72:75]\n"
"ds_read_b64_tr_b16 a[232:233], v10 offset:45056\n"
"ds_read_b64_tr_b16 a[234:235], v10 offset:47104\n"
"v_mfma_f32_16x16x32_bf16 v[68:71], a[180:183], v[192:195], v[68:71]\n"
"ds_read_b64_tr_b16 a[236:237], v11 offset:45056\n"
"ds_read_b64_tr_b16 a[238:239], v11 offset:47104\n"
"v_mfma_f32_16x16x32_bf16 v[76:79], a[180:183], v[200:203], v[76:79]\n"
"ds_read_b64_tr_b16 a[240:241], v30 offset:57344\n"
"ds_read_b64_tr_b16 a[242:243], v30 offset:59392\n"
"v_mfma_f32_16x16x32_bf16 v[80:83], a[184:187], v[192:195], v[80:83]\n"
"buffer_load_dwordx4 v26, s[36:39], s66 offen offset:2048 lds\n"
"v_mfma_f32_16x16x32_bf16 v[88:91], a[184:187], v[200:203], v[88:91]\n"
"ds_read_b64_tr_b16 a[244:245], v30 offset:61440\n"
"ds_read_b64_tr_b16 a[246:247], v30 offset:63488\n"
"v_mfma_f32_16x16x32_bf16 v[84:87], a[188:191], v[192:195], v[84:87]\n"
"ds_read_b64_tr_b16 a[176:177], v30 offset:0\n"
"ds_read_b64_tr_b16 a[178:179], v30 offset:2048\n"
"v_mfma_f32_16x16x32_bf16 v[92:95], a[188:191], v[200:203], v[92:95]\n"
"ds_read_b64_tr_b16 a[180:181], v30 offset:4096\n"
"ds_read_b64_tr_b16 a[182:183], v30 offset:6144\n"
"v_mfma_f32_16x16x32_bf16 v[96:99], a[192:195], v[192:195], v[96:99]\n"
"ds_read_b64_tr_b16 a[184:185], v30 offset:8192\n"
"ds_read_b64_tr_b16 a[186:187], v30 offset:10240\n"
"v_mfma_f32_16x16x32_bf16 v[104:107], a[192:195], v[200:203], v[104:107]\n"
"ds_read_b64_tr_b16 a[188:189], v30 offset:12288\n"
"s_waitcnt lgkmcnt(15)\n"
"v_mfma_f32_16x16x32_bf16 v[100:103], a[196:199], v[192:195], v[100:103]\n"
"buffer_load_dwordx4 v27, s[36:39], s66 offen offset:3072 lds\n"
"v_mfma_f32_16x16x32_bf16 v[108:111], a[196:199], v[200:203], v[108:111]\n"
"ds_read_b64_tr_b16 a[190:191], v30 offset:14336\n"
"s_add_u32 m0, s61, 32768\n"
"v_mfma_f32_16x16x32_bf16 v[112:115], a[200:203], v[192:195], v[112:115]\n"
"s_cmp_ge_u32 s58, 0x40000000\n"
"s_cselect_b64 vcc, -1, 0\n"
"v_mfma_f32_16x16x32_bf16 v[120:123], a[200:203], v[200:203], v[120:123]\n"
"v_cndmask_b32 v252, v252, v56, vcc\n"
"v_cndmask_b32 v253, v253, v57, vcc\n"
"v_mfma_f32_16x16x32_bf16 v[116:119], a[204:207], v[192:195], v[116:119]\n"
"v_cndmask_b32 v254, v254, v58, vcc\n"
"v_cndmask_b32 v255, v255, v59, vcc\n"
"v_mfma_f32_16x16x32_bf16 v[124:127], a[204:207], v[200:203], v[124:127]\n"
"s_cmp_ge_u32 s64, s77\n"
"s_cselect_b32 s84, 200576, 0\n"
"v_mfma_f32_16x16x32_bf16 v[128:131], a[208:211], v[192:195], v[128:131]\n"
"s_sub_u32 s64, s64, s84\n"
"s_add_u32 s67, s67, 16384\n"
"v_mfma_f32_16x16x32_bf16 v[136:139], a[208:211], v[200:203], v[136:139]\n"
"buffer_load_dwordx4 v16, s[44:47], s64 offen offset:0 lds\n"
"v_mfma_f32_16x16x32_bf16 v[132:135], a[212:215], v[192:195], v[132:135]\n"
"v_exp_f32 v32, v32\n"
"v_mfma_f32_16x16x32_bf16 v[140:143], a[212:215], v[200:203], v[140:143]\n"
"v_exp_f32 v33, v33\n"
"v_mfma_f32_16x16x32_bf16 v[144:147], a[216:219], v[192:195], v[144:147]\n"
"v_exp_f32 v34, v34\n"
"v_mfma_f32_16x16x32_bf16 v[152:155], a[216:219], v[200:203], v[152:155]\n"
"v_exp_f32 v35, v35\n"
"v_mfma_f32_16x16x32_bf16 v[148:151], a[220:223], v[192:195], v[148:151]\n"
"buffer_load_dwordx4 v17, s[44:47], s64 offen offset:1024 lds\n"
"v_mfma_f32_16x16x32_bf16 v[156:159], a[220:223], v[200:203], v[156:159]\n"
"v_exp_f32 v40, v40\n"
"v_mfma_f32_16x16x32_bf16 v[160:163], a[224:227], v[192:195], v[160:163]\n"
"v_exp_f32 v41, v41\n"
"v_mfma_f32_16x16x32_bf16 v[168:171], a[224:227], v[200:203], v[168:171]\n"
"v_cvt_pk_bf16_f32 v32, v32, v33\n"
"v_cvt_pk_bf16_f32 v33, v34, v35\n"
"v_mfma_f32_16x16x32_bf16 v[164:167], a[228:231], v[192:195], v[164:167]\n"
"v_exp_f32 v36, v36\n"
"v_mfma_f32_16x16x32_bf16 v[172:175], a[228:231], v[200:203], v[172:175]\n"
"v_cvt_pk_bf16_f32 v40, v40, v41\n"
"s_waitcnt lgkmcnt(0)\n"
"v_mfma_f32_16x16x32_bf16 v[176:179], a[232:235], v[192:195], v[176:179]\n"
"buffer_load_dwordx4 v18, s[44:47], s64 offen offset:2048 lds\n"
"v_mfma_f32_16x16x32_bf16 v[184:187], a[232:235], v[200:203], v[184:187]\n"
"v_exp_f32 v37, v37\n"
"v_mfma_f32_16x16x32_bf16 v[180:183], a[236:239], v[192:195], v[180:183]\n"
"v_exp_f32 v38, v38\n"
"v_mfma_f32_16x16x32_bf16 v[188:191], a[236:239], v[200:203], v[188:191]\n"
"v_exp_f32 v39, v39\n"
"v_mfma_f32_16x16x32_bf16 v[48:51], a[240:243], a[0:3], 0\n"
"v_exp_f32 v42, v42\n"
"v_mfma_f32_16x16x32_bf16 v[52:55], a[240:243], a[4:7], 0\n"
"buffer_load_dwordx4 v19, s[44:47], s64 offen offset:3072 lds\n"
"v_mfma_f32_16x16x32_bf16 v[56:59], a[240:243], a[8:11], 0\n"
"s_add_u32 m0, s61, 81920\n"
"v_cvt_pk_bf16_f32 v34, v36, v37\n"
"v_mfma_f32_16x16x32_bf16 v[60:63], a[240:243], a[12:15], 0\n"
"v_exp_f32 v43, v43\n"
"v_mfma_f32_16x16x32_bf16 v[48:51], a[244:247], a[16:19], v[48:51]\n"
"v_cvt_pk_bf16_f32 v35, v38, v39\n"
"v_cvt_pk_bf16_f32 v41, v42, v43\n"
"v_mfma_f32_16x16x32_bf16 v[52:55], a[244:247], a[20:23], v[52:55]\n"
"s_waitcnt vmcnt(12)\n"
"s_barrier\n"
"ds_read_b64_tr_b16 a[192:193], v30 offset:16384\n"
"ds_read_b64_tr_b16 a[194:195], v30 offset:18432\n"
"v_mfma_f32_16x16x32_bf16 v[56:59], a[244:247], a[24:27], v[56:59]\n"
"buffer_load_dwordx4 v20, s[40:43], s67 offen offset:0 lds\n"
"ds_read_b128 a[196:199], v12 offset:0\n"
"v_mfma_f32_16x16x32_bf16 v[60:63], a[244:247], a[28:31], v[60:63]\n"
"ds_read_b128 a[200:203], v12 offset:8192\n"
"s_add_u32 s66, s66, 16384\n"
"v_mfma_f32_16x16x32_bf16 v[48:51], a[176:179], a[32:35], v[48:51]\n"
"ds_read_b128 a[204:207], v13 offset:0\n"
"s_add_u32 s64, s64, 114688\n"
"v_mfma_f32_16x16x32_bf16 v[52:55], a[176:179], a[36:39], v[52:55]\n"
"ds_read_b128 a[208:211], v13 offset:8192\n"
"v_add_u32 v2, 98304, v2\n"
"v_mfma_f32_16x16x32_bf16 v[56:59], a[176:179], a[40:43], v[56:59]\n"
"ds_read_b128 a[212:215], v14 offset:0\n"
"v_add_u32 v245, 98304, v1\n"
"v_mfma_f32_16x16x32_bf16 v[60:63], a[176:179], a[44:47], v[60:63]\n"
"buffer_load_dwordx4 v21, s[40:43], s67 offen offset:1024 lds\n"
"v_mfma_f32_16x16x32_bf16 v[48:51], a[180:183], a[48:51], v[48:51]\n"
"ds_read_b128 a[216:219], v14 offset:8192\n"
"v_mfma_f32_16x16x32_bf16 v[52:55], a[180:183], a[52:55], v[52:55]\n"
"ds_read_b128 a[220:223], v15 offset:0\n"
"v_mfma_f32_16x16x32_bf16 v[56:59], a[180:183], a[56:59], v[56:59]\n"
"ds_read_b128 a[224:227], v15 offset:8192\n"
"v_mfma_f32_16x16x32_bf16 v[60:63], a[180:183], a[60:63], v[60:63]\n"
"ds_read_b128 a[228:231], v12 offset:256\n"
"v_mfma_f32_16x16x32_bf16 v[48:51], a[184:187], a[64:67], v[48:51]\n"
"buffer_load_dwordx4 v22, s[40:43], s67 offen offset:2048 lds\n"
"v_mfma_f32_16x16x32_bf16 v[52:55], a[184:187], a[68:71], v[52:55]\n"
"ds_read_b128 a[232:235], v12 offset:8448\n"
"v_mfma_f32_16x16x32_bf16 v[56:59], a[184:187], a[72:75], v[56:59]\n"
"ds_read_b128 a[236:239], v13 offset:256\n"
"v_mfma_f32_16x16x32_bf16 v[60:63], a[184:187], a[76:79], v[60:63]\n"
"ds_read_b128 a[240:243], v13 offset:8448\n"
"v_mfma_f32_16x16x32_bf16 v[48:51], a[188:191], a[80:83], v[48:51]\n"
"ds_read_b128 a[244:247], v14 offset:256\n"
"v_mfma_f32_16x16x32_bf16 v[52:55], a[188:191], a[84:87], v[52:55]\n"
"buffer_load_dwordx4 v23, s[40:43], s67 offen offset:3072 lds\n"
"v_mfma_f32_16x16x32_bf16 v[56:59], a[188:191], a[88:91], v[56:59]\n"
"ds_read_b128 a[176:179], v14 offset:8448\n"
"s_add_u32 m0, s61, 147456\n"
"v_mfma_f32_16x16x32_bf16 v[60:63], a[188:191], a[92:95], v[60:63]\n"
"ds_read_b128 a[180:183], v15 offset:256\n"
"s_waitcnt lgkmcnt(8)\n"
"v_mfma_f32_16x16x32_bf16 v[48:51], a[192:195], a[96:99], v[48:51]\n"
"ds_read_b128 a[184:187], v15 offset:8448\n"
"s_add_u32 s67, s67, 16384\n"
"v_mfma_f32_16x16x32_bf16 v[52:55], a[192:195], a[100:103], v[52:55]\n"
"ds_read_b64_tr_b16 a[188:189], v8 offset:0\n"
"ds_read_b64_tr_b16 a[190:191], v8 offset:2048\n"
"v_mfma_f32_16x16x32_bf16 v[56:59], a[192:195], a[104:107], v[56:59]\n"
"buffer_load_dwordx4 v24, s[36:39], s66 offen offset:0 lds\n"
"v_mfma_f32_16x16x32_bf16 v[60:63], a[192:195], a[108:111], v[60:63]\n"
"ds_read_b64_tr_b16 a[192:193], v9 offset:0\n"
"ds_read_b64_tr_b16 a[194:195], v9 offset:2048\n"
"v_mfma_f32_16x16x32_bf16 v[192:195], a[196:199], a[112:115], v[208:211]\n"
"v_exp_f32 v44, v44\n"
"v_mfma_f32_16x16x32_bf16 v[200:203], a[196:199], a[116:119], v[212:215]\n"
"ds_read_b64_tr_b16 a[196:197], v10 offset:0\n"
"ds_read_b64_tr_b16 a[198:199], v10 offset:2048\n"
"v_mfma_f32_16x16x32_bf16 v[196:199], a[200:203], a[112:115], v[208:211]\n"
"v_exp_f32 v45, v45\n"
"v_mfma_f32_16x16x32_bf16 v[204:207], a[200:203], a[116:119], v[212:215]\n"
"buffer_load_dwordx4 v25, s[36:39], s66 offen offset:1024 lds\n"
"v_mfma_f32_16x16x32_bf16 v[192:195], a[204:207], a[120:123], v[192:195]\n"
"ds_read_b64_tr_b16 a[200:201], v11 offset:0\n"
"ds_read_b64_tr_b16 a[202:203], v11 offset:2048\n"
"v_mfma_f32_16x16x32_bf16 v[200:203], a[204:207], a[124:127], v[200:203]\n"
"ds_read_b64_tr_b16 a[206:207], v8 offset:6144\n"
"ds_read_b64_tr_b16 a[204:205], v8 offset:4096\n"
"v_mfma_f32_16x16x32_bf16 v[196:199], a[208:211], a[120:123], v[196:199]\n"
"v_exp_f32 v46, v46\n"
"v_mfma_f32_16x16x32_bf16 v[204:207], a[208:211], a[124:127], v[204:207]\n"
"ds_read_b64_tr_b16 a[208:209], v9 offset:4096\n"
"ds_read_b64_tr_b16 a[210:211], v9 offset:6144\n"
"v_mfma_f32_16x16x32_bf16 v[192:195], a[212:215], a[128:131], v[192:195]\n"
"buffer_load_dwordx4 v26, s[36:39], s66 offen offset:2048 lds\n"
"v_mfma_f32_16x16x32_bf16 v[200:203], a[212:215], a[132:135], v[200:203]\n"
"ds_read_b64_tr_b16 a[212:213], v10 offset:4096\n"
"ds_read_b64_tr_b16 a[214:215], v10 offset:6144\n"
"v_mfma_f32_16x16x32_bf16 v[196:199], a[216:219], a[128:131], v[196:199]\n"
"v_exp_f32 v47, v47\n"
"v_mfma_f32_16x16x32_bf16 v[204:207], a[216:219], a[132:135], v[204:207]\n"
"ds_read_b64_tr_b16 a[216:217], v11 offset:4096\n"
"ds_read_b64_tr_b16 a[218:219], v11 offset:6144\n"
"v_mfma_f32_16x16x32_bf16 v[192:195], a[220:223], a[136:139], v[192:195]\n"
"v_cvt_pk_bf16_f32 v42, v44, v45\n"
"v_cvt_pk_bf16_f32 v43, v46, v47\n"
"v_mfma_f32_16x16x32_bf16 v[200:203], a[220:223], a[140:143], v[200:203]\n"
"ds_read_b64_tr_b16 a[220:221], v8 offset:8192\n"
"s_waitcnt lgkmcnt(11)\n"
"v_mfma_f32_16x16x32_bf16 v[196:199], a[224:227], a[136:139], v[196:199]\n"
"buffer_load_dwordx4 v27, s[36:39], s66 offen offset:3072 lds\n"
"v_mfma_f32_16x16x32_bf16 v[204:207], a[224:227], a[140:143], v[204:207]\n"
"ds_read_b64_tr_b16 a[222:223], v8 offset:10240\n"
"ds_read_b64_tr_b16 a[224:225], v9 offset:8192\n"
"v_mfma_f32_16x16x32_bf16 v[192:195], a[228:231], a[144:147], v[192:195]\n"
"ds_read_b64_tr_b16 a[226:227], v9 offset:10240\n"
"v_cvt_pk_bf16_f32 v48, v48, v49\n"
"v_mfma_f32_16x16x32_bf16 v[200:203], a[228:231], a[148:151], v[200:203]\n"
"ds_read_b64_tr_b16 a[228:229], v10 offset:8192\n"
"ds_read_b64_tr_b16 a[230:231], v10 offset:10240\n"
"v_mfma_f32_16x16x32_bf16 v[196:199], a[232:235], a[144:147], v[196:199]\n"
"s_add_u32 m0, s61, 98304\n"
"v_cvt_pk_bf16_f32 v49, v50, v51\n"
"v_mfma_f32_16x16x32_bf16 v[204:207], a[232:235], a[148:151], v[204:207]\n"
"ds_read_b64_tr_b16 a[232:233], v11 offset:8192\n"
"ds_read_b64_tr_b16 a[234:235], v11 offset:10240\n"
"v_mfma_f32_16x16x32_bf16 v[192:195], a[236:239], a[152:155], v[192:195]\n"
"v_cvt_pk_bf16_f32 v56, v56, v57\n"
"v_cvt_pk_bf16_f32 v50, v52, v53\n"
"v_mfma_f32_16x16x32_bf16 v[200:203], a[236:239], a[156:159], v[200:203]\n"
"ds_read_b64_tr_b16 a[236:237], v8 offset:12288\n"
"ds_read_b64_tr_b16 a[238:239], v8 offset:14336\n"
"v_mfma_f32_16x16x32_bf16 v[196:199], a[240:243], a[152:155], v[196:199]\n"
"v_cvt_pk_bf16_f32 v51, v54, v55\n"
"v_cvt_pk_bf16_f32 v57, v58, v59\n"
"v_mfma_f32_16x16x32_bf16 v[204:207], a[240:243], a[156:159], v[204:207]\n"
"ds_read_b64_tr_b16 a[240:241], v9 offset:12288\n"
"ds_read_b64_tr_b16 a[242:243], v9 offset:14336\n"
"v_mfma_f32_16x16x32_bf16 v[192:195], a[244:247], a[160:163], v[192:195]\n"
"v_cvt_pk_bf16_f32 v58, v60, v61\n"
"v_cvt_pk_bf16_f32 v59, v62, v63\n"
"v_mfma_f32_16x16x32_bf16 v[200:203], a[244:247], a[164:167], v[200:203]\n"
"ds_read_b64_tr_b16 a[244:245], v10 offset:12288\n"
"ds_read_b64_tr_b16 a[246:247], v10 offset:14336\n"
"v_mfma_f32_16x16x32_bf16 v[196:199], a[176:179], a[160:163], v[196:199]\n"
"s_add_u32 s66, s66, 16384\n"
"v_mfma_f32_16x16x32_bf16 v[204:207], a[176:179], a[164:167], v[204:207]\n"
"ds_read_b64_tr_b16 a[176:177], v11 offset:12288\n"
"ds_read_b64_tr_b16 a[178:179], v11 offset:14336\n"
"v_mfma_f32_16x16x32_bf16 v[192:195], a[180:183], a[168:171], v[192:195]\n"
"s_cmp_ge_u32 s30, 0x40000000\n"
"s_cselect_b64 vcc, -1, 0\n"
"v_mfma_f32_16x16x32_bf16 v[200:203], a[180:183], a[172:175], v[200:203]\n"
"buffer_store_dwordx4 v[48:51], v1, s[20:23], s68 offen sc1\n"
"v_mfma_f32_16x16x32_bf16 v[196:199], a[184:187], a[168:171], v[196:199]\n"
"v_cndmask_b32 v252, v252, v48, vcc\n"
"v_cndmask_b32 v253, v253, v49, vcc\n"
"v_mfma_f32_16x16x32_bf16 v[204:207], a[184:187], a[172:175], v[204:207]\n"
"v_exp_f32 v192, v192\n"
"v_mfma_f32_16x16x32_bf16 v[216:219], v[240:243], v[32:35], v[216:219]\n"
"v_exp_f32 v193, v193\n"
"v_mfma_f32_16x16x32_bf16 v[220:223], v[240:243], v[40:43], v[220:223]\n"
"v_exp_f32 v200, v200\n"
"v_mfma_f32_16x16x32_bf16 v[64:67], a[188:191], v[32:35], v[64:67]\n"
"buffer_store_dwordx4 v[56:59], v1, s[20:23], s69 offen sc1\n"
"v_mfma_f32_16x16x32_bf16 v[72:75], a[188:191], v[40:43], v[72:75]\n"
"v_exp_f32 v201, v201\n"
"v_mfma_f32_16x16x32_bf16 v[68:71], a[192:195], v[32:35], v[68:71]\n"
"v_cvt_pk_bf16_f32 v192, v192, v193\n"
"v_cvt_pk_bf16_f32 v200, v200, v201\n"
"v_mfma_f32_16x16x32_bf16 v[76:79], a[192:195], v[40:43], v[76:79]\n"
"v_exp_f32 v194, v194\n"
"v_mfma_f32_16x16x32_bf16 v[80:83], a[196:199], v[32:35], v[80:83]\n"
"v_exp_f32 v195, v195\n"
"v_mfma_f32_16x16x32_bf16 v[88:91], a[196:199], v[40:43], v[88:91]\n"
"v_cvt_pk_bf16_f32 v193, v194, v195\n"
"s_waitcnt lgkmcnt(0)\n"
"v_mfma_f32_16x16x32_bf16 v[84:87], a[200:203], v[32:35], v[84:87]\n"
"v_exp_f32 v202, v202\n"
"v_mfma_f32_16x16x32_bf16 v[92:95], a[200:203], v[40:43], v[92:95]\n"
"v_exp_f32 v203, v203\n"
"v_mfma_f32_16x16x32_bf16 v[96:99], a[204:207], v[32:35], v[96:99]\n"
"v_exp_f32 v196, v196\n"
"v_mfma_f32_16x16x32_bf16 v[104:107], a[204:207], v[40:43], v[104:107]\n"
"s_waitcnt vmcnt(6)\n"
"s_barrier\n"
"ds_read_b128 a[180:183], v12 offset:16384\n"
"v_mfma_f32_16x16x32_bf16 v[100:103], a[208:211], v[32:35], v[100:103]\n"
"buffer_load_dwordx4 v20, s[40:43], s67 offen offset:0 lds\n"
"ds_read_b128 a[184:187], v12 offset:24576\n"
"v_mfma_f32_16x16x32_bf16 v[108:111], a[208:211], v[40:43], v[108:111]\n"
"ds_read_b128 a[188:191], v13 offset:16384\n"
"v_cvt_pk_bf16_f32 v201, v202, v203\n"
"v_mfma_f32_16x16x32_bf16 v[112:115], a[212:215], v[32:35], v[112:115]\n"
"ds_read_b128 a[192:195], v13 offset:24576\n"
"v_cndmask_b32 v254, v254, v50, vcc\n"
"v_mfma_f32_16x16x32_bf16 v[120:123], a[212:215], v[40:43], v[120:123]\n"
"ds_read_b128 a[196:199], v14 offset:16384\n"
"v_cndmask_b32 v255, v255, v51, vcc\n"
"v_mfma_f32_16x16x32_bf16 v[116:119], a[216:219], v[32:35], v[116:119]\n"
"ds_read_b128 a[200:203], v14 offset:24576\n"
"s_add_u32 s68, s31, 24576\n"
"v_mfma_f32_16x16x32_bf16 v[124:127], a[216:219], v[40:43], v[124:127]\n"
"buffer_load_dwordx4 v21, s[40:43], s67 offen offset:1024 lds\n"
"v_mfma_f32_16x16x32_bf16 v[128:131], a[220:223], v[32:35], v[128:131]\n"
"ds_read_b128 a[204:207], v15 offset:16384\n"
"s_add_u32 s69, s33, 24576\n"
"v_mfma_f32_16x16x32_bf16 v[136:139], a[220:223], v[40:43], v[136:139]\n"
"ds_read_b128 a[208:211], v15 offset:24576\n"
"v_mfma_f32_16x16x32_bf16 v[132:135], a[224:227], v[32:35], v[132:135]\n"
"ds_read_b128 a[212:215], v12 offset:16640\n"
"v_mfma_f32_16x16x32_bf16 v[140:143], a[224:227], v[40:43], v[140:143]\n"
"ds_read_b128 a[216:219], v12 offset:24832\n"
"v_mfma_f32_16x16x32_bf16 v[144:147], a[228:231], v[32:35], v[144:147]\n"
"buffer_load_dwordx4 v22, s[40:43], s67 offen offset:2048 lds\n"
"v_mfma_f32_16x16x32_bf16 v[152:155], a[228:231], v[40:43], v[152:155]\n"
"ds_read_b128 a[220:223], v13 offset:16640\n"
"v_mfma_f32_16x16x32_bf16 v[148:151], a[232:235], v[32:35], v[148:151]\n"
"ds_read_b128 a[224:227], v13 offset:24832\n"
"v_mfma_f32_16x16x32_bf16 v[156:159], a[232:235], v[40:43], v[156:159]\n"
"ds_read_b128 a[228:231], v14 offset:16640\n"
"v_mfma_f32_16x16x32_bf16 v[160:163], a[236:239], v[32:35], v[160:163]\n"
"ds_read_b128 a[232:235], v14 offset:24832\n"
"v_mfma_f32_16x16x32_bf16 v[168:171], a[236:239], v[40:43], v[168:171]\n"
"buffer_load_dwordx4 v23, s[40:43], s67 offen offset:3072 lds\n"
"v_mfma_f32_16x16x32_bf16 v[164:167], a[240:243], v[32:35], v[164:167]\n"
"ds_read_b128 a[236:239], v15 offset:16640\n"
"s_add_u32 m0, s61, 114688\n"
"v_mfma_f32_16x16x32_bf16 v[172:175], a[240:243], v[40:43], v[172:175]\n"
"ds_read_b128 a[240:243], v15 offset:24832\n"
"s_add_u32 s67, s67, 16384\n"
"v_mfma_f32_16x16x32_bf16 v[176:179], a[244:247], v[32:35], v[176:179]\n"
"v_exp_f32 v197, v197\n"
"v_mfma_f32_16x16x32_bf16 v[184:187], a[244:247], v[40:43], v[184:187]\n"
"ds_read_b64_tr_b16 a[244:245], v8 offset:16384\n"
"ds_read_b64_tr_b16 a[246:247], v8 offset:18432\n"
"v_mfma_f32_16x16x32_bf16 v[180:183], a[176:179], v[32:35], v[180:183]\n"
"buffer_load_dwordx4 v24, s[36:39], s66 offen offset:0 lds\n"
"v_mfma_f32_16x16x32_bf16 v[188:191], a[176:179], v[40:43], v[188:191]\n"
"ds_read_b64_tr_b16 a[176:177], v9 offset:16384\n"
"s_waitcnt lgkmcnt(8)\n"
"v_mfma_f32_16x16x32_bf16 v[32:35], a[180:183], a[112:115], v[208:211]\n"
"ds_read_b64_tr_b16 a[178:179], v9 offset:18432\n"
"v_cvt_pk_bf16_f32 v194, v196, v197\n"
"v_mfma_f32_16x16x32_bf16 v[40:43], a[180:183], a[116:119], v[212:215]\n"
"ds_read_b64_tr_b16 a[180:181], v10 offset:16384\n"
"ds_read_b64_tr_b16 a[182:183], v10 offset:18432\n"
"v_mfma_f32_16x16x32_bf16 v[36:39], a[184:187], a[112:115], v[208:211]\n"
"v_exp_f32 v198, v198\n"
"v_mfma_f32_16x16x32_bf16 v[44:47], a[184:187], a[116:119], v[212:215]\n"
"buffer_load_dwordx4 v25, s[36:39], s66 offen offset:1024 lds\n"
"v_mfma_f32_16x16x32_bf16 v[32:35], a[188:191], a[120:123], v[32:35]\n"
"ds_read_b64_tr_b16 a[184:185], v11 offset:16384\n"
"ds_read_b64_tr_b16 a[186:187], v11 offset:18432\n"
"v_mfma_f32_16x16x32_bf16 v[40:43], a[188:191], a[124:127], v[40:43]\n"
"ds_read_b64_tr_b16 a[188:189], v8 offset:20480\n"
"ds_read_b64_tr_b16 a[190:191], v8 offset:22528\n"
"v_mfma_f32_16x16x32_bf16 v[36:39], a[192:195], a[120:123], v[36:39]\n"
"v_exp_f32 v199, v199\n"
"v_mfma_f32_16x16x32_bf16 v[44:47], a[192:195], a[124:127], v[44:47]\n"
"ds_read_b64_tr_b16 a[192:193], v9 offset:20480\n"
"ds_read_b64_tr_b16 a[194:195], v9 offset:22528\n"
"v_mfma_f32_16x16x32_bf16 v[32:35], a[196:199], a[128:131], v[32:35]\n"
"buffer_load_dwordx4 v26, s[36:39], s66 offen offset:2048 lds\n"
"v_mfma_f32_16x16x32_bf16 v[40:43], a[196:199], a[132:135], v[40:43]\n"
"ds_read_b64_tr_b16 a[196:197], v10 offset:20480\n"
"ds_read_b64_tr_b16 a[198:199], v10 offset:22528\n"
"v_mfma_f32_16x16x32_bf16 v[36:39], a[200:203], a[128:131], v[36:39]\n"
"v_exp_f32 v204, v204\n"
"v_mfma_f32_16x16x32_bf16 v[44:47], a[200:203], a[132:135], v[44:47]\n"
"ds_read_b64_tr_b16 a[200:201], v11 offset:20480\n"
"ds_read_b64_tr_b16 a[202:203], v11 offset:22528\n"
"v_mfma_f32_16x16x32_bf16 v[32:35], a[204:207], a[136:139], v[32:35]\n"
"v_exp_f32 v205, v205\n"
"v_mfma_f32_16x16x32_bf16 v[40:43], a[204:207], a[140:143], v[40:43]\n"
"buffer_load_dwordx4 v27, s[36:39], s66 offen offset:3072 lds\n"
"v_mfma_f32_16x16x32_bf16 v[36:39], a[208:211], a[136:139], v[36:39]\n"
"ds_read_b64_tr_b16 a[204:205], v8 offset:24576\n"
"ds_read_b64_tr_b16 a[206:207], v8 offset:26624\n"
"v_mfma_f32_16x16x32_bf16 v[44:47], a[208:211], a[140:143], v[44:47]\n"
"ds_read_b64_tr_b16 a[208:209], v9 offset:24576\n"
"ds_read_b64_tr_b16 a[210:211], v9 offset:26624\n"
"v_mfma_f32_16x16x32_bf16 v[32:35], a[212:215], a[144:147], v[32:35]\n"
"v_exp_f32 v206, v206\n"
"v_mfma_f32_16x16x32_bf16 v[40:43], a[212:215], a[148:151], v[40:43]\n"
"s_add_u32 m0, s61, 49152\n"
"ds_read_b64_tr_b16 a[212:213], v10 offset:24576\n"
"v_mfma_f32_16x16x32_bf16 v[36:39], a[216:219], a[144:147], v[36:39]\n"
"ds_read_b64_tr_b16 a[214:215], v10 offset:26624\n"
"v_cvt_pk_bf16_f32 v195, v198, v199\n"
"v_mfma_f32_16x16x32_bf16 v[44:47], a[216:219], a[148:151], v[44:47]\n"
"v_exp_f32 v207, v207\n"
"v_mfma_f32_16x16x32_bf16 v[32:35], a[220:223], a[152:155], v[32:35]\n"
"v_cvt_pk_bf16_f32 v202, v204, v205\n"
"v_cvt_pk_bf16_f32 v203, v206, v207\n"
"v_mfma_f32_16x16x32_bf16 v[40:43], a[220:223], a[156:159], v[40:43]\n"
"ds_read_b64_tr_b16 a[216:217], v11 offset:24576\n"
"s_waitcnt lgkmcnt(7)\n"
"v_mfma_f32_16x16x32_bf16 v[36:39], a[224:227], a[152:155], v[36:39]\n"
"ds_read_b64_tr_b16 a[218:219], v11 offset:26624\n"
"ds_read_b64_tr_b16 a[220:221], v8 offset:28672\n"
"v_mfma_f32_16x16x32_bf16 v[44:47], a[224:227], a[156:159], v[44:47]\n"
"ds_read_b64_tr_b16 a[222:223], v8 offset:30720\n"
"ds_read_b64_tr_b16 a[224:225], v9 offset:28672\n"
"v_mfma_f32_16x16x32_bf16 v[32:35], a[228:231], a[160:163], v[32:35]\n"
"ds_read_b64_tr_b16 a[226:227], v9 offset:30720\n"
"s_add_u32 s66, s66, 16384\n"
"v_mfma_f32_16x16x32_bf16 v[40:43], a[228:231], a[164:167], v[40:43]\n"
"ds_read_b64_tr_b16 a[228:229], v10 offset:28672\n"
"ds_read_b64_tr_b16 a[230:231], v10 offset:30720\n"
"v_mfma_f32_16x16x32_bf16 v[36:39], a[232:235], a[160:163], v[36:39]\n"
"s_cmp_ge_u32 s64, s77\n"
"s_cselect_b32 s84, 200576, 0\n"
"v_mfma_f32_16x16x32_bf16 v[44:47], a[232:235], a[164:167], v[44:47]\n"
"ds_read_b64_tr_b16 a[232:233], v11 offset:28672\n"
"ds_read_b64_tr_b16 a[234:235], v11 offset:30720\n"
"v_mfma_f32_16x16x32_bf16 v[32:35], a[236:239], a[168:171], v[32:35]\n"
"s_cmp_ge_u32 s59, 0x40000000\n"
"s_cselect_b64 vcc, -1, 0\n"
"v_mfma_f32_16x16x32_bf16 v[40:43], a[236:239], a[172:175], v[40:43]\n"
"ds_read_b64_tr_b16 a[236:237], v30 offset:20480\n"
"ds_read_b64_tr_b16 a[238:239], v30 offset:22528\n"
"v_mfma_f32_16x16x32_bf16 v[36:39], a[240:243], a[168:171], v[36:39]\n"
"s_sub_u32 s64, s64, s84\n"
"v_cndmask_b32 v252, v252, v56, vcc\n"
"v_mfma_f32_16x16x32_bf16 v[44:47], a[240:243], a[172:175], v[44:47]\n"
"buffer_load_dwordx4 v16, s[44:47], s64 offen offset:0 lds\n"
"v_mfma_f32_16x16x32_bf16 v[216:219], v[240:243], v[192:195], v[216:219]\n"
"ds_read_b64_tr_b16 a[240:241], v30 offset:24576\n"
"ds_read_b64_tr_b16 a[242:243], v30 offset:26624\n"
"v_mfma_f32_16x16x32_bf16 v[220:223], v[240:243], v[200:203], v[220:223]\n"
"v_cndmask_b32 v253, v253, v57, vcc\n"
"v_cndmask_b32 v254, v254, v58, vcc\n"
"v_mfma_f32_16x16x32_bf16 v[64:67], a[244:247], v[192:195], v[64:67]\n"
"v_cndmask_b32 v255, v255, v59, vcc\n"
"v_mfma_f32_16x16x32_bf16 v[72:75], a[244:247], v[200:203], v[72:75]\n"
"ds_read_b64_tr_b16 a[244:245], v30 offset:28672\n"
"ds_read_b64_tr_b16 a[246:247], v30 offset:30720\n"
"v_mfma_f32_16x16x32_bf16 v[68:71], a[176:179], v[192:195], v[68:71]\n"
"buffer_load_dwordx4 v17, s[44:47], s64 offen offset:1024 lds\n"
"v_mfma_f32_16x16x32_bf16 v[76:79], a[176:179], v[200:203], v[76:79]\n"
"ds_read_b64_tr_b16 a[176:177], v30 offset:32768\n"
"ds_read_b64_tr_b16 a[178:179], v30 offset:34816\n"
"v_mfma_f32_16x16x32_bf16 v[80:83], a[180:183], v[192:195], v[80:83]\n"
"v_exp_f32 v32, v32\n"
"v_mfma_f32_16x16x32_bf16 v[88:91], a[180:183], v[200:203], v[88:91]\n"
"ds_read_b64_tr_b16 a[180:181], v30 offset:36864\n"
"ds_read_b64_tr_b16 a[182:183], v30 offset:38912\n"
"v_mfma_f32_16x16x32_bf16 v[84:87], a[184:187], v[192:195], v[84:87]\n"
"v_exp_f32 v33, v33\n"
"v_mfma_f32_16x16x32_bf16 v[92:95], a[184:187], v[200:203], v[92:95]\n"
"buffer_load_dwordx4 v18, s[44:47], s64 offen offset:2048 lds\n"
"v_mfma_f32_16x16x32_bf16 v[96:99], a[188:191], v[192:195], v[96:99]\n"
"ds_read_b64_tr_b16 a[184:185], v30 offset:40960\n"
"ds_read_b64_tr_b16 a[186:187], v30 offset:43008\n"
"v_mfma_f32_16x16x32_bf16 v[104:107], a[188:191], v[200:203], v[104:107]\n"
"ds_read_b64_tr_b16 a[188:189], v30 offset:45056\n"
"ds_read_b64_tr_b16 a[190:191], v30 offset:47104\n"
"v_mfma_f32_16x16x32_bf16 v[100:103], a[192:195], v[192:195], v[100:103]\n"
"v_exp_f32 v34, v34\n"
"v_mfma_f32_16x16x32_bf16 v[108:111], a[192:195], v[200:203], v[108:111]\n"
"v_exp_f32 v35, v35\n"
"v_mfma_f32_16x16x32_bf16 v[112:115], a[196:199], v[192:195], v[112:115]\n"
"buffer_load_dwordx4 v19, s[44:47], s64 offen offset:3072 lds\n"
"v_mfma_f32_16x16x32_bf16 v[120:123], a[196:199], v[200:203], v[120:123]\n"
"s_add_u32 s64, s64, 114688\n"
"s_add_u32 m0, s61, 0\n"
"v_mfma_f32_16x16x32_bf16 v[116:119], a[200:203], v[192:195], v[116:119]\n"
"s_cmp_ge_u32 s64, s77\n"
"s_cselect_b32 s84, 200576, 0\n"
"v_mfma_f32_16x16x32_bf16 v[124:127], a[200:203], v[200:203], v[124:127]\n"
"s_sub_u32 s64, s64, s84\n"
"s_waitcnt lgkmcnt(4)\n"
"v_mfma_f32_16x16x32_bf16 v[128:131], a[204:207], v[192:195], v[128:131]\n"
"v_exp_f32 v40, v40\n"
"v_mfma_f32_16x16x32_bf16 v[136:139], a[204:207], v[200:203], v[136:139]\n"
"buffer_load_dwordx4 v16, s[44:47], s64 offen offset:0 lds\n"
"v_mfma_f32_16x16x32_bf16 v[132:135], a[208:211], v[192:195], v[132:135]\n"
"v_exp_f32 v41, v41\n"
"v_mfma_f32_16x16x32_bf16 v[140:143], a[208:211], v[200:203], v[140:143]\n"
"v_cvt_pk_bf16_f32 v32, v32, v33\n"
"v_cvt_pk_bf16_f32 v33, v34, v35\n"
"v_mfma_f32_16x16x32_bf16 v[144:147], a[212:215], v[192:195], v[144:147]\n"
"v_exp_f32 v36, v36\n"
"v_mfma_f32_16x16x32_bf16 v[152:155], a[212:215], v[200:203], v[152:155]\n"
"v_exp_f32 v37, v37\n"
"v_mfma_f32_16x16x32_bf16 v[148:151], a[216:219], v[192:195], v[148:151]\n"
"buffer_load_dwordx4 v17, s[44:47], s64 offen offset:1024 lds\n"
"v_mfma_f32_16x16x32_bf16 v[156:159], a[216:219], v[200:203], v[156:159]\n"
"v_exp_f32 v38, v38\n"
"v_mfma_f32_16x16x32_bf16 v[160:163], a[220:223], v[192:195], v[160:163]\n"
"v_exp_f32 v39, v39\n"
"v_mfma_f32_16x16x32_bf16 v[168:171], a[220:223], v[200:203], v[168:171]\n"
"v_cvt_pk_bf16_f32 v40, v40, v41\n"
"v_cvt_pk_bf16_f32 v34, v36, v37\n"
"v_mfma_f32_16x16x32_bf16 v[164:167], a[224:227], v[192:195], v[164:167]\n"
"v_exp_f32 v42, v42\n"
"v_mfma_f32_16x16x32_bf16 v[172:175], a[224:227], v[200:203], v[172:175]\n"
"buffer_load_dwordx4 v18, s[44:47], s64 offen offset:2048 lds\n"
"v_mfma_f32_16x16x32_bf16 v[176:179], a[228:231], v[192:195], v[176:179]\n"
"v_exp_f32 v43, v43\n"
"v_mfma_f32_16x16x32_bf16 v[184:187], a[228:231], v[200:203], v[184:187]\n"
"v_cvt_pk_bf16_f32 v35, v38, v39\n"
"v_cvt_pk_bf16_f32 v41, v42, v43\n"
"v_mfma_f32_16x16x32_bf16 v[180:183], a[232:235], v[192:195], v[180:183]\n"
"v_exp_f32 v44, v44\n"
"v_mfma_f32_16x16x32_bf16 v[188:191], a[232:235], v[200:203], v[188:191]\n"
"v_exp_f32 v45, v45\n"
"v_mfma_f32_16x16x32_bf16 v[48:51], a[236:239], a[0:3], 0\n"
"buffer_load_dwordx4 v19, s[44:47], s64 offen offset:3072 lds\n"
"v_mfma_f32_16x16x32_bf16 v[52:55], a[236:239], a[4:7], 0\n"
"s_add_u32 m0, s61, 65536\n"
"v_cvt_pk_bf16_f32 v42, v44, v45\n"
"v_mfma_f32_16x16x32_bf16 v[56:59], a[236:239], a[8:11], 0\n"
"v_exp_f32 v46, v46\n"
"v_mfma_f32_16x16x32_bf16 v[60:63], a[236:239], a[12:15], 0\n"
"v_exp_f32 v47, v47\n"
"v_mfma_f32_16x16x32_bf16 v[48:51], a[240:243], a[16:19], v[48:51]\n"
"v_cvt_pk_bf16_f32 v43, v46, v47\n"
"s_add_u32 s64, s64, 114688\n"
"v_mfma_f32_16x16x32_bf16 v[52:55], a[240:243], a[20:23], v[52:55]\n"
"s_waitcnt vmcnt(12)\n"
"s_barrier\n"
"ds_read_b128 a[192:195], v12 offset:32768\n"
"v_mfma_f32_16x16x32_bf16 v[56:59], a[240:243], a[24:27], v[56:59]\n"
"buffer_load_dwordx4 v20, s[40:43], s67 offen offset:0 lds\n"
"ds_read_b128 a[196:199], v12 offset:40960\n"
"v_mfma_f32_16x16x32_bf16 v[60:63], a[240:243], a[28:31], v[60:63]\n"
"ds_read_b128 a[200:203], v13 offset:32768\n"
"v_mfma_f32_16x16x32_bf16 v[48:51], a[244:247], a[32:35], v[48:51]\n"
"s_cmp_ge_u32 s31, 0x40000000\n"
"s_cselect_b64 vcc, -1, 0\n"
"v_mfma_f32_16x16x32_bf16 v[52:55], a[244:247], a[36:39], v[52:55]\n"
"ds_read_b128 a[204:207], v13 offset:40960\n"
"v_mfma_f32_16x16x32_bf16 v[56:59], a[244:247], a[40:43], v[56:59]\n"
"ds_read_b128 a[208:211], v14 offset:32768\n"
"v_mfma_f32_16x16x32_bf16 v[60:63], a[244:247], a[44:47], v[60:63]\n"
"buffer_load_dwordx4 v21, s[40:43], s67 offen offset:1024 lds\n"
"v_mfma_f32_16x16x32_bf16 v[48:51], a[176:179], a[48:51], v[48:51]\n"
"ds_read_b128 a[212:215], v14 offset:40960\n"
"v_mfma_f32_16x16x32_bf16 v[52:55], a[176:179], a[52:55], v[52:55]\n"
"ds_read_b128 a[216:219], v15 offset:32768\n"
"v_mfma_f32_16x16x32_bf16 v[56:59], a[176:179], a[56:59], v[56:59]\n"
"ds_read_b128 a[220:223], v15 offset:40960\n"
"v_mfma_f32_16x16x32_bf16 v[60:63], a[176:179], a[60:63], v[60:63]\n"
"ds_read_b128 a[224:227], v12 offset:33024\n"
"v_mfma_f32_16x16x32_bf16 v[48:51], a[180:183], a[64:67], v[48:51]\n"
"buffer_load_dwordx4 v22, s[40:43], s67 offen offset:2048 lds\n"
"v_mfma_f32_16x16x32_bf16 v[52:55], a[180:183], a[68:71], v[52:55]\n"
"ds_read_b128 a[228:231], v12 offset:41216\n"
"v_mfma_f32_16x16x32_bf16 v[56:59], a[180:183], a[72:75], v[56:59]\n"
"ds_read_b128 a[232:235], v13 offset:33024\n"
"v_mfma_f32_16x16x32_bf16 v[60:63], a[180:183], a[76:79], v[60:63]\n"
"ds_read_b128 a[236:239], v13 offset:41216\n"
"s_waitcnt lgkmcnt(8)\n"
"v_mfma_f32_16x16x32_bf16 v[48:51], a[184:187], a[80:83], v[48:51]\n"
"ds_read_b128 a[240:243], v14 offset:33024\n"
"v_mfma_f32_16x16x32_bf16 v[52:55], a[184:187], a[84:87], v[52:55]\n"
"buffer_load_dwordx4 v23, s[40:43], s67 offen offset:3072 lds\n"
"v_mfma_f32_16x16x32_bf16 v[56:59], a[184:187], a[88:91], v[56:59]\n"
"ds_read_b128 a[244:247], v14 offset:41216\n"
"s_add_u32 m0, s61, 131072\n"
"v_mfma_f32_16x16x32_bf16 v[60:63], a[184:187], a[92:95], v[60:63]\n"
"ds_read_b128 a[176:179], v15 offset:33024\n"
"s_add_u32 s67, s67, 16384\n"
"v_mfma_f32_16x16x32_bf16 v[48:51], a[188:191], a[96:99], v[48:51]\n"
"ds_read_b128 a[180:183], v15 offset:41216\n"
"v_mfma_f32_16x16x32_bf16 v[52:55], a[188:191], a[100:103], v[52:55]\n"
"ds_read_b64_tr_b16 a[184:185], v8 offset:32768\n"
"ds_read_b64_tr_b16 a[186:187], v8 offset:34816\n"
"v_mfma_f32_16x16x32_bf16 v[56:59], a[188:191], a[104:107], v[56:59]\n"
"buffer_load_dwordx4 v24, s[36:39], s66 offen offset:0 lds\n"
"v_mfma_f32_16x16x32_bf16 v[60:63], a[188:191], a[108:111], v[60:63]\n"
"ds_read_b64_tr_b16 a[188:189], v9 offset:32768\n"
"ds_read_b64_tr_b16 a[190:191], v9 offset:34816\n"
"v_mfma_f32_16x16x32_bf16 v[192:195], a[192:195], a[112:115], v[208:211]\n"
"v_cvt_pk_bf16_f32 v48, v48, v49\n"
"v_cvt_pk_bf16_f32 v49, v50, v51\n"
"v_mfma_f32_16x16x32_bf16 v[200:203], a[192:195], a[116:119], v[212:215]\n"
"ds_read_b64_tr_b16 a[192:193], v10 offset:32768\n"
"ds_read_b64_tr_b16 a[194:195], v10 offset:34816\n"
"v_mfma_f32_16x16x32_bf16 v[196:199], a[196:199], a[112:115], v[208:211]\n"
"v_cvt_pk_bf16_f32 v50, v52, v53\n"
"v_cvt_pk_bf16_f32 v51, v54, v55\n"
"v_mfma_f32_16x16x32_bf16 v[204:207], a[196:199], a[116:119], v[212:215]\n"
"buffer_load_dwordx4 v25, s[36:39], s66 offen offset:1024 lds\n"
"v_mfma_f32_16x16x32_bf16 v[192:195], a[200:203], a[120:123], v[192:195]\n"
"ds_read_b64_tr_b16 a[196:197], v11 offset:32768\n"
"ds_read_b64_tr_b16 a[198:199], v11 offset:34816\n"
"v_mfma_f32_16x16x32_bf16 v[200:203], a[200:203], a[124:127], v[200:203]\n"
"ds_read_b64_tr_b16 a[202:203], v8 offset:38912\n"
"ds_read_b64_tr_b16 a[200:201], v8 offset:36864\n"
"v_mfma_f32_16x16x32_bf16 v[196:199], a[204:207], a[120:123], v[196:199]\n"
"v_cvt_pk_bf16_f32 v56, v56, v57\n"
"v_cvt_pk_bf16_f32 v57, v58, v59\n"
"v_mfma_f32_16x16x32_bf16 v[204:207], a[204:207], a[124:127], v[204:207]\n"
"ds_read_b64_tr_b16 a[204:205], v9 offset:36864\n"
"s_waitcnt lgkmcnt(9)\n"
"v_mfma_f32_16x16x32_bf16 v[192:195], a[208:211], a[128:131], v[192:195]\n"
"buffer_load_dwordx4 v26, s[36:39], s66 offen offset:2048 lds\n"
"v_mfma_f32_16x16x32_bf16 v[200:203], a[208:211], a[132:135], v[200:203]\n"
"ds_read_b64_tr_b16 a[206:207], v9 offset:38912\n"
"ds_read_b64_tr_b16 a[208:209], v10 offset:36864\n"
"v_mfma_f32_16x16x32_bf16 v[196:199], a[212:215], a[128:131], v[196:199]\n"
"ds_read_b64_tr_b16 a[210:211], v10 offset:38912\n"
"v_cvt_pk_bf16_f32 v58, v60, v61\n"
"v_mfma_f32_16x16x32_bf16 v[204:207], a[212:215], a[132:135], v[204:207]\n"
"ds_read_b64_tr_b16 a[212:213], v11 offset:36864\n"
"ds_read_b64_tr_b16 a[214:215], v11 offset:38912\n"
"v_mfma_f32_16x16x32_bf16 v[192:195], a[216:219], a[136:139], v[192:195]\n"
"v_cvt_pk_bf16_f32 v59, v62, v63\n"
"v_cndmask_b32 v252, v252, v48, vcc\n"
"v_mfma_f32_16x16x32_bf16 v[200:203], a[216:219], a[140:143], v[200:203]\n"
"buffer_load_dwordx4 v27, s[36:39], s66 offen offset:3072 lds\n"
"v_mfma_f32_16x16x32_bf16 v[196:199], a[220:223], a[136:139], v[196:199]\n"
"ds_read_b64_tr_b16 a[216:217], v8 offset:40960\n"
"ds_read_b64_tr_b16 a[218:219], v8 offset:43008\n"
"v_mfma_f32_16x16x32_bf16 v[204:207], a[220:223], a[140:143], v[204:207]\n"
"ds_read_b64_tr_b16 a[220:221], v9 offset:40960\n"
"ds_read_b64_tr_b16 a[222:223], v9 offset:43008\n"
"v_mfma_f32_16x16x32_bf16 v[192:195], a[224:227], a[144:147], v[192:195]\n"
"s_add_u32 m0, s61, 81920\n"
"s_add_u32 s66, s66, 16384\n"
"v_mfma_f32_16x16x32_bf16 v[200:203], a[224:227], a[148:151], v[200:203]\n"
"ds_read_b64_tr_b16 a[224:225], v10 offset:40960\n"
"ds_read_b64_tr_b16 a[226:227], v10 offset:43008\n"
"v_mfma_f32_16x16x32_bf16 v[196:199], a[228:231], a[144:147], v[196:199]\n"
"v_cndmask_b32 v253, v253, v49, vcc\n"
"v_cndmask_b32 v254, v254, v50, vcc\n"
"v_mfma_f32_16x16x32_bf16 v[204:207], a[228:231], a[148:151], v[204:207]\n"
"ds_read_b64_tr_b16 a[228:229], v11 offset:40960\n"
"ds_read_b64_tr_b16 a[230:231], v11 offset:43008\n"
"v_mfma_f32_16x16x32_bf16 v[192:195], a[232:235], a[152:155], v[192:195]\n"
"v_cndmask_b32 v255, v255, v51, vcc\n"
"v_mfma_f32_16x16x32_bf16 v[200:203], a[232:235], a[156:159], v[200:203]\n"
"ds_read_b64_tr_b16 a[232:233], v8 offset:45056\n"
"ds_read_b64_tr_b16 a[234:235], v8 offset:47104\n"
"v_mfma_f32_16x16x32_bf16 v[196:199], a[236:239], a[152:155], v[196:199]\n"
"s_cmp_ge_u32 s64, s77\n"
"s_cselect_b32 s84, 200576, 0\n"
"v_mfma_f32_16x16x32_bf16 v[204:207], a[236:239], a[156:159], v[204:207]\n"
"ds_read_b64_tr_b16 a[236:237], v9 offset:45056\n"
"ds_read_b64_tr_b16 a[238:239], v9 offset:47104\n"
"v_mfma_f32_16x16x32_bf16 v[192:195], a[240:243], a[160:163], v[192:195]\n"
"s_cmp_ge_u32 s33, 0x40000000\n"
"s_cselect_b64 vcc, -1, 0\n"
"v_mfma_f32_16x16x32_bf16 v[200:203], a[240:243], a[164:167], v[200:203]\n"
"ds_read_b64_tr_b16 a[240:241], v10 offset:45056\n"
"ds_read_b64_tr_b16 a[242:243], v10 offset:47104\n"
"v_mfma_f32_16x16x32_bf16 v[196:199], a[244:247], a[160:163], v[196:199]\n"
"s_sub_u32 s64, s64, s84\n"
"v_cndmask_b32 v252, v252, v56, vcc\n"
"v_mfma_f32_16x16x32_bf16 v[204:207], a[244:247], a[164:167], v[204:207]\n"
"ds_read_b64_tr_b16 a[244:245], v11 offset:45056\n"
"ds_read_b64_tr_b16 a[246:247], v11 offset:47104\n"
"v_mfma_f32_16x16x32_bf16 v[192:195], a[176:179], a[168:171], v[192:195]\n"
"v_cndmask_b32 v253, v253, v57, vcc\n"
"v_cndmask_b32 v254, v254, v58, vcc\n"
"v_mfma_f32_16x16x32_bf16 v[200:203], a[176:179], a[172:175], v[200:203]\n"
"buffer_store_dwordx4 v[48:51], v1, s[20:23], s68 offen sc1\n"
"v_mfma_f32_16x16x32_bf16 v[196:199], a[180:183], a[168:171], v[196:199]\n"
"v_cndmask_b32 v255, v255, v59, vcc\n"
"s_add_u32 s68, s28, 32768\n"
"v_mfma_f32_16x16x32_bf16 v[204:207], a[180:183], a[172:175], v[204:207]\n"
"v_exp_f32 v192, v192\n"
"v_mfma_f32_16x16x32_bf16 v[216:219], v[240:243], v[32:35], v[216:219]\n"
"v_exp_f32 v193, v193\n"
"v_mfma_f32_16x16x32_bf16 v[220:223], v[240:243], v[40:43], v[220:223]\n"
"v_exp_f32 v194, v194\n"
"v_mfma_f32_16x16x32_bf16 v[64:67], a[184:187], v[32:35], v[64:67]\n"
"buffer_store_dwordx4 v[56:59], v1, s[20:23], s69 offen sc1\n"
"v_mfma_f32_16x16x32_bf16 v[72:75], a[184:187], v[40:43], v[72:75]\n"
"v_cvt_pk_bf16_f32 v192, v192, v193\n"
"s_waitcnt lgkmcnt(2)\n"
"v_mfma_f32_16x16x32_bf16 v[68:71], a[188:191], v[32:35], v[68:71]\n"
"v_exp_f32 v195, v195\n"
"v_mfma_f32_16x16x32_bf16 v[76:79], a[188:191], v[40:43], v[76:79]\n"
"v_exp_f32 v200, v200\n"
"v_mfma_f32_16x16x32_bf16 v[80:83], a[192:195], v[32:35], v[80:83]\n"
"v_exp_f32 v201, v201\n"
"v_mfma_f32_16x16x32_bf16 v[88:91], a[192:195], v[40:43], v[88:91]\n"
"v_exp_f32 v196, v196\n"
"v_mfma_f32_16x16x32_bf16 v[84:87], a[196:199], v[32:35], v[84:87]\n"
"v_exp_f32 v197, v197\n"
"v_mfma_f32_16x16x32_bf16 v[92:95], a[196:199], v[40:43], v[92:95]\n"
"v_exp_f32 v198, v198\n"
"v_mfma_f32_16x16x32_bf16 v[96:99], a[200:203], v[32:35], v[96:99]\n"
"v_exp_f32 v199, v199\n"
"v_mfma_f32_16x16x32_bf16 v[104:107], a[200:203], v[40:43], v[104:107]\n"
"s_waitcnt vmcnt(6)\n"
"s_barrier\n"
"ds_read_b128 a[176:179], v12 offset:0\n"
"v_mfma_f32_16x16x32_bf16 v[100:103], a[204:207], v[32:35], v[100:103]\n"
"buffer_load_dwordx4 v20, s[40:43], s67 offen offset:0 lds\n"
"ds_read_b128 a[180:183], v12 offset:8192\n"
"v_mfma_f32_16x16x32_bf16 v[108:111], a[204:207], v[40:43], v[108:111]\n"
"ds_read_b128 a[184:187], v13 offset:0\n"
"v_cvt_pk_bf16_f32 v193, v194, v195\n"
"v_mfma_f32_16x16x32_bf16 v[112:115], a[208:211], v[32:35], v[112:115]\n"
"v_cvt_pk_bf16_f32 v200, v200, v201\n"
"v_cvt_pk_bf16_f32 v194, v196, v197\n"
"v_mfma_f32_16x16x32_bf16 v[120:123], a[208:211], v[40:43], v[120:123]\n"
"ds_read_b128 a[188:191], v13 offset:8192\n"
"v_cvt_pk_bf16_f32 v195, v198, v199\n"
"v_mfma_f32_16x16x32_bf16 v[116:119], a[212:215], v[32:35], v[116:119]\n"
"ds_read_b128 a[192:195], v14 offset:0\n"
"s_add_u32 s69, s57, 32768\n"
"v_mfma_f32_16x16x32_bf16 v[124:127], a[212:215], v[40:43], v[124:127]\n"
"buffer_load_dwordx4 v21, s[40:43], s67 offen offset:1024 lds\n"
"v_mfma_f32_16x16x32_bf16 v[128:131], a[216:219], v[32:35], v[128:131]\n"
"ds_read_b128 a[196:199], v14 offset:8192\n"
"v_mfma_f32_16x16x32_bf16 v[136:139], a[216:219], v[40:43], v[136:139]\n"
"ds_read_b128 a[200:203], v15 offset:0\n"
"v_mfma_f32_16x16x32_bf16 v[132:135], a[220:223], v[32:35], v[132:135]\n"
"ds_read_b128 a[204:207], v15 offset:8192\n"
"v_mfma_f32_16x16x32_bf16 v[140:143], a[220:223], v[40:43], v[140:143]\n"
"ds_read_b128 a[208:211], v12 offset:256\n"
"v_mfma_f32_16x16x32_bf16 v[144:147], a[224:227], v[32:35], v[144:147]\n"
"buffer_load_dwordx4 v22, s[40:43], s67 offen offset:2048 lds\n"
"v_mfma_f32_16x16x32_bf16 v[152:155], a[224:227], v[40:43], v[152:155]\n"
"ds_read_b128 a[212:215], v12 offset:8448\n"
"v_mfma_f32_16x16x32_bf16 v[148:151], a[228:231], v[32:35], v[148:151]\n"
"ds_read_b128 a[216:219], v13 offset:256\n"
"v_mfma_f32_16x16x32_bf16 v[156:159], a[228:231], v[40:43], v[156:159]\n"
"ds_read_b128 a[220:223], v13 offset:8448\n"
"v_mfma_f32_16x16x32_bf16 v[160:163], a[232:235], v[32:35], v[160:163]\n"
"ds_read_b128 a[224:227], v14 offset:256\n"
"v_mfma_f32_16x16x32_bf16 v[168:171], a[232:235], v[40:43], v[168:171]\n"
"buffer_load_dwordx4 v23, s[40:43], s67 offen offset:3072 lds\n"
"v_mfma_f32_16x16x32_bf16 v[164:167], a[236:239], v[32:35], v[164:167]\n"
"ds_read_b128 a[228:231], v14 offset:8448\n"
"s_add_u32 m0, s61, 147456\n"
"v_mfma_f32_16x16x32_bf16 v[172:175], a[236:239], v[40:43], v[172:175]\n"
"ds_read_b128 a[232:235], v15 offset:256\n"
"s_add_u32 s67, s67, 16384\n"
"v_mfma_f32_16x16x32_bf16 v[176:179], a[240:243], v[32:35], v[176:179]\n"
"ds_read_b128 a[236:239], v15 offset:8448\n"
"v_mfma_f32_16x16x32_bf16 v[184:187], a[240:243], v[40:43], v[184:187]\n"
"ds_read_b64_tr_b16 a[240:241], v8 offset:0\n"
"s_waitcnt lgkmcnt(8)\n"
"v_mfma_f32_16x16x32_bf16 v[180:183], a[244:247], v[32:35], v[180:183]\n"
"buffer_load_dwordx4 v24, s[36:39], s66 offen offset:0 lds\n"
"v_mfma_f32_16x16x32_bf16 v[188:191], a[244:247], v[40:43], v[188:191]\n"
"ds_read_b64_tr_b16 a[242:243], v8 offset:2048\n"
"ds_read_b64_tr_b16 a[244:245], v9 offset:0\n"
"v_mfma_f32_16x16x32_bf16 v[32:35], a[176:179], a[112:115], v[208:211]\n"
"ds_read_b64_tr_b16 a[246:247], v9 offset:2048\n"
"v_mfma_f32_16x16x32_bf16 v[40:43], a[176:179], a[116:119], v[212:215]\n"
"ds_read_b64_tr_b16 a[176:177], v10 offset:0\n"
"ds_read_b64_tr_b16 a[178:179], v10 offset:2048\n"
"v_mfma_f32_16x16x32_bf16 v[36:39], a[180:183], a[112:115], v[208:211]\n"
"v_exp_f32 v202, v202\n"
"v_mfma_f32_16x16x32_bf16 v[44:47], a[180:183], a[116:119], v[212:215]\n"
"buffer_load_dwordx4 v25, s[36:39], s66 offen offset:1024 lds\n"
"ds_read_b64_tr_b16 a[180:181], v11 offset:0\n"
"ds_read_b64_tr_b16 a[182:183], v11 offset:2048\n"
"v_mfma_f32_16x16x32_bf16 v[32:35], a[184:187], a[120:123], v[32:35]\n"
"v_exp_f32 v203, v203\n"
"v_mfma_f32_16x16x32_bf16 v[40:43], a[184:187], a[124:127], v[40:43]\n"
"ds_read_b64_tr_b16 a[186:187], v8 offset:6144\n"
"ds_read_b64_tr_b16 a[184:185], v8 offset:4096\n"
"v_mfma_f32_16x16x32_bf16 v[36:39], a[188:191], a[120:123], v[36:39]\n"
"v_cvt_pk_bf16_f32 v201, v202, v203\n"
"v_mfma_f32_16x16x32_bf16 v[44:47], a[188:191], a[124:127], v[44:47]\n"
"ds_read_b64_tr_b16 a[188:189], v9 offset:4096\n"
"ds_read_b64_tr_b16 a[190:191], v9 offset:6144\n"
"v_mfma_f32_16x16x32_bf16 v[32:35], a[192:195], a[128:131], v[32:35]\n"
"buffer_load_dwordx4 v26, s[36:39], s66 offen offset:2048 lds\n"
"v_mfma_f32_16x16x32_bf16 v[40:43], a[192:195], a[132:135], v[40:43]\n"
"ds_read_b64_tr_b16 a[192:193], v10 offset:4096\n"
"ds_read_b64_tr_b16 a[194:195], v10 offset:6144\n"
"v_mfma_f32_16x16x32_bf16 v[36:39], a[196:199], a[128:131], v[36:39]\n"
"v_exp_f32 v204, v204\n"
"v_mfma_f32_16x16x32_bf16 v[44:47], a[196:199], a[132:135], v[44:47]\n"
"ds_read_b64_tr_b16 a[196:197], v11 offset:4096\n"
"ds_read_b64_tr_b16 a[198:199], v11 offset:6144\n"
"v_mfma_f32_16x16x32_bf16 v[32:35], a[200:203], a[136:139], v[32:35]\n"
"v_exp_f32 v205, v205\n"
"v_mfma_f32_16x16x32_bf16 v[40:43], a[200:203], a[140:143], v[40:43]\n"
"buffer_load_dwordx4 v27, s[36:39], s66 offen offset:3072 lds\n"
"v_mfma_f32_16x16x32_bf16 v[36:39], a[204:207], a[136:139], v[36:39]\n"
"ds_read_b64_tr_b16 a[200:201], v8 offset:8192\n"
"ds_read_b64_tr_b16 a[202:203], v8 offset:10240\n"
"v_mfma_f32_16x16x32_bf16 v[44:47], a[204:207], a[140:143], v[44:47]\n"
"ds_read_b64_tr_b16 a[204:205], v9 offset:8192\n"
"ds_read_b64_tr_b16 a[206:207], v9 offset:10240\n"
"v_mfma_f32_16x16x32_bf16 v[32:35], a[208:211], a[144:147], v[32:35]\n"
"v_exp_f32 v206, v206\n"
"v_mfma_f32_16x16x32_bf16 v[40:43], a[208:211], a[148:151], v[40:43]\n"
"s_add_u32 m0, s61, 16384\n"
"s_waitcnt lgkmcnt(8)\n"
"v_mfma_f32_16x16x32_bf16 v[36:39], a[212:215], a[144:147], v[36:39]\n"
"buffer_load_dwordx4 v16, s[44:47], s64 offen offset:0 lds\n"
"v_mfma_f32_16x16x32_bf16 v[44:47], a[212:215], a[148:151], v[44:47]\n"
"ds_read_b64_tr_b16 a[208:209], v10 offset:8192\n"
"ds_read_b64_tr_b16 a[210:211], v10 offset:10240\n"
"v_mfma_f32_16x16x32_bf16 v[32:35], a[216:219], a[152:155], v[32:35]\n"
"v_exp_f32 v207, v207\n"
"v_mfma_f32_16x16x32_bf16 v[40:43], a[216:219], a[156:159], v[40:43]\n"
"v_cvt_pk_bf16_f32 v202, v204, v205\n"
"v_cvt_pk_bf16_f32 v203, v206, v207\n"
"v_mfma_f32_16x16x32_bf16 v[36:39], a[220:223], a[152:155], v[36:39]\n"
"ds_read_b64_tr_b16 a[212:213], v11 offset:8192\n"
"ds_read_b64_tr_b16 a[214:215], v11 offset:10240\n"
"v_mfma_f32_16x16x32_bf16 v[44:47], a[220:223], a[156:159], v[44:47]\n"
"buffer_load_dwordx4 v17, s[44:47], s64 offen offset:1024 lds\n"
"v_mfma_f32_16x16x32_bf16 v[32:35], a[224:227], a[160:163], v[32:35]\n"
"ds_read_b64_tr_b16 a[216:217], v8 offset:12288\n"
"ds_read_b64_tr_b16 a[218:219], v8 offset:14336\n"
"v_mfma_f32_16x16x32_bf16 v[40:43], a[224:227], a[164:167], v[40:43]\n"
"ds_read_b64_tr_b16 a[220:221], v9 offset:12288\n"
"ds_read_b64_tr_b16 a[222:223], v9 offset:14336\n"
"v_mfma_f32_16x16x32_bf16 v[36:39], a[228:231], a[160:163], v[36:39]\n"
"ds_read_b64_tr_b16 a[224:225], v10 offset:12288\n"
"ds_read_b64_tr_b16 a[226:227], v10 offset:14336\n"
"v_mfma_f32_16x16x32_bf16 v[44:47], a[228:231], a[164:167], v[44:47]\n"
"ds_read_b64_tr_b16 a[228:229], v11 offset:12288\n"
"ds_read_b64_tr_b16 a[230:231], v11 offset:14336\n"
"v_mfma_f32_16x16x32_bf16 v[32:35], a[232:235], a[168:171], v[32:35]\n"
"buffer_load_dwordx4 v18, s[44:47], s64 offen offset:2048 lds\n"
"v_mfma_f32_16x16x32_bf16 v[40:43], a[232:235], a[172:175], v[40:43]\n"
"ds_read_b64_tr_b16 a[232:233], v30 offset:49152\n"
"ds_read_b64_tr_b16 a[234:235], v30 offset:51200\n"
"v_mfma_f32_16x16x32_bf16 v[36:39], a[236:239], a[168:171], v[36:39]\n"
"s_add_u32 s66, s66, 16384\n"
"v_mfma_f32_16x16x32_bf16 v[44:47], a[236:239], a[172:175], v[44:47]\n"
"ds_read_b64_tr_b16 a[236:237], v30 offset:53248\n"
"ds_read_b64_tr_b16 a[238:239], v30 offset:55296\n"
"v_mfma_f32_16x16x32_bf16 v[216:219], v[240:243], v[192:195], v[216:219]\n"
"v_exp_f32 v32, v32\n"
"v_mfma_f32_16x16x32_bf16 v[220:223], v[240:243], v[200:203], v[220:223]\n"
"buffer_load_dwordx4 v19, s[44:47], s64 offen offset:3072 lds\n"
"v_mfma_f32_16x16x32_bf16 v[64:67], a[240:243], v[192:195], v[64:67]\n"
"s_add_u32 s64, s64, 114688\n"
"s_add_u32 m0, s61, 32768\n"
"v_mfma_f32_16x16x32_bf16 v[72:75], a[240:243], v[200:203], v[72:75]\n"
"ds_read_b64_tr_b16 a[240:241], v30 offset:57344\n"
"ds_read_b64_tr_b16 a[242:243], v30 offset:59392\n"
"v_mfma_f32_16x16x32_bf16 v[68:71], a[244:247], v[192:195], v[68:71]\n"
"s_cmp_ge_u32 s64, s77\n"
"s_cselect_b32 s84, 200576, 0\n"
"v_mfma_f32_16x16x32_bf16 v[76:79], a[244:247], v[200:203], v[76:79]\n"
"ds_read_b64_tr_b16 a[244:245], v30 offset:61440\n"
"ds_read_b64_tr_b16 a[246:247], v30 offset:63488\n"
"v_mfma_f32_16x16x32_bf16 v[80:83], a[176:179], v[192:195], v[80:83]\n"
"s_sub_u32 s64, s64, s84\n"
"v_mfma_f32_16x16x32_bf16 v[88:91], a[176:179], v[200:203], v[88:91]\n"
"buffer_load_dwordx4 v16, s[44:47], s64 offen offset:0 lds\n"
"v_mfma_f32_16x16x32_bf16 v[84:87], a[180:183], v[192:195], v[84:87]\n"
"ds_read_b64_tr_b16 a[176:177], v30 offset:0\n"
"ds_read_b64_tr_b16 a[178:179], v30 offset:2048\n"
"v_mfma_f32_16x16x32_bf16 v[92:95], a[180:183], v[200:203], v[92:95]\n"
"ds_read_b64_tr_b16 a[180:181], v30 offset:4096\n"
"ds_read_b64_tr_b16 a[182:183], v30 offset:6144\n"
"v_mfma_f32_16x16x32_bf16 v[96:99], a[184:187], v[192:195], v[96:99]\n"
"v_exp_f32 v33, v33\n"
"v_mfma_f32_16x16x32_bf16 v[104:107], a[184:187], v[200:203], v[104:107]\n"
"ds_read_b64_tr_b16 a[184:185], v30 offset:8192\n"
"ds_read_b64_tr_b16 a[186:187], v30 offset:10240\n"
"v_mfma_f32_16x16x32_bf16 v[100:103], a[188:191], v[192:195], v[100:103]\n"
"buffer_load_dwordx4 v17, s[44:47], s64 offen offset:1024 lds\n"
"v_mfma_f32_16x16x32_bf16 v[108:111], a[188:191], v[200:203], v[108:111]\n"
"v_cvt_pk_bf16_f32 v32, v32, v33\n"
"s_waitcnt lgkmcnt(8)\n"
"v_mfma_f32_16x16x32_bf16 v[112:115], a[192:195], v[192:195], v[112:115]\n"
"v_exp_f32 v34, v34\n"
"v_mfma_f32_16x16x32_bf16 v[120:123], a[192:195], v[200:203], v[120:123]\n"
"v_exp_f32 v35, v35\n"
"v_mfma_f32_16x16x32_bf16 v[116:119], a[196:199], v[192:195], v[116:119]\n"
"v_exp_f32 v40, v40\n"
"v_mfma_f32_16x16x32_bf16 v[124:127], a[196:199], v[200:203], v[124:127]\n"
"buffer_load_dwordx4 v18, s[44:47], s64 offen offset:2048 lds\n"
"v_mfma_f32_16x16x32_bf16 v[128:131], a[200:203], v[192:195], v[128:131]\n"
"v_exp_f32 v41, v41\n"
"v_mfma_f32_16x16x32_bf16 v[136:139], a[200:203], v[200:203], v[136:139]\n"
"v_cvt_pk_bf16_f32 v33, v34, v35\n"
"v_cvt_pk_bf16_f32 v40, v40, v41\n"
"v_mfma_f32_16x16x32_bf16 v[132:135], a[204:207], v[192:195], v[132:135]\n"
"v_exp_f32 v36, v36\n"
"v_mfma_f32_16x16x32_bf16 v[140:143], a[204:207], v[200:203], v[140:143]\n"
"v_exp_f32 v37, v37\n"
"v_mfma_f32_16x16x32_bf16 v[144:147], a[208:211], v[192:195], v[144:147]\n"
"buffer_load_dwordx4 v19, s[44:47], s64 offen offset:3072 lds\n"
"v_mfma_f32_16x16x32_bf16 v[152:155], a[208:211], v[200:203], v[152:155]\n"
"s_add_u32 m0, s61, 98304\n"
"v_cvt_pk_bf16_f32 v34, v36, v37\n"
"v_mfma_f32_16x16x32_bf16 v[148:151], a[212:215], v[192:195], v[148:151]\n"
"v_exp_f32 v38, v38\n"
"v_mfma_f32_16x16x32_bf16 v[156:159], a[212:215], v[200:203], v[156:159]\n"
"v_exp_f32 v39, v39\n"
"v_mfma_f32_16x16x32_bf16 v[160:163], a[216:219], v[192:195], v[160:163]\n"
"v_exp_f32 v42, v42\n"
"v_mfma_f32_16x16x32_bf16 v[168:171], a[216:219], v[200:203], v[168:171]\n"
"v_exp_f32 v43, v43\n"
"v_mfma_f32_16x16x32_bf16 v[164:167], a[220:223], v[192:195], v[164:167]\n"
"v_cvt_pk_bf16_f32 v35, v38, v39\n"
"v_cvt_pk_bf16_f32 v41, v42, v43\n"
"v_mfma_f32_16x16x32_bf16 v[172:175], a[220:223], v[200:203], v[172:175]\n"
"v_exp_f32 v44, v44\n"
"v_mfma_f32_16x16x32_bf16 v[176:179], a[224:227], v[192:195], v[176:179]\n"
"v_exp_f32 v45, v45\n"
"v_mfma_f32_16x16x32_bf16 v[184:187], a[224:227], v[200:203], v[184:187]\n"
"v_exp_f32 v46, v46\n"
"v_mfma_f32_16x16x32_bf16 v[180:183], a[228:231], v[192:195], v[180:183]\n"
"v_exp_f32 v47, v47\n"
"v_mfma_f32_16x16x32_bf16 v[188:191], a[228:231], v[200:203], v[188:191]\n"
"v_cvt_pk_bf16_f32 v42, v44, v45\n"
"v_cvt_pk_bf16_f32 v43, v46, v47\n"
"v_mfma_f32_16x16x32_bf16 v[48:51], a[232:235], a[0:3], 0\n"
"s_cmp_ge_u32 s28, 0x40000000\n"
"s_cselect_b64 vcc, -1, 0\n"
"v_mfma_f32_16x16x32_bf16 v[52:55], a[232:235], a[4:7], 0\n"
"s_add_u32 s64, s64, 114688\n"
"v_mfma_f32_16x16x32_bf16 v[56:59], a[232:235], a[8:11], 0\n"
"s_cmp_ge_u32 s64, s77\n"
"s_cselect_b32 s84, 200576, 0\n"
"v_mfma_f32_16x16x32_bf16 v[60:63], a[232:235], a[12:15], 0\n"
"s_sub_u32 s64, s64, s84\n"
"v_mfma_f32_16x16x32_bf16 v[48:51], a[236:239], a[16:19], v[48:51]\n"
"v_mfma_f32_16x16x32_bf16 v[52:55], a[236:239], a[20:23], v[52:55]\n"
"s_waitcnt vmcnt(12)\n"
"s_barrier\n"
"ds_read_b128 a[188:191], v12 offset:16384\n"
"v_mfma_f32_16x16x32_bf16 v[56:59], a[236:239], a[24:27], v[56:59]\n"
"buffer_load_dwordx4 v20, s[40:43], s67 offen offset:0 lds\n"
"ds_read_b128 a[192:195], v12 offset:24576\n"
"v_mfma_f32_16x16x32_bf16 v[60:63], a[236:239], a[28:31], v[60:63]\n"
"ds_read_b128 a[196:199], v13 offset:16384\n"
"v_mfma_f32_16x16x32_bf16 v[48:51], a[240:243], a[32:35], v[48:51]\n"
"ds_read_b128 a[200:203], v13 offset:24576\n"
"v_mfma_f32_16x16x32_bf16 v[52:55], a[240:243], a[36:39], v[52:55]\n"
"ds_read_b128 a[204:207], v14 offset:16384\n"
"v_mfma_f32_16x16x32_bf16 v[56:59], a[240:243], a[40:43], v[56:59]\n"
"ds_read_b128 a[208:211], v14 offset:24576\n"
"v_mfma_f32_16x16x32_bf16 v[60:63], a[240:243], a[44:47], v[60:63]\n"
"ds_read_b128 a[212:215], v15 offset:16384\n"
"s_waitcnt lgkmcnt(7)\n"
"v_mfma_f32_16x16x32_bf16 v[48:51], a[244:247], a[48:51], v[48:51]\n"
"buffer_load_dwordx4 v21, s[40:43], s67 offen offset:1024 lds\n"
"v_mfma_f32_16x16x32_bf16 v[52:55], a[244:247], a[52:55], v[52:55]\n"
"ds_read_b128 a[216:219], v15 offset:24576\n"
"v_mfma_f32_16x16x32_bf16 v[56:59], a[244:247], a[56:59], v[56:59]\n"
"ds_read_b128 a[220:223], v12 offset:16640\n"
"v_mfma_f32_16x16x32_bf16 v[60:63], a[244:247], a[60:63], v[60:63]\n"
"ds_read_b128 a[224:227], v12 offset:24832\n"
"v_mfma_f32_16x16x32_bf16 v[48:51], a[176:179], a[64:67], v[48:51]\n"
"ds_read_b128 a[228:231], v13 offset:16640\n"
"v_mfma_f32_16x16x32_bf16 v[52:55], a[176:179], a[68:71], v[52:55]\n"
"buffer_load_dwordx4 v22, s[40:43], s67 offen offset:2048 lds\n"
"v_mfma_f32_16x16x32_bf16 v[56:59], a[176:179], a[72:75], v[56:59]\n"
"ds_read_b128 a[232:235], v13 offset:24832\n"
"v_mfma_f32_16x16x32_bf16 v[60:63], a[176:179], a[76:79], v[60:63]\n"
"ds_read_b128 a[236:239], v14 offset:16640\n"
"v_mfma_f32_16x16x32_bf16 v[48:51], a[180:183], a[80:83], v[48:51]\n"
"ds_read_b128 a[240:243], v14 offset:24832\n"
"v_mfma_f32_16x16x32_bf16 v[52:55], a[180:183], a[84:87], v[52:55]\n"
"ds_read_b128 a[244:247], v15 offset:16640\n"
"v_mfma_f32_16x16x32_bf16 v[56:59], a[180:183], a[88:91], v[56:59]\n"
"buffer_load_dwordx4 v23, s[40:43], s67 offen offset:3072 lds\n"
"v_mfma_f32_16x16x32_bf16 v[60:63], a[180:183], a[92:95], v[60:63]\n"
"ds_read_b128 a[176:179], v15 offset:24832\n"
"s_add_u32 m0, s61, 114688\n"
"v_mfma_f32_16x16x32_bf16 v[48:51], a[184:187], a[96:99], v[48:51]\n"
"ds_read_b64_tr_b16 a[180:181], v8 offset:16384\n"
"ds_read_b64_tr_b16 a[182:183], v8 offset:18432\n"
"v_mfma_f32_16x16x32_bf16 v[52:55], a[184:187], a[100:103], v[52:55]\n"
"s_add_u32 s67, s67, 16384\n"
"v_mfma_f32_16x16x32_bf16 v[56:59], a[184:187], a[104:107], v[56:59]\n"
"v_mfma_f32_16x16x32_bf16 v[60:63], a[184:187], a[108:111], v[60:63]\n"
"ds_read_b64_tr_b16 a[184:185], v9 offset:16384\n"
"s_waitcnt lgkmcnt(8)\n"
"v_mfma_f32_16x16x32_bf16 v[192:195], a[188:191], a[112:115], v[208:211]\n"
"buffer_load_dwordx4 v24, s[36:39], s66 offen offset:0 lds\n"
"v_mfma_f32_16x16x32_bf16 v[200:203], a[188:191], a[116:119], v[212:215]\n"
"ds_read_b64_tr_b16 a[186:187], v9 offset:18432\n"
"ds_read_b64_tr_b16 a[188:189], v10 offset:16384\n"
"v_mfma_f32_16x16x32_bf16 v[196:199], a[192:195], a[112:115], v[208:211]\n"
"ds_read_b64_tr_b16 a[190:191], v10 offset:18432\n"
"v_cvt_pk_bf16_f32 v48, v48, v49\n"
"v_mfma_f32_16x16x32_bf16 v[204:207], a[192:195], a[116:119], v[212:215]\n"
"ds_read_b64_tr_b16 a[192:193], v11 offset:16384\n"
"ds_read_b64_tr_b16 a[194:195], v11 offset:18432\n"
"v_mfma_f32_16x16x32_bf16 v[192:195], a[196:199], a[120:123], v[192:195]\n"
"v_cvt_pk_bf16_f32 v49, v50, v51\n"
"v_cvt_pk_bf16_f32 v50, v52, v53\n"
"v_mfma_f32_16x16x32_bf16 v[200:203], a[196:199], a[124:127], v[200:203]\n"
"buffer_load_dwordx4 v25, s[36:39], s66 offen offset:1024 lds\n"
"v_mfma_f32_16x16x32_bf16 v[196:199], a[200:203], a[120:123], v[196:199]\n"
"ds_read_b64_tr_b16 a[196:197], v8 offset:20480\n"
"ds_read_b64_tr_b16 a[198:199], v8 offset:22528\n"
"v_mfma_f32_16x16x32_bf16 v[204:207], a[200:203], a[124:127], v[204:207]\n"
"ds_read_b64_tr_b16 a[200:201], v9 offset:20480\n"
"ds_read_b64_tr_b16 a[202:203], v9 offset:22528\n"
"v_mfma_f32_16x16x32_bf16 v[192:195], a[204:207], a[128:131], v[192:195]\n"
"v_cvt_pk_bf16_f32 v51, v54, v55\n"
"v_cvt_pk_bf16_f32 v56, v56, v57\n"
"v_mfma_f32_16x16x32_bf16 v[200:203], a[204:207], a[132:135], v[200:203]\n"
"ds_read_b64_tr_b16 a[204:205], v10 offset:20480\n"
"ds_read_b64_tr_b16 a[206:207], v10 offset:22528\n"
"v_mfma_f32_16x16x32_bf16 v[196:199], a[208:211], a[128:131], v[196:199]\n"
"buffer_load_dwordx4 v26, s[36:39], s66 offen offset:2048 lds\n"
"v_mfma_f32_16x16x32_bf16 v[204:207], a[208:211], a[132:135], v[204:207]\n"
"ds_read_b64_tr_b16 a[208:209], v11 offset:20480\n"
"ds_read_b64_tr_b16 a[210:211], v11 offset:22528\n"
"v_mfma_f32_16x16x32_bf16 v[192:195], a[212:215], a[136:139], v[192:195]\n"
"v_cvt_pk_bf16_f32 v57, v58, v59\n"
"v_cvt_pk_bf16_f32 v58, v60, v61\n"
"v_mfma_f32_16x16x32_bf16 v[200:203], a[212:215], a[140:143], v[200:203]\n"
"ds_read_b64_tr_b16 a[212:213], v8 offset:24576\n"
"ds_read_b64_tr_b16 a[214:215], v8 offset:26624\n"
"v_mfma_f32_16x16x32_bf16 v[196:199], a[216:219], a[136:139], v[196:199]\n"
"v_cvt_pk_bf16_f32 v59, v62, v63\n"
"v_cndmask_b32 v228, v228, v48, vcc\n"
"v_mfma_f32_16x16x32_bf16 v[204:207], a[216:219], a[140:143], v[204:207]\n"
"buffer_load_dwordx4 v27, s[36:39], s66 offen offset:3072 lds\n"
"v_mfma_f32_16x16x32_bf16 v[192:195], a[220:223], a[144:147], v[192:195]\n"
"ds_read_b64_tr_b16 a[216:217], v9 offset:24576\n"
"ds_read_b64_tr_b16 a[218:219], v9 offset:26624\n"
"v_mfma_f32_16x16x32_bf16 v[200:203], a[220:223], a[148:151], v[200:203]\n"
"ds_read_b64_tr_b16 a[220:221], v10 offset:24576\n"
"ds_read_b64_tr_b16 a[222:223], v10 offset:26624\n"
"v_mfma_f32_16x16x32_bf16 v[196:199], a[224:227], a[144:147], v[196:199]\n"
"s_add_u32 m0, s61, 65536\n"
"v_cndmask_b32 v229, v229, v49, vcc\n"
"v_mfma_f32_16x16x32_bf16 v[204:207], a[224:227], a[148:151], v[204:207]\n"
"ds_read_b64_tr_b16 a[224:225], v11 offset:24576\n"
"ds_read_b64_tr_b16 a[226:227], v11 offset:26624\n"
"v_mfma_f32_16x16x32_bf16 v[192:195], a[228:231], a[152:155], v[192:195]\n"
"v_cndmask_b32 v230, v230, v50, vcc\n"
"v_cndmask_b32 v231, v231, v51, vcc\n"
"v_mfma_f32_16x16x32_bf16 v[200:203], a[228:231], a[156:159], v[200:203]\n"
"ds_read_b64_tr_b16 a[228:229], v8 offset:28672\n"
"s_waitcnt lgkmcnt(9)\n"
"v_mfma_f32_16x16x32_bf16 v[196:199], a[232:235], a[152:155], v[196:199]\n"
"ds_read_b64_tr_b16 a[230:231], v8 offset:30720\n"
"s_add_u32 s66, s66, 16384\n"
"v_mfma_f32_16x16x32_bf16 v[204:207], a[232:235], a[156:159], v[204:207]\n"
"ds_read_b64_tr_b16 a[232:233], v9 offset:28672\n"
"ds_read_b64_tr_b16 a[234:235], v9 offset:30720\n"
"v_mfma_f32_16x16x32_bf16 v[192:195], a[236:239], a[160:163], v[192:195]\n"
"s_cmp_ge_u32 s57, 0x40000000\n"
"s_cselect_b64 vcc, -1, 0\n"
"v_mfma_f32_16x16x32_bf16 v[200:203], a[236:239], a[164:167], v[200:203]\n"
"ds_read_b64_tr_b16 a[236:237], v10 offset:28672\n"
"ds_read_b64_tr_b16 a[238:239], v10 offset:30720\n"
"v_mfma_f32_16x16x32_bf16 v[196:199], a[240:243], a[160:163], v[196:199]\n"
"v_cndmask_b32 v228, v228, v56, vcc\n"
"v_cndmask_b32 v229, v229, v57, vcc\n"
"v_mfma_f32_16x16x32_bf16 v[204:207], a[240:243], a[164:167], v[204:207]\n"
"ds_read_b64_tr_b16 a[240:241], v11 offset:28672\n"
"ds_read_b64_tr_b16 a[242:243], v11 offset:30720\n"
"v_mfma_f32_16x16x32_bf16 v[192:195], a[244:247], a[168:171], v[192:195]\n"
"v_cndmask_b32 v230, v230, v58, vcc\n"
"v_cndmask_b32 v231, v231, v59, vcc\n"
"v_mfma_f32_16x16x32_bf16 v[200:203], a[244:247], a[172:175], v[200:203]\n"
"buffer_store_dwordx4 v[48:51], v1, s[20:23], s68 offen sc1\n"
"v_mfma_f32_16x16x32_bf16 v[196:199], a[176:179], a[168:171], v[196:199]\n"
"v_cmp_gt_u32 vcc, 28, v7\n"
"s_add_u32 s68, s29, 40960\n"
"v_mfma_f32_16x16x32_bf16 v[204:207], a[176:179], a[172:175], v[204:207]\n"
"v_exp_f32 v192, v192\n"
"v_mfma_f32_16x16x32_bf16 v[216:219], v[240:243], v[32:35], v[216:219]\n"
"v_exp_f32 v193, v193\n"
"v_mfma_f32_16x16x32_bf16 v[220:223], v[240:243], v[40:43], v[220:223]\n"
"v_exp_f32 v194, v194\n"
"v_mfma_f32_16x16x32_bf16 v[64:67], a[180:183], v[32:35], v[64:67]\n"
"buffer_store_dwordx4 v[56:59], v1, s[20:23], s69 offen sc1\n"
"v_mfma_f32_16x16x32_bf16 v[72:75], a[180:183], v[40:43], v[72:75]\n"
"v_exp_f32 v195, v195\n"
"v_mfma_f32_16x16x32_bf16 v[68:71], a[184:187], v[32:35], v[68:71]\n"
"v_exp_f32 v200, v200\n"
"v_mfma_f32_16x16x32_bf16 v[76:79], a[184:187], v[40:43], v[76:79]\n"
"v_exp_f32 v201, v201\n"
"v_mfma_f32_16x16x32_bf16 v[80:83], a[188:191], v[32:35], v[80:83]\n"
"v_cvt_pk_bf16_f32 v192, v192, v193\n"
"v_cvt_pk_bf16_f32 v193, v194, v195\n"
"v_mfma_f32_16x16x32_bf16 v[88:91], a[188:191], v[40:43], v[88:91]\n"
"v_exp_f32 v196, v196\n"
"v_mfma_f32_16x16x32_bf16 v[84:87], a[192:195], v[32:35], v[84:87]\n"
"v_exp_f32 v197, v197\n"
"v_mfma_f32_16x16x32_bf16 v[92:95], a[192:195], v[40:43], v[92:95]\n"
"v_exp_f32 v198, v198\n"
"v_mfma_f32_16x16x32_bf16 v[96:99], a[196:199], v[32:35], v[96:99]\n"
"v_exp_f32 v199, v199\n"
"v_mfma_f32_16x16x32_bf16 v[104:107], a[196:199], v[40:43], v[104:107]\n"
"s_waitcnt vmcnt(6)\n"
"s_barrier\n"
"ds_read_b128 a[244:247], v12 offset:32768\n"
"v_mfma_f32_16x16x32_bf16 v[100:103], a[200:203], v[32:35], v[100:103]\n"
"buffer_load_dwordx4 v20, s[40:43], s67 offen offset:0 lds\n"
"ds_read_b128 a[176:179], v12 offset:40960\n"
"v_mfma_f32_16x16x32_bf16 v[108:111], a[200:203], v[40:43], v[108:111]\n"
"ds_read_b128 a[180:183], v13 offset:32768\n"
"v_cvt_pk_bf16_f32 v200, v200, v201\n"
"v_mfma_f32_16x16x32_bf16 v[112:115], a[204:207], v[32:35], v[112:115]\n"
"v_exp_f32 v202, v202\n"
"v_mfma_f32_16x16x32_bf16 v[120:123], a[204:207], v[40:43], v[120:123]\n"
"ds_read_b128 a[184:187], v13 offset:40960\n"
"v_cvt_pk_bf16_f32 v194, v196, v197\n"
"v_mfma_f32_16x16x32_bf16 v[116:119], a[208:211], v[32:35], v[116:119]\n"
"ds_read_b128 a[188:191], v14 offset:32768\n"
"v_cvt_pk_bf16_f32 v195, v198, v199\n"
"v_mfma_f32_16x16x32_bf16 v[124:127], a[208:211], v[40:43], v[124:127]\n"
"ds_read_b128 a[192:195], v14 offset:40960\n"
"s_waitcnt lgkmcnt(6)\n"
"v_mfma_f32_16x16x32_bf16 v[128:131], a[212:215], v[32:35], v[128:131]\n"
"buffer_load_dwordx4 v21, s[40:43], s67 offen offset:1024 lds\n"
"v_mfma_f32_16x16x32_bf16 v[136:139], a[212:215], v[40:43], v[136:139]\n"
"ds_read_b128 a[196:199], v15 offset:32768\n"
"v_cndmask_b32 v0, v28, v29, vcc\n"
"v_mfma_f32_16x16x32_bf16 v[132:135], a[216:219], v[32:35], v[132:135]\n"
"ds_read_b128 a[200:203], v15 offset:40960\n"
"s_add_u32 s69, s58, 40960\n"
"v_mfma_f32_16x16x32_bf16 v[140:143], a[216:219], v[40:43], v[140:143]\n"
"ds_read_b128 a[204:207], v12 offset:33024\n"
"v_mfma_f32_16x16x32_bf16 v[144:147], a[220:223], v[32:35], v[144:147]\n"
"ds_read_b128 a[208:211], v12 offset:41216\n"
"v_mfma_f32_16x16x32_bf16 v[152:155], a[220:223], v[40:43], v[152:155]\n"
"buffer_load_dwordx4 v22, s[40:43], s67 offen offset:2048 lds\n"
"v_mfma_f32_16x16x32_bf16 v[148:151], a[224:227], v[32:35], v[148:151]\n"
"ds_read_b128 a[212:215], v13 offset:33024\n"
"v_mfma_f32_16x16x32_bf16 v[156:159], a[224:227], v[40:43], v[156:159]\n"
"ds_read_b128 a[216:219], v13 offset:41216\n"
"v_mfma_f32_16x16x32_bf16 v[160:163], a[228:231], v[32:35], v[160:163]\n"
"ds_read_b128 a[220:223], v14 offset:33024\n"
"v_mfma_f32_16x16x32_bf16 v[168:171], a[228:231], v[40:43], v[168:171]\n"
"ds_read_b128 a[224:227], v14 offset:41216\n"
"v_mfma_f32_16x16x32_bf16 v[164:167], a[232:235], v[32:35], v[164:167]\n"
"buffer_load_dwordx4 v23, s[40:43], s67 offen offset:3072 lds\n"
"v_mfma_f32_16x16x32_bf16 v[172:175], a[232:235], v[40:43], v[172:175]\n"
"ds_read_b128 a[228:231], v15 offset:33024\n"
"s_add_u32 m0, s61, 131072\n"
"v_mfma_f32_16x16x32_bf16 v[176:179], a[236:239], v[32:35], v[176:179]\n"
"ds_read_b128 a[232:235], v15 offset:41216\n"
"s_add_u32 s67, s67, 16384\n"
"v_mfma_f32_16x16x32_bf16 v[184:187], a[236:239], v[40:43], v[184:187]\n"
"ds_read_b64_tr_b16 a[236:237], v8 offset:32768\n"
"ds_read_b64_tr_b16 a[238:239], v8 offset:34816\n"
"v_mfma_f32_16x16x32_bf16 v[180:183], a[240:243], v[32:35], v[180:183]\n"
"v_exp_f32 v203, v203\n"
"v_mfma_f32_16x16x32_bf16 v[188:191], a[240:243], v[40:43], v[188:191]\n"
"ds_read_b64_tr_b16 a[240:241], v9 offset:32768\n"
"s_waitcnt lgkmcnt(9)\n"
"v_mfma_f32_16x16x32_bf16 v[32:35], a[244:247], a[112:115], v[208:211]\n"
"buffer_load_dwordx4 v24, s[36:39], s66 offen offset:0 lds\n"
"v_mfma_f32_16x16x32_bf16 v[40:43], a[244:247], a[116:119], v[212:215]\n"
"ds_read_b64_tr_b16 a[242:243], v9 offset:34816\n"
"ds_read_b64_tr_b16 a[244:245], v10 offset:32768\n"
"v_mfma_f32_16x16x32_bf16 v[36:39], a[176:179], a[112:115], v[208:211]\n"
"ds_read_b64_tr_b16 a[246:247], v10 offset:34816\n"
"v_cvt_pk_bf16_f32 v201, v202, v203\n"
"v_mfma_f32_16x16x32_bf16 v[44:47], a[176:179], a[116:119], v[212:215]\n"
"ds_read_b64_tr_b16 a[176:177], v11 offset:32768\n"
"ds_read_b64_tr_b16 a[178:179], v11 offset:34816\n"
"v_mfma_f32_16x16x32_bf16 v[32:35], a[180:183], a[120:123], v[32:35]\n"
"v_exp_f32 v204, v204\n"
"v_mfma_f32_16x16x32_bf16 v[40:43], a[180:183], a[124:127], v[40:43]\n"
"buffer_load_dwordx4 v25, s[36:39], s66 offen offset:1024 lds\n"
"v_mfma_f32_16x16x32_bf16 v[36:39], a[184:187], a[120:123], v[36:39]\n"
"ds_read_b64_tr_b16 a[182:183], v8 offset:38912\n"
"ds_read_b64_tr_b16 a[180:181], v8 offset:36864\n"
"v_mfma_f32_16x16x32_bf16 v[44:47], a[184:187], a[124:127], v[44:47]\n"
"ds_read_b64_tr_b16 a[184:185], v9 offset:36864\n"
"ds_read_b64_tr_b16 a[186:187], v9 offset:38912\n"
"v_mfma_f32_16x16x32_bf16 v[32:35], a[188:191], a[128:131], v[32:35]\n"
"v_exp_f32 v205, v205\n"
"v_mfma_f32_16x16x32_bf16 v[40:43], a[188:191], a[132:135], v[40:43]\n"
"ds_read_b64_tr_b16 a[188:189], v10 offset:36864\n"
"ds_read_b64_tr_b16 a[190:191], v10 offset:38912\n"
"v_mfma_f32_16x16x32_bf16 v[36:39], a[192:195], a[128:131], v[36:39]\n"
"buffer_load_dwordx4 v26, s[36:39], s66 offen offset:2048 lds\n"
"v_mfma_f32_16x16x32_bf16 v[44:47], a[192:195], a[132:135], v[44:47]\n"
"ds_read_b64_tr_b16 a[192:193], v11 offset:36864\n"
"ds_read_b64_tr_b16 a[194:195], v11 offset:38912\n"
"v_mfma_f32_16x16x32_bf16 v[32:35], a[196:199], a[136:139], v[32:35]\n"
"v_exp_f32 v206, v206\n"
"v_mfma_f32_16x16x32_bf16 v[40:43], a[196:199], a[140:143], v[40:43]\n"
"ds_read_b64_tr_b16 a[196:197], v8 offset:40960\n"
"ds_read_b64_tr_b16 a[198:199], v8 offset:43008\n"
"v_mfma_f32_16x16x32_bf16 v[36:39], a[200:203], a[136:139], v[36:39]\n"
"v_exp_f32 v207, v207\n"
"v_mfma_f32_16x16x32_bf16 v[44:47], a[200:203], a[140:143], v[44:47]\n"
"buffer_load_dwordx4 v27, s[36:39], s66 offen offset:3072 lds\n"
"v_mfma_f32_16x16x32_bf16 v[32:35], a[204:207], a[144:147], v[32:35]\n"
"ds_read_b64_tr_b16 a[200:201], v9 offset:40960\n"
"ds_read_b64_tr_b16 a[202:203], v9 offset:43008\n"
"v_mfma_f32_16x16x32_bf16 v[40:43], a[204:207], a[148:151], v[40:43]\n"
"ds_read_b64_tr_b16 a[204:205], v10 offset:40960\n"
"ds_read_b64_tr_b16 a[206:207], v10 offset:43008\n"
"v_mfma_f32_16x16x32_bf16 v[36:39], a[208:211], a[144:147], v[36:39]\n"
"v_cvt_pk_bf16_f32 v202, v204, v205\n"
"v_cvt_pk_bf16_f32 v203, v206, v207\n"
"v_mfma_f32_16x16x32_bf16 v[44:47], a[208:211], a[148:151], v[44:47]\n"
"s_add_u32 m0, s61, 49152\n"
"s_waitcnt lgkmcnt(8)\n"
"v_mfma_f32_16x16x32_bf16 v[32:35], a[212:215], a[152:155], v[32:35]\n"
"buffer_load_dwordx4 v16, s[44:47], s64 offen offset:0 lds\n"
"v_mfma_f32_16x16x32_bf16 v[40:43], a[212:215], a[156:159], v[40:43]\n"
"ds_read_b64_tr_b16 a[208:209], v11 offset:40960\n"
"ds_read_b64_tr_b16 a[210:211], v11 offset:43008\n"
"v_mfma_f32_16x16x32_bf16 v[36:39], a[216:219], a[152:155], v[36:39]\n"
"ds_read_b64_tr_b16 a[212:213], v8 offset:45056\n"
"ds_read_b64_tr_b16 a[214:215], v8 offset:47104\n"
"v_mfma_f32_16x16x32_bf16 v[44:47], a[216:219], a[156:159], v[44:47]\n"
"ds_read_b64_tr_b16 a[216:217], v9 offset:45056\n"
"ds_read_b64_tr_b16 a[218:219], v9 offset:47104\n"
"v_mfma_f32_16x16x32_bf16 v[32:35], a[220:223], a[160:163], v[32:35]\n"
"s_add_u32 s66, s66, 16384\n"
"v_mfma_f32_16x16x32_bf16 v[40:43], a[220:223], a[164:167], v[40:43]\n"
"buffer_load_dwordx4 v17, s[44:47], s64 offen offset:1024 lds\n"
"v_mfma_f32_16x16x32_bf16 v[36:39], a[224:227], a[160:163], v[36:39]\n"
"ds_read_b64_tr_b16 a[220:221], v10 offset:45056\n"
"ds_read_b64_tr_b16 a[222:223], v10 offset:47104\n"
"v_mfma_f32_16x16x32_bf16 v[44:47], a[224:227], a[164:167], v[44:47]\n"
"ds_read_b64_tr_b16 a[224:225], v11 offset:45056\n"
"ds_read_b64_tr_b16 a[226:227], v11 offset:47104\n"
"v_mfma_f32_16x16x32_bf16 v[32:35], a[228:231], a[168:171], v[32:35]\n"
"s_cmp_ge_u32 s29, 0x40000000\n"
"s_cselect_b64 vcc, -1, 0\n"
"v_mfma_f32_16x16x32_bf16 v[40:43], a[228:231], a[172:175], v[40:43]\n"
"ds_read_b64_tr_b16 a[228:229], v30 offset:12288\n"
"ds_read_b64_tr_b16 a[230:231], v30 offset:14336\n"
"v_mfma_f32_16x16x32_bf16 v[36:39], a[232:235], a[168:171], v[36:39]\n"
"buffer_load_dwordx4 v18, s[44:47], s64 offen offset:2048 lds\n"
"v_mfma_f32_16x16x32_bf16 v[44:47], a[232:235], a[172:175], v[44:47]\n"
"ds_read_b64_tr_b16 a[232:233], v30 offset:16384\n"
"ds_read_b64_tr_b16 a[234:235], v30 offset:18432\n"
"v_mfma_f32_16x16x32_bf16 v[216:219], v[240:243], v[192:195], v[216:219]\n"
"v_exp_f32 v32, v32\n"
"v_mfma_f32_16x16x32_bf16 v[220:223], v[240:243], v[200:203], v[220:223]\n"
"v_exp_f32 v33, v33\n"
"v_mfma_f32_16x16x32_bf16 v[64:67], a[236:239], v[192:195], v[64:67]\n"
"v_exp_f32 v40, v40\n"
"v_mfma_f32_16x16x32_bf16 v[72:75], a[236:239], v[200:203], v[72:75]\n"
"buffer_load_dword v5, v0, s[48:51], s85 offen\n"
"v_mfma_f32_16x16x32_bf16 v[68:71], a[240:243], v[192:195], v[68:71]\n"
"ds_read_b64_tr_b16 a[236:237], v30 offset:20480\n"
"ds_read_b64_tr_b16 a[238:239], v30 offset:22528\n"
"v_mfma_f32_16x16x32_bf16 v[76:79], a[240:243], v[200:203], v[76:79]\n"
"ds_read_b64_tr_b16 a[240:241], v30 offset:24576\n"
"ds_read_b64_tr_b16 a[242:243], v30 offset:26624\n"
"v_mfma_f32_16x16x32_bf16 v[80:83], a[244:247], v[192:195], v[80:83]\n"
"v_exp_f32 v41, v41\n"
"v_mfma_f32_16x16x32_bf16 v[88:91], a[244:247], v[200:203], v[88:91]\n"
"ds_read_b64_tr_b16 a[244:245], v30 offset:28672\n"
"ds_read_b64_tr_b16 a[246:247], v30 offset:30720\n"
"v_mfma_f32_16x16x32_bf16 v[84:87], a[176:179], v[192:195], v[84:87]\n"
"buffer_load_dwordx4 v19, s[44:47], s64 offen offset:3072 lds\n"
"v_mfma_f32_16x16x32_bf16 v[92:95], a[176:179], v[200:203], v[92:95]\n"
"s_add_u32 m0, s61, 81920\n"
"v_cvt_pk_bf16_f32 v32, v32, v33\n"
"v_mfma_f32_16x16x32_bf16 v[96:99], a[180:183], v[192:195], v[96:99]\n"
"v_exp_f32 v34, v34\n"
"v_mfma_f32_16x16x32_bf16 v[104:107], a[180:183], v[200:203], v[104:107]\n"
"v_exp_f32 v35, v35\n"
"v_mfma_f32_16x16x32_bf16 v[100:103], a[184:187], v[192:195], v[100:103]\n"
"v_cvt_pk_bf16_f32 v40, v40, v41\n"
"v_cvt_pk_bf16_f32 v33, v34, v35\n"
"v_mfma_f32_16x16x32_bf16 v[108:111], a[184:187], v[200:203], v[108:111]\n"
"v_exp_f32 v42, v42\n"
"v_mfma_f32_16x16x32_bf16 v[112:115], a[188:191], v[192:195], v[112:115]\n"
"v_exp_f32 v43, v43\n"
"v_mfma_f32_16x16x32_bf16 v[120:123], a[188:191], v[200:203], v[120:123]\n"
"v_cvt_pk_bf16_f32 v41, v42, v43\n"
"s_waitcnt lgkmcnt(2)\n"
"v_mfma_f32_16x16x32_bf16 v[116:119], a[192:195], v[192:195], v[116:119]\n"
"v_exp_f32 v36, v36\n"
"v_mfma_f32_16x16x32_bf16 v[124:127], a[192:195], v[200:203], v[124:127]\n"
"v_exp_f32 v37, v37\n"
"v_mfma_f32_16x16x32_bf16 v[128:131], a[196:199], v[192:195], v[128:131]\n"
"v_exp_f32 v38, v38\n"
"v_mfma_f32_16x16x32_bf16 v[136:139], a[196:199], v[200:203], v[136:139]\n"
"v_exp_f32 v39, v39\n"
"v_mfma_f32_16x16x32_bf16 v[132:135], a[200:203], v[192:195], v[132:135]\n"
"v_exp_f32 v44, v44\n"
"v_mfma_f32_16x16x32_bf16 v[140:143], a[200:203], v[200:203], v[140:143]\n"
"v_exp_f32 v45, v45\n"
"v_mfma_f32_16x16x32_bf16 v[144:147], a[204:207], v[192:195], v[144:147]\n"
"v_exp_f32 v46, v46\n"
"v_mfma_f32_16x16x32_bf16 v[152:155], a[204:207], v[200:203], v[152:155]\n"
"v_exp_f32 v47, v47\n"
"v_mfma_f32_16x16x32_bf16 v[148:151], a[208:211], v[192:195], v[148:151]\n"
"v_cvt_pk_bf16_f32 v34, v36, v37\n"
"v_cvt_pk_bf16_f32 v35, v38, v39\n"
"v_mfma_f32_16x16x32_bf16 v[156:159], a[208:211], v[200:203], v[156:159]\n"
"v_cvt_pk_bf16_f32 v42, v44, v45\n"
"v_cvt_pk_bf16_f32 v43, v46, v47\n"
"v_mfma_f32_16x16x32_bf16 v[160:163], a[212:215], v[192:195], v[160:163]\n"
"s_add_u32 s64, s64, 114688\n"
"v_mfma_f32_16x16x32_bf16 v[168:171], a[212:215], v[200:203], v[168:171]\n"
"s_cmp_ge_u32 s64, s77\n"
"s_cselect_b32 s84, 200576, 0\n"
"v_mfma_f32_16x16x32_bf16 v[164:167], a[216:219], v[192:195], v[164:167]\n"
"s_sub_u32 s64, s64, s84\n"
"v_mfma_f32_16x16x32_bf16 v[172:175], a[216:219], v[200:203], v[172:175]\n"
"s_cmp_eq_u32 s60, 0\n"
"s_cselect_b32 s64, s64, 0x40000000\n"
"v_mfma_f32_16x16x32_bf16 v[176:179], a[220:223], v[192:195], v[176:179]\n"
"v_mfma_f32_16x16x32_bf16 v[184:187], a[220:223], v[200:203], v[184:187]\n"
"v_mfma_f32_16x16x32_bf16 v[180:183], a[224:227], v[192:195], v[180:183]\n"
"v_mfma_f32_16x16x32_bf16 v[188:191], a[224:227], v[200:203], v[188:191]\n"
"v_mfma_f32_16x16x32_bf16 v[48:51], a[228:231], a[0:3], 0\n"
"v_mfma_f32_16x16x32_bf16 v[52:55], a[228:231], a[4:7], 0\n"
"v_mfma_f32_16x16x32_bf16 v[56:59], a[228:231], a[8:11], 0\n"
"v_mfma_f32_16x16x32_bf16 v[60:63], a[228:231], a[12:15], 0\n"
"v_mfma_f32_16x16x32_bf16 v[48:51], a[232:235], a[16:19], v[48:51]\n"
"v_mfma_f32_16x16x32_bf16 v[52:55], a[232:235], a[20:23], v[52:55]\n"
"s_waitcnt vmcnt(9)\n"
"s_barrier\n"
"ds_read_b64_tr_b16 a[176:177], v30 offset:32768\n"
"ds_read_b64_tr_b16 a[178:179], v30 offset:34816\n"
"v_mfma_f32_16x16x32_bf16 v[56:59], a[232:235], a[24:27], v[56:59]\n"
"buffer_load_dwordx4 v20, s[40:43], s67 offen offset:0 lds\n"
"ds_read_b64_tr_b16 a[180:181], v30 offset:36864\n"
"ds_read_b64_tr_b16 a[182:183], v30 offset:38912\n"
"v_mfma_f32_16x16x32_bf16 v[60:63], a[232:235], a[28:31], v[60:63]\n"
"ds_read_b128 a[184:187], v12 offset:0\n"
"v_mfma_f32_16x16x32_bf16 v[48:51], a[236:239], a[32:35], v[48:51]\n"
"ds_read_b128 a[188:191], v12 offset:8192\n"
"v_mfma_f32_16x16x32_bf16 v[52:55], a[236:239], a[36:39], v[52:55]\n"
"ds_read_b128 a[192:195], v13 offset:0\n"
"v_mfma_f32_16x16x32_bf16 v[56:59], a[236:239], a[40:43], v[56:59]\n"
"ds_read_b128 a[196:199], v13 offset:8192\n"
"v_mfma_f32_16x16x32_bf16 v[60:63], a[236:239], a[44:47], v[60:63]\n"
"buffer_load_dwordx4 v21, s[40:43], s67 offen offset:1024 lds\n"
"ds_read_b128 a[200:203], v14 offset:0\n"
"v_mfma_f32_16x16x32_bf16 v[48:51], a[240:243], a[48:51], v[48:51]\n"
"v_mfma_f32_16x16x32_bf16 v[52:55], a[240:243], a[52:55], v[52:55]\n"
"ds_read_b128 a[204:207], v14 offset:8192\n"
"v_mfma_f32_16x16x32_bf16 v[56:59], a[240:243], a[56:59], v[56:59]\n"
"ds_read_b128 a[208:211], v15 offset:0\n"
"v_mfma_f32_16x16x32_bf16 v[60:63], a[240:243], a[60:63], v[60:63]\n"
"ds_read_b128 a[212:215], v15 offset:8192\n"
"s_waitcnt lgkmcnt(10)\n"
"v_mfma_f32_16x16x32_bf16 v[48:51], a[244:247], a[64:67], v[48:51]\n"
"buffer_load_dwordx4 v22, s[40:43], s67 offen offset:2048 lds\n"
"v_mfma_f32_16x16x32_bf16 v[52:55], a[244:247], a[68:71], v[52:55]\n"
"ds_read_b128 a[216:219], v12 offset:256\n"
"v_mfma_f32_16x16x32_bf16 v[56:59], a[244:247], a[72:75], v[56:59]\n"
"ds_read_b128 a[220:223], v12 offset:8448\n"
"v_mfma_f32_16x16x32_bf16 v[60:63], a[244:247], a[76:79], v[60:63]\n"
"ds_read_b128 a[224:227], v13 offset:256\n"
"v_mfma_f32_16x16x32_bf16 v[48:51], a[176:179], a[80:83], v[48:51]\n"
"buffer_load_dwordx4 v23, s[40:43], s67 offen offset:3072 lds\n"
"ds_read_b128 a[228:231], v13 offset:8448\n"
"v_mfma_f32_16x16x32_bf16 v[52:55], a[176:179], a[84:87], v[52:55]\n"
"s_add_u32 m0, s61, 147456\n"
"s_add_u32 s67, s67, 16384\n"
"v_mfma_f32_16x16x32_bf16 v[56:59], a[176:179], a[88:91], v[56:59]\n"
"ds_read_b128 a[232:235], v14 offset:256\n"
"v_mfma_f32_16x16x32_bf16 v[60:63], a[176:179], a[92:95], v[60:63]\n"
"ds_read_b128 a[236:239], v14 offset:8448\n"
"s_waitcnt lgkmcnt(8)\n"
"v_mfma_f32_16x16x32_bf16 v[48:51], a[180:183], a[96:99], v[48:51]\n"
"ds_read_b128 a[240:243], v15 offset:256\n"
"v_mfma_f32_16x16x32_bf16 v[52:55], a[180:183], a[100:103], v[52:55]\n"
"buffer_load_dwordx4 v24, s[36:39], s66 offen offset:0 lds\n"
"v_mfma_f32_16x16x32_bf16 v[56:59], a[180:183], a[104:107], v[56:59]\n"
"ds_read_b128 a[244:247], v15 offset:8448\n"
"v_mfma_f32_16x16x32_bf16 v[60:63], a[180:183], a[108:111], v[60:63]\n"
"ds_read_b64_tr_b16 a[176:177], v8 offset:0\n"
"ds_read_b64_tr_b16 a[178:179], v8 offset:2048\n"
"v_mfma_f32_16x16x32_bf16 v[192:195], a[184:187], a[112:115], v[208:211]\n"
"ds_read_b64_tr_b16 a[180:181], v9 offset:0\n"
"ds_read_b64_tr_b16 a[182:183], v9 offset:2048\n"
"v_mfma_f32_16x16x32_bf16 v[200:203], a[184:187], a[116:119], v[212:215]\n"
"ds_read_b64_tr_b16 a[184:185], v10 offset:0\n"
"ds_read_b64_tr_b16 a[186:187], v10 offset:2048\n"
"v_mfma_f32_16x16x32_bf16 v[196:199], a[188:191], a[112:115], v[208:211]\n"
"buffer_load_dwordx4 v25, s[36:39], s66 offen offset:1024 lds\n"
"v_mfma_f32_16x16x32_bf16 v[204:207], a[188:191], a[116:119], v[212:215]\n"
"ds_read_b64_tr_b16 a[188:189], v11 offset:0\n"
"ds_read_b64_tr_b16 a[190:191], v11 offset:2048\n"
"v_mfma_f32_16x16x32_bf16 v[192:195], a[192:195], a[120:123], v[192:195]\n"
"v_cvt_pk_bf16_f32 v48, v48, v49\n"
"v_cvt_pk_bf16_f32 v49, v50, v51\n"
"v_mfma_f32_16x16x32_bf16 v[200:203], a[192:195], a[124:127], v[200:203]\n"
"ds_read_b64_tr_b16 a[192:193], v8 offset:4096\n"
"ds_read_b64_tr_b16 a[194:195], v8 offset:6144\n"
"v_mfma_f32_16x16x32_bf16 v[196:199], a[196:199], a[120:123], v[196:199]\n"
"v_cvt_pk_bf16_f32 v56, v56, v57\n"
"v_cvt_pk_bf16_f32 v50, v52, v53\n"
"v_mfma_f32_16x16x32_bf16 v[204:207], a[196:199], a[124:127], v[204:207]\n"
"buffer_load_dwordx4 v26, s[36:39], s66 offen offset:2048 lds\n"
"v_mfma_f32_16x16x32_bf16 v[192:195], a[200:203], a[128:131], v[192:195]\n"
"ds_read_b64_tr_b16 a[196:197], v9 offset:4096\n"
"ds_read_b64_tr_b16 a[198:199], v9 offset:6144\n"
"v_mfma_f32_16x16x32_bf16 v[200:203], a[200:203], a[132:135], v[200:203]\n"
"ds_read_b64_tr_b16 a[200:201], v10 offset:4096\n"
"ds_read_b64_tr_b16 a[202:203], v10 offset:6144\n"
"v_mfma_f32_16x16x32_bf16 v[196:199], a[204:207], a[128:131], v[196:199]\n"
"v_cvt_pk_bf16_f32 v51, v54, v55\n"
"v_cvt_pk_bf16_f32 v57, v58, v59\n"
"v_mfma_f32_16x16x32_bf16 v[204:207], a[204:207], a[132:135], v[204:207]\n"
"ds_read_b64_tr_b16 a[204:205], v11 offset:4096\n"
"s_waitcnt lgkmcnt(9)\n"
"v_mfma_f32_16x16x32_bf16 v[192:195], a[208:211], a[136:139], v[192:195]\n"
"buffer_load_dwordx4 v27, s[36:39], s66 offen offset:3072 lds\n"
"v_mfma_f32_16x16x32_bf16 v[200:203], a[208:211], a[140:143], v[200:203]\n"
"ds_read_b64_tr_b16 a[208:209], v8 offset:8192\n"
"ds_read_b64_tr_b16 a[210:211], v8 offset:10240\n"
"v_mfma_f32_16x16x32_bf16 v[196:199], a[212:215], a[136:139], v[196:199]\n"
"ds_read_b64_tr_b16 a[206:207], v11 offset:6144\n"
"s_add_u32 s66, s66, 16384\n"
"v_mfma_f32_16x16x32_bf16 v[204:207], a[212:215], a[140:143], v[204:207]\n"
"ds_read_b64_tr_b16 a[212:213], v9 offset:8192\n"
"ds_read_b64_tr_b16 a[214:215], v9 offset:10240\n"
"v_mfma_f32_16x16x32_bf16 v[192:195], a[216:219], a[144:147], v[192:195]\n"
"s_add_u32 m0, s61, 98304\n"
"v_cvt_pk_bf16_f32 v58, v60, v61\n"
"v_mfma_f32_16x16x32_bf16 v[200:203], a[216:219], a[148:151], v[200:203]\n"
"ds_read_b64_tr_b16 a[216:217], v10 offset:8192\n"
"ds_read_b64_tr_b16 a[218:219], v10 offset:10240\n"
"v_mfma_f32_16x16x32_bf16 v[196:199], a[220:223], a[144:147], v[196:199]\n"
"v_cvt_pk_bf16_f32 v59, v62, v63\n"
"v_cndmask_b32 v228, v228, v48, vcc\n"
"v_mfma_f32_16x16x32_bf16 v[204:207], a[220:223], a[148:151], v[204:207]\n"
"ds_read_b64_tr_b16 a[220:221], v11 offset:8192\n"
"ds_read_b64_tr_b16 a[222:223], v11 offset:10240\n"
"v_mfma_f32_16x16x32_bf16 v[192:195], a[224:227], a[152:155], v[192:195]\n"
"v_cndmask_b32 v229, v229, v49, vcc\n"
"v_cndmask_b32 v230, v230, v50, vcc\n"
"v_mfma_f32_16x16x32_bf16 v[200:203], a[224:227], a[156:159], v[200:203]\n"
"ds_read_b64_tr_b16 a[224:225], v8 offset:12288\n"
"ds_read_b64_tr_b16 a[226:227], v8 offset:14336\n"
"v_mfma_f32_16x16x32_bf16 v[196:199], a[228:231], a[152:155], v[196:199]\n"
"v_cndmask_b32 v231, v231, v51, vcc\n"
"v_mfma_f32_16x16x32_bf16 v[204:207], a[228:231], a[156:159], v[204:207]\n"
"ds_read_b64_tr_b16 a[228:229], v9 offset:12288\n"
"ds_read_b64_tr_b16 a[230:231], v9 offset:14336\n"
"v_mfma_f32_16x16x32_bf16 v[192:195], a[232:235], a[160:163], v[192:195]\n"
"s_cmp_ge_u32 s58, 0x40000000\n"
"s_cselect_b64 vcc, -1, 0\n"
"v_mfma_f32_16x16x32_bf16 v[200:203], a[232:235], a[164:167], v[200:203]\n"
"ds_read_b64_tr_b16 a[232:233], v10 offset:12288\n"
"ds_read_b64_tr_b16 a[234:235], v10 offset:14336\n"
"v_mfma_f32_16x16x32_bf16 v[196:199], a[236:239], a[160:163], v[196:199]\n"
"v_cndmask_b32 v228, v228, v56, vcc\n"
"v_cndmask_b32 v229, v229, v57, vcc\n"
"v_mfma_f32_16x16x32_bf16 v[204:207], a[236:239], a[164:167], v[204:207]\n"
"ds_read_b64_tr_b16 a[236:237], v11 offset:12288\n"
"ds_read_b64_tr_b16 a[238:239], v11 offset:14336\n"
"v_mfma_f32_16x16x32_bf16 v[192:195], a[240:243], a[168:171], v[192:195]\n"
"v_cndmask_b32 v230, v230, v58, vcc\n"
"v_cndmask_b32 v231, v231, v59, vcc\n"
"v_mfma_f32_16x16x32_bf16 v[200:203], a[240:243], a[172:175], v[200:203]\n"
"buffer_store_dwordx4 v[48:51], v1, s[20:23], s68 offen sc1\n"
"v_mfma_f32_16x16x32_bf16 v[196:199], a[244:247], a[168:171], v[196:199]\n"
"s_add_u32 s68, s30, 49152\n"
"v_mfma_f32_16x16x32_bf16 v[204:207], a[244:247], a[172:175], v[204:207]\n"
"s_cmp_ge_u32 s30, 0x40000000\n"
"s_cselect_b64 vcc, -1, 0\n"
"v_mfma_f32_16x16x32_bf16 v[216:219], v[240:243], v[32:35], v[216:219]\n"
"v_exp_f32 v192, v192\n"
"v_mfma_f32_16x16x32_bf16 v[220:223], v[240:243], v[40:43], v[220:223]\n"
"v_exp_f32 v193, v193\n"
"v_mfma_f32_16x16x32_bf16 v[64:67], a[176:179], v[32:35], v[64:67]\n"
"buffer_store_dwordx4 v[56:59], v1, s[20:23], s69 offen sc1\n"
"v_mfma_f32_16x16x32_bf16 v[72:75], a[176:179], v[40:43], v[72:75]\n"
"v_cvt_pk_bf16_f32 v192, v192, v193\n"
"s_add_u32 s69, s59, 49152\n"
"v_mfma_f32_16x16x32_bf16 v[68:71], a[180:183], v[32:35], v[68:71]\n"
"v_exp_f32 v194, v194\n"
"v_mfma_f32_16x16x32_bf16 v[76:79], a[180:183], v[40:43], v[76:79]\n"
"v_exp_f32 v195, v195\n"
"v_mfma_f32_16x16x32_bf16 v[80:83], a[184:187], v[32:35], v[80:83]\n"
"v_exp_f32 v200, v200\n"
"v_mfma_f32_16x16x32_bf16 v[88:91], a[184:187], v[40:43], v[88:91]\n"
"v_cvt_pk_bf16_f32 v193, v194, v195\n"
"s_waitcnt lgkmcnt(0)\n"
"v_mfma_f32_16x16x32_bf16 v[84:87], a[188:191], v[32:35], v[84:87]\n"
"v_exp_f32 v201, v201\n"
"v_mfma_f32_16x16x32_bf16 v[92:95], a[188:191], v[40:43], v[92:95]\n"
"v_exp_f32 v196, v196\n"
"v_mfma_f32_16x16x32_bf16 v[96:99], a[192:195], v[32:35], v[96:99]\n"
"v_exp_f32 v197, v197\n"
"v_mfma_f32_16x16x32_bf16 v[104:107], a[192:195], v[40:43], v[104:107]\n"
"s_waitcnt vmcnt(6)\n"
"s_barrier\n"
"ds_read_b128 a[240:243], v12 offset:16384\n"
"v_mfma_f32_16x16x32_bf16 v[100:103], a[196:199], v[32:35], v[100:103]\n"
"buffer_load_dwordx4 v20, s[40:43], s67 offen offset:0 lds\n"
"ds_read_b128 a[244:247], v12 offset:24576\n"
"v_mfma_f32_16x16x32_bf16 v[108:111], a[196:199], v[40:43], v[108:111]\n"
"ds_read_b128 a[176:179], v13 offset:16384\n"
"v_cvt_pk_bf16_f32 v200, v200, v201\n"
"v_mfma_f32_16x16x32_bf16 v[112:115], a[200:203], v[32:35], v[112:115]\n"
"v_exp_f32 v198, v198\n"
"v_mfma_f32_16x16x32_bf16 v[120:123], a[200:203], v[40:43], v[120:123]\n"
"ds_read_b128 a[180:183], v13 offset:24576\n"
"v_cvt_pk_bf16_f32 v194, v196, v197\n"
"v_mfma_f32_16x16x32_bf16 v[116:119], a[204:207], v[32:35], v[116:119]\n"
"buffer_load_dwordx4 v21, s[40:43], s67 offen offset:1024 lds\n"
"ds_read_b128 a[184:187], v14 offset:16384\n"
"v_mfma_f32_16x16x32_bf16 v[124:127], a[204:207], v[40:43], v[124:127]\n"
"v_exp_f32 v199, v199\n"
"v_mfma_f32_16x16x32_bf16 v[128:131], a[208:211], v[32:35], v[128:131]\n"
"ds_read_b128 a[188:191], v14 offset:24576\n"
"v_cvt_pk_bf16_f32 v195, v198, v199\n"
"v_mfma_f32_16x16x32_bf16 v[136:139], a[208:211], v[40:43], v[136:139]\n"
"ds_read_b128 a[192:195], v15 offset:16384\n"
"v_mfma_f32_16x16x32_bf16 v[132:135], a[212:215], v[32:35], v[132:135]\n"
"buffer_load_dwordx4 v22, s[40:43], s67 offen offset:2048 lds\n"
"v_mfma_f32_16x16x32_bf16 v[140:143], a[212:215], v[40:43], v[140:143]\n"
"ds_read_b128 a[196:199], v15 offset:24576\n"
"v_mfma_f32_16x16x32_bf16 v[144:147], a[216:219], v[32:35], v[144:147]\n"
"ds_read_b128 a[200:203], v12 offset:16640\n"
"v_mfma_f32_16x16x32_bf16 v[152:155], a[216:219], v[40:43], v[152:155]\n"
"ds_read_b128 a[204:207], v12 offset:24832\n"
"v_mfma_f32_16x16x32_bf16 v[148:151], a[220:223], v[32:35], v[148:151]\n"
"buffer_load_dwordx4 v23, s[40:43], s67 offen offset:3072 lds\n"
"s_add_u32 m0, s61, 114688\n"
"v_mfma_f32_16x16x32_bf16 v[156:159], a[220:223], v[40:43], v[156:159]\n"
"ds_read_b128 a[208:211], v13 offset:16640\n"
"s_add_u32 s67, s67, 16384\n"
"v_mfma_f32_16x16x32_bf16 v[160:163], a[224:227], v[32:35], v[160:163]\n"
"ds_read_b128 a[212:215], v13 offset:24832\n"
"v_mfma_f32_16x16x32_bf16 v[168:171], a[224:227], v[40:43], v[168:171]\n"
"ds_read_b128 a[216:219], v14 offset:16640\n"
"v_mfma_f32_16x16x32_bf16 v[164:167], a[228:231], v[32:35], v[164:167]\n"
"buffer_load_dwordx4 v24, s[36:39], s66 offen offset:0 lds\n"
"v_mfma_f32_16x16x32_bf16 v[172:175], a[228:231], v[40:43], v[172:175]\n"
"ds_read_b128 a[220:223], v14 offset:24832\n"
"v_mfma_f32_16x16x32_bf16 v[176:179], a[232:235], v[32:35], v[176:179]\n"
"ds_read_b128 a[224:227], v15 offset:16640\n"
"v_mfma_f32_16x16x32_bf16 v[184:187], a[232:235], v[40:43], v[184:187]\n"
"ds_read_b128 a[228:231], v15 offset:24832\n"
"v_mfma_f32_16x16x32_bf16 v[180:183], a[236:239], v[32:35], v[180:183]\n"
"buffer_load_dwordx4 v25, s[36:39], s66 offen offset:1024 lds\n"
"ds_read_b64_tr_b16 a[232:233], v8 offset:16384\n"
"ds_read_b64_tr_b16 a[234:235], v8 offset:18432\n"
"v_mfma_f32_16x16x32_bf16 v[188:191], a[236:239], v[40:43], v[188:191]\n"
"s_waitcnt lgkmcnt(8)\n"
"v_mfma_f32_16x16x32_bf16 v[32:35], a[240:243], a[112:115], v[208:211]\n"
"ds_read_b64_tr_b16 a[236:237], v9 offset:16384\n"
"ds_read_b64_tr_b16 a[238:239], v9 offset:18432\n"
"v_mfma_f32_16x16x32_bf16 v[40:43], a[240:243], a[116:119], v[212:215]\n"
"ds_read_b64_tr_b16 a[240:241], v10 offset:16384\n"
"ds_read_b64_tr_b16 a[242:243], v10 offset:18432\n"
"v_mfma_f32_16x16x32_bf16 v[36:39], a[244:247], a[112:115], v[208:211]\n"
"buffer_load_dwordx4 v26, s[36:39], s66 offen offset:2048 lds\n"
"v_mfma_f32_16x16x32_bf16 v[44:47], a[244:247], a[116:119], v[212:215]\n"
"ds_read_b64_tr_b16 a[244:245], v11 offset:16384\n"
"ds_read_b64_tr_b16 a[246:247], v11 offset:18432\n"
"v_mfma_f32_16x16x32_bf16 v[32:35], a[176:179], a[120:123], v[32:35]\n"
"v_exp_f32 v202, v202\n"
"v_mfma_f32_16x16x32_bf16 v[40:43], a[176:179], a[124:127], v[40:43]\n"
"ds_read_b64_tr_b16 a[176:177], v8 offset:20480\n"
"ds_read_b64_tr_b16 a[178:179], v8 offset:22528\n"
"v_mfma_f32_16x16x32_bf16 v[36:39], a[180:183], a[120:123], v[36:39]\n"
"buffer_load_dwordx4 v27, s[36:39], s66 offen offset:3072 lds\n"
"s_add_u32 m0, s61, 0\n"
"v_mfma_f32_16x16x32_bf16 v[44:47], a[180:183], a[124:127], v[44:47]\n"
"ds_read_b64_tr_b16 a[180:181], v9 offset:20480\n"
"ds_read_b64_tr_b16 a[182:183], v9 offset:22528\n"
"v_mfma_f32_16x16x32_bf16 v[32:35], a[184:187], a[128:131], v[32:35]\n"
"v_exp_f32 v203, v203\n"
"v_mfma_f32_16x16x32_bf16 v[40:43], a[184:187], a[132:135], v[40:43]\n"
"ds_read_b64_tr_b16 a[184:185], v10 offset:20480\n"
"ds_read_b64_tr_b16 a[186:187], v10 offset:22528\n"
"v_mfma_f32_16x16x32_bf16 v[36:39], a[188:191], a[128:131], v[36:39]\n"
"buffer_load_dwordx4 v16, s[44:47], s64 offen offset:0 lds\n"
"v_mfma_f32_16x16x32_bf16 v[44:47], a[188:191], a[132:135], v[44:47]\n"
"ds_read_b64_tr_b16 a[188:189], v11 offset:20480\n"
"ds_read_b64_tr_b16 a[190:191], v11 offset:22528\n"
"v_mfma_f32_16x16x32_bf16 v[32:35], a[192:195], a[136:139], v[32:35]\n"
"v_exp_f32 v204, v204\n"
"v_mfma_f32_16x16x32_bf16 v[40:43], a[192:195], a[140:143], v[40:43]\n"
"ds_read_b64_tr_b16 a[192:193], v8 offset:24576\n"
"ds_read_b64_tr_b16 a[194:195], v8 offset:26624\n"
"v_mfma_f32_16x16x32_bf16 v[36:39], a[196:199], a[136:139], v[36:39]\n"
"buffer_load_dwordx4 v17, s[44:47], s64 offen offset:1024 lds\n"
"v_mfma_f32_16x16x32_bf16 v[44:47], a[196:199], a[140:143], v[44:47]\n"
"ds_read_b64_tr_b16 a[196:197], v9 offset:24576\n"
"ds_read_b64_tr_b16 a[198:199], v9 offset:26624\n"
"v_mfma_f32_16x16x32_bf16 v[32:35], a[200:203], a[144:147], v[32:35]\n"
"v_exp_f32 v205, v205\n"
"v_mfma_f32_16x16x32_bf16 v[40:43], a[200:203], a[148:151], v[40:43]\n"
"ds_read_b64_tr_b16 a[200:201], v10 offset:24576\n"
"ds_read_b64_tr_b16 a[202:203], v10 offset:26624\n"
"v_mfma_f32_16x16x32_bf16 v[36:39], a[204:207], a[144:147], v[36:39]\n"
"buffer_load_dwordx4 v18, s[44:47], s64 offen offset:2048 lds\n"
"v_mfma_f32_16x16x32_bf16 v[44:47], a[204:207], a[148:151], v[44:47]\n"
"v_cvt_pk_bf16_f32 v201, v202, v203\n"
"s_waitcnt lgkmcnt(8)\n"
"v_mfma_f32_16x16x32_bf16 v[32:35], a[208:211], a[152:155], v[32:35]\n"
"v_exp_f32 v206, v206\n"
"v_mfma_f32_16x16x32_bf16 v[40:43], a[208:211], a[156:159], v[40:43]\n"
"v_exp_f32 v207, v207\n"
"v_mfma_f32_16x16x32_bf16 v[36:39], a[212:215], a[152:155], v[36:39]\n"
"buffer_load_dwordx4 v19, s[44:47], s64 offen offset:3072 lds\n"
"v_mfma_f32_16x16x32_bf16 v[44:47], a[212:215], a[156:159], v[44:47]\n"
"v_cvt_pk_bf16_f32 v202, v204, v205\n"
"v_cvt_pk_bf16_f32 v203, v206, v207\n"
"v_mfma_f32_16x16x32_bf16 v[32:35], a[216:219], a[160:163], v[32:35]\n"
"ds_read_b64_tr_b16 a[204:205], v11 offset:24576\n"
"ds_read_b64_tr_b16 a[206:207], v11 offset:26624\n"
"v_mfma_f32_16x16x32_bf16 v[40:43], a[216:219], a[164:167], v[40:43]\n"
"ds_read_b64_tr_b16 a[208:209], v8 offset:28672\n"
"ds_read_b64_tr_b16 a[210:211], v8 offset:30720\n"
"v_mfma_f32_16x16x32_bf16 v[36:39], a[220:223], a[160:163], v[36:39]\n"
"s_add_u32 m0, s61, 65536\n"
"ds_read_b64_tr_b16 a[212:213], v9 offset:28672\n"
"v_mfma_f32_16x16x32_bf16 v[44:47], a[220:223], a[164:167], v[44:47]\n"
"ds_read_b64_tr_b16 a[214:215], v9 offset:30720\n"
"ds_read_b64_tr_b16 a[216:217], v10 offset:28672\n"
"v_mfma_f32_16x16x32_bf16 v[32:35], a[224:227], a[168:171], v[32:35]\n"
"ds_read_b64_tr_b16 a[218:219], v10 offset:30720\n"
"ds_read_b64_tr_b16 a[220:221], v11 offset:28672\n"
"v_mfma_f32_16x16x32_bf16 v[40:43], a[224:227], a[172:175], v[40:43]\n"
"ds_read_b64_tr_b16 a[222:223], v11 offset:30720\n"
"ds_read_b64_tr_b16 a[224:225], v30 offset:40960\n"
"v_mfma_f32_16x16x32_bf16 v[36:39], a[228:231], a[168:171], v[36:39]\n"
"ds_read_b64_tr_b16 a[226:227], v30 offset:43008\n"
"s_add_u32 s66, s66, 16384\n"
"v_mfma_f32_16x16x32_bf16 v[44:47], a[228:231], a[172:175], v[44:47]\n"
"ds_read_b64_tr_b16 a[228:229], v30 offset:45056\n"
"ds_read_b64_tr_b16 a[230:231], v30 offset:47104\n"
"v_mfma_f32_16x16x32_bf16 v[216:219], v[240:243], v[192:195], v[216:219]\n"
"v_exp_f32 v32, v32\n"
"v_mfma_f32_16x16x32_bf16 v[220:223], v[240:243], v[200:203], v[220:223]\n"
"v_exp_f32 v33, v33\n"
"v_mfma_f32_16x16x32_bf16 v[64:67], a[232:235], v[192:195], v[64:67]\n"
"v_exp_f32 v40, v40\n"
"v_mfma_f32_16x16x32_bf16 v[72:75], a[232:235], v[200:203], v[72:75]\n"
"ds_read_b64_tr_b16 a[232:233], v30 offset:49152\n"
"ds_read_b64_tr_b16 a[234:235], v30 offset:51200\n"
"v_mfma_f32_16x16x32_bf16 v[68:71], a[236:239], v[192:195], v[68:71]\n"
"v_exp_f32 v41, v41\n"
"v_mfma_f32_16x16x32_bf16 v[76:79], a[236:239], v[200:203], v[76:79]\n"
"ds_read_b64_tr_b16 a[236:237], v30 offset:53248\n"
"ds_read_b64_tr_b16 a[238:239], v30 offset:55296\n"
"v_mfma_f32_16x16x32_bf16 v[80:83], a[240:243], v[192:195], v[80:83]\n"
"v_cvt_pk_bf16_f32 v32, v32, v33\n"
"v_cvt_pk_bf16_f32 v40, v40, v41\n"
"v_mfma_f32_16x16x32_bf16 v[88:91], a[240:243], v[200:203], v[88:91]\n"
"ds_read_b64_tr_b16 a[240:241], v30 offset:57344\n"
"ds_read_b64_tr_b16 a[242:243], v30 offset:59392\n"
"v_mfma_f32_16x16x32_bf16 v[84:87], a[244:247], v[192:195], v[84:87]\n"
"v_exp_f32 v34, v34\n"
"v_mfma_f32_16x16x32_bf16 v[92:95], a[244:247], v[200:203], v[92:95]\n"
"ds_read_b64_tr_b16 a[244:245], v30 offset:61440\n"
"ds_read_b64_tr_b16 a[246:247], v30 offset:63488\n"
"v_mfma_f32_16x16x32_bf16 v[96:99], a[176:179], v[192:195], v[96:99]\n"
"v_exp_f32 v35, v35\n"
"v_mfma_f32_16x16x32_bf16 v[104:107], a[176:179], v[200:203], v[104:107]\n"
"v_exp_f32 v42, v42\n"
"v_mfma_f32_16x16x32_bf16 v[100:103], a[180:183], v[192:195], v[100:103]\n"
"v_exp_f32 v43, v43\n"
"v_mfma_f32_16x16x32_bf16 v[108:111], a[180:183], v[200:203], v[108:111]\n"
"v_cvt_pk_bf16_f32 v33, v34, v35\n"
"v_cvt_pk_bf16_f32 v41, v42, v43\n"
"v_mfma_f32_16x16x32_bf16 v[112:115], a[184:187], v[192:195], v[112:115]\n"
"v_exp_f32 v36, v36\n"
"v_mfma_f32_16x16x32_bf16 v[120:123], a[184:187], v[200:203], v[120:123]\n"
"s_waitcnt lgkmcnt(4)\n"
"v_mfma_f32_16x16x32_bf16 v[116:119], a[188:191], v[192:195], v[116:119]\n"
"v_exp_f32 v37, v37\n"
"v_mfma_f32_16x16x32_bf16 v[124:127], a[188:191], v[200:203], v[124:127]\n"
"v_exp_f32 v38, v38\n"
"v_mfma_f32_16x16x32_bf16 v[128:131], a[192:195], v[192:195], v[128:131]\n"
"v_exp_f32 v39, v39\n"
"v_mfma_f32_16x16x32_bf16 v[136:139], a[192:195], v[200:203], v[136:139]\n"
"v_exp_f32 v44, v44\n"
"v_mfma_f32_16x16x32_bf16 v[132:135], a[196:199], v[192:195], v[132:135]\n"
"v_exp_f32 v45, v45\n"
"v_mfma_f32_16x16x32_bf16 v[140:143], a[196:199], v[200:203], v[140:143]\n"
"v_exp_f32 v46, v46\n"
"v_mfma_f32_16x16x32_bf16 v[144:147], a[200:203], v[192:195], v[144:147]\n"
"v_exp_f32 v47, v47\n"
"v_mfma_f32_16x16x32_bf16 v[152:155], a[200:203], v[200:203], v[152:155]\n"
"v_cvt_pk_bf16_f32 v34, v36, v37\n"
"v_cvt_pk_bf16_f32 v35, v38, v39\n"
"v_mfma_f32_16x16x32_bf16 v[148:151], a[204:207], v[192:195], v[148:151]\n"
"v_cvt_pk_bf16_f32 v42, v44, v45\n"
"v_cvt_pk_bf16_f32 v43, v46, v47\n"
"v_mfma_f32_16x16x32_bf16 v[156:159], a[204:207], v[200:203], v[156:159]\n"
"v_mfma_f32_16x16x32_bf16 v[160:163], a[208:211], v[192:195], v[160:163]\n"
"v_mfma_f32_16x16x32_bf16 v[168:171], a[208:211], v[200:203], v[168:171]\n"
"v_mfma_f32_16x16x32_bf16 v[164:167], a[212:215], v[192:195], v[164:167]\n"
"v_mfma_f32_16x16x32_bf16 v[172:175], a[212:215], v[200:203], v[172:175]\n"
"v_mfma_f32_16x16x32_bf16 v[176:179], a[216:219], v[192:195], v[176:179]\n"
"v_mfma_f32_16x16x32_bf16 v[184:187], a[216:219], v[200:203], v[184:187]\n"
"v_mfma_f32_16x16x32_bf16 v[180:183], a[220:223], v[192:195], v[180:183]\n"
"v_mfma_f32_16x16x32_bf16 v[188:191], a[220:223], v[200:203], v[188:191]\n"
"v_mfma_f32_16x16x32_bf16 v[48:51], a[224:227], a[0:3], 0\n"
"v_mfma_f32_16x16x32_bf16 v[52:55], a[224:227], a[4:7], 0\n"
"v_mfma_f32_16x16x32_bf16 v[56:59], a[224:227], a[8:11], 0\n"
"v_mfma_f32_16x16x32_bf16 v[60:63], a[224:227], a[12:15], 0\n"
"v_mfma_f32_16x16x32_bf16 v[48:51], a[228:231], a[16:19], v[48:51]\n"
"v_mfma_f32_16x16x32_bf16 v[52:55], a[228:231], a[20:23], v[52:55]\n"
"s_waitcnt vmcnt(0)\n"
"s_barrier\n"
"ds_read_b64_tr_b16 a[176:177], v30 offset:0\n"
"ds_read_b64_tr_b16 a[178:179], v30 offset:2048\n"
"v_mfma_f32_16x16x32_bf16 v[56:59], a[228:231], a[24:27], v[56:59]\n"
"buffer_load_dwordx4 v20, s[40:43], s67 offen offset:0 lds\n"
"ds_read_b128 a[180:183], v12 offset:32768\n"
"v_mfma_f32_16x16x32_bf16 v[60:63], a[228:231], a[28:31], v[60:63]\n"
"ds_read_b128 a[184:187], v12 offset:40960\n"
"v_mfma_f32_16x16x32_bf16 v[48:51], a[232:235], a[32:35], v[48:51]\n"
"ds_read_b128 a[188:191], v13 offset:32768\n"
"v_mfma_f32_16x16x32_bf16 v[52:55], a[232:235], a[36:39], v[52:55]\n"
"ds_read_b128 a[192:195], v13 offset:40960\n"
"v_mfma_f32_16x16x32_bf16 v[56:59], a[232:235], a[40:43], v[56:59]\n"
"ds_read_b128 a[196:199], v14 offset:32768\n"
"v_mfma_f32_16x16x32_bf16 v[60:63], a[232:235], a[44:47], v[60:63]\n"
"buffer_load_dwordx4 v21, s[40:43], s67 offen offset:1024 lds\n"
"v_mfma_f32_16x16x32_bf16 v[48:51], a[236:239], a[48:51], v[48:51]\n"
"ds_read_b128 a[200:203], v14 offset:40960\n"
"v_mfma_f32_16x16x32_bf16 v[52:55], a[236:239], a[52:55], v[52:55]\n"
"ds_read_b128 a[204:207], v15 offset:32768\n"
"v_mfma_f32_16x16x32_bf16 v[56:59], a[236:239], a[56:59], v[56:59]\n"
"ds_read_b128 a[208:211], v15 offset:40960\n"
"v_mfma_f32_16x16x32_bf16 v[60:63], a[236:239], a[60:63], v[60:63]\n"
"buffer_load_dwordx4 v22, s[40:43], s67 offen offset:2048 lds\n"
"s_waitcnt lgkmcnt(8)\n"
"v_mfma_f32_16x16x32_bf16 v[48:51], a[240:243], a[64:67], v[48:51]\n"
"ds_read_b128 a[212:215], v12 offset:33024\n"
"v_mfma_f32_16x16x32_bf16 v[52:55], a[240:243], a[68:71], v[52:55]\n"
"ds_read_b128 a[216:219], v12 offset:41216\n"
"v_mfma_f32_16x16x32_bf16 v[56:59], a[240:243], a[72:75], v[56:59]\n"
"ds_read_b128 a[220:223], v13 offset:33024\n"
"v_mfma_f32_16x16x32_bf16 v[60:63], a[240:243], a[76:79], v[60:63]\n"
"buffer_load_dwordx4 v23, s[40:43], s67 offen offset:3072 lds\n"
"ds_read_b128 a[224:227], v13 offset:41216\n"
"s_add_u32 m0, s61, 131072\n"
"v_mfma_f32_16x16x32_bf16 v[48:51], a[244:247], a[80:83], v[48:51]\n"
"ds_read_b128 a[228:231], v14 offset:33024\n"
"s_add_u32 s67, s67, 16384\n"
"v_mfma_f32_16x16x32_bf16 v[52:55], a[244:247], a[84:87], v[52:55]\n"
"ds_read_b128 a[232:235], v14 offset:41216\n"
"v_mfma_f32_16x16x32_bf16 v[56:59], a[244:247], a[88:91], v[56:59]\n"
"ds_read_b128 a[236:239], v15 offset:33024\n"
"v_mfma_f32_16x16x32_bf16 v[60:63], a[244:247], a[92:95], v[60:63]\n"
"ds_read_b128 a[240:243], v15 offset:41216\n"
"v_mfma_f32_16x16x32_bf16 v[48:51], a[176:179], a[96:99], v[48:51]\n"
"buffer_load_dwordx4 v24, s[36:39], s66 offen offset:0 lds\n"
"v_mfma_f32_16x16x32_bf16 v[52:55], a[176:179], a[100:103], v[52:55]\n"
"ds_read_b64_tr_b16 a[244:245], v8 offset:32768\n"
"ds_read_b64_tr_b16 a[246:247], v8 offset:34816\n"
"v_mfma_f32_16x16x32_bf16 v[56:59], a[176:179], a[104:107], v[56:59]\n"
"v_mfma_f32_16x16x32_bf16 v[60:63], a[176:179], a[108:111], v[60:63]\n"
"ds_read_b64_tr_b16 a[176:177], v9 offset:32768\n"
"s_waitcnt lgkmcnt(9)\n"
"v_mfma_f32_16x16x32_bf16 v[192:195], a[180:183], a[112:115], v[208:211]\n"
"ds_read_b64_tr_b16 a[178:179], v9 offset:34816\n"
"v_cvt_pk_bf16_f32 v48, v48, v49\n"
"v_mfma_f32_16x16x32_bf16 v[200:203], a[180:183], a[116:119], v[212:215]\n"
"buffer_load_dwordx4 v25, s[36:39], s66 offen offset:1024 lds\n"
"ds_read_b64_tr_b16 a[180:181], v10 offset:32768\n"
"v_mfma_f32_16x16x32_bf16 v[196:199], a[184:187], a[112:115], v[208:211]\n"
"ds_read_b64_tr_b16 a[182:183], v10 offset:34816\n"
"v_cvt_pk_bf16_f32 v49, v50, v51\n"
"v_mfma_f32_16x16x32_bf16 v[204:207], a[184:187], a[116:119], v[212:215]\n"
"v_cvt_pk_bf16_f32 v56, v56, v57\n"
"v_cvt_pk_bf16_f32 v50, v52, v53\n"
"v_mfma_f32_16x16x32_bf16 v[192:195], a[188:191], a[120:123], v[192:195]\n"
"ds_read_b64_tr_b16 a[184:185], v11 offset:32768\n"
"v_cvt_pk_bf16_f32 v51, v54, v55\n"
"v_mfma_f32_16x16x32_bf16 v[200:203], a[188:191], a[124:127], v[200:203]\n"
"ds_read_b64_tr_b16 a[186:187], v11 offset:34816\n"
"ds_read_b64_tr_b16 a[188:189], v8 offset:36864\n"
"v_mfma_f32_16x16x32_bf16 v[196:199], a[192:195], a[120:123], v[196:199]\n"
"buffer_load_dwordx4 v26, s[36:39], s66 offen offset:2048 lds\n"
"v_mfma_f32_16x16x32_bf16 v[204:207], a[192:195], a[124:127], v[204:207]\n"
"v_cvt_pk_bf16_f32 v57, v58, v59\n"
"ds_read_b64_tr_b16 a[190:191], v8 offset:38912\n"
"ds_read_b64_tr_b16 a[192:193], v9 offset:36864\n"
"v_mfma_f32_16x16x32_bf16 v[192:195], a[196:199], a[128:131], v[192:195]\n"
"ds_read_b64_tr_b16 a[194:195], v9 offset:38912\n"
"v_cvt_pk_bf16_f32 v58, v60, v61\n"
"v_mfma_f32_16x16x32_bf16 v[200:203], a[196:199], a[132:135], v[200:203]\n"
"v_cvt_pk_bf16_f32 v59, v62, v63\n"
"ds_read_b64_tr_b16 a[196:197], v10 offset:36864\n"
"v_mfma_f32_16x16x32_bf16 v[196:199], a[200:203], a[128:131], v[196:199]\n"
"buffer_store_dwordx4 v[48:51], v1, s[20:23], s68 offen sc1\n"
"v_mfma_f32_16x16x32_bf16 v[204:207], a[200:203], a[132:135], v[204:207]\n"
"ds_read_b64_tr_b16 a[200:201], v11 offset:36864\n"
"ds_read_b64_tr_b16 a[202:203], v11 offset:38912\n"
"v_mfma_f32_16x16x32_bf16 v[192:195], a[204:207], a[136:139], v[192:195]\n"
"ds_read_b64_tr_b16 a[198:199], v10 offset:38912\n"
"v_cndmask_b32 v228, v228, v48, vcc\n"
"v_mfma_f32_16x16x32_bf16 v[200:203], a[204:207], a[140:143], v[200:203]\n"
"ds_read_b64_tr_b16 a[204:205], v8 offset:40960\n"
"ds_read_b64_tr_b16 a[206:207], v8 offset:43008\n"
"v_mfma_f32_16x16x32_bf16 v[196:199], a[208:211], a[136:139], v[196:199]\n"
"buffer_store_dwordx4 v[56:59], v1, s[20:23], s69 offen sc1\n"
"v_mfma_f32_16x16x32_bf16 v[204:207], a[208:211], a[140:143], v[204:207]\n"
"ds_read_b64_tr_b16 a[208:209], v9 offset:40960\n"
"ds_read_b64_tr_b16 a[210:211], v9 offset:43008\n"
"v_mfma_f32_16x16x32_bf16 v[192:195], a[212:215], a[144:147], v[192:195]\n"
"v_cndmask_b32 v229, v229, v49, vcc\n"
"v_cndmask_b32 v230, v230, v50, vcc\n"
"v_mfma_f32_16x16x32_bf16 v[200:203], a[212:215], a[148:151], v[200:203]\n"
"ds_read_b64_tr_b16 a[212:213], v10 offset:40960\n"
"ds_read_b64_tr_b16 a[214:215], v10 offset:43008\n"
"v_mfma_f32_16x16x32_bf16 v[196:199], a[216:219], a[144:147], v[196:199]\n"
"v_cndmask_b32 v231, v231, v51, vcc\n"
"s_add_u32 s68, s28, 15040512\n"
"v_mfma_f32_16x16x32_bf16 v[204:207], a[216:219], a[148:151], v[204:207]\n"
"ds_read_b64_tr_b16 a[216:217], v11 offset:40960\n"
"s_waitcnt lgkmcnt(10)\n"
"v_mfma_f32_16x16x32_bf16 v[192:195], a[220:223], a[152:155], v[192:195]\n"
"buffer_load_dwordx4 v27, s[36:39], s66 offen offset:3072 lds\n"
"v_mfma_f32_16x16x32_bf16 v[200:203], a[220:223], a[156:159], v[200:203]\n"
"ds_read_b64_tr_b16 a[218:219], v11 offset:43008\n"
"s_add_u32 m0, s61, 16384\n"
"v_mfma_f32_16x16x32_bf16 v[196:199], a[224:227], a[152:155], v[196:199]\n"
"ds_read_b64_tr_b16 a[220:221], v8 offset:45056\n"
"ds_read_b64_tr_b16 a[222:223], v8 offset:47104\n"
"v_mfma_f32_16x16x32_bf16 v[204:207], a[224:227], a[156:159], v[204:207]\n"
"ds_read_b64_tr_b16 a[224:225], v9 offset:45056\n"
"ds_read_b64_tr_b16 a[226:227], v9 offset:47104\n"
"v_mfma_f32_16x16x32_bf16 v[192:195], a[228:231], a[160:163], v[192:195]\n"
"s_cmp_ge_u32 s59, 0x40000000\n"
"s_cselect_b64 vcc, -1, 0\n"
"v_mfma_f32_16x16x32_bf16 v[200:203], a[228:231], a[164:167], v[200:203]\n"
"buffer_load_dwordx4 v16, s[48:51], s65 offen offset:0 lds\n"
"v_mfma_f32_16x16x32_bf16 v[196:199], a[232:235], a[160:163], v[196:199]\n"
"ds_read_b64_tr_b16 a[228:229], v10 offset:45056\n"
"ds_read_b64_tr_b16 a[230:231], v10 offset:47104\n"
"v_mfma_f32_16x16x32_bf16 v[204:207], a[232:235], a[164:167], v[204:207]\n"
"ds_read_b64_tr_b16 a[232:233], v11 offset:45056\n"
"ds_read_b64_tr_b16 a[234:235], v11 offset:47104\n"
"v_mfma_f32_16x16x32_bf16 v[192:195], a[236:239], a[168:171], v[192:195]\n"
"v_cndmask_b32 v228, v228, v56, vcc\n"
"v_cndmask_b32 v229, v229, v57, vcc\n"
"v_mfma_f32_16x16x32_bf16 v[200:203], a[236:239], a[172:175], v[200:203]\n"
"v_cndmask_b32 v230, v230, v58, vcc\n"
"v_cndmask_b32 v231, v231, v59, vcc\n"
"v_mfma_f32_16x16x32_bf16 v[196:199], a[240:243], a[168:171], v[196:199]\n"
"buffer_load_dwordx4 v17, s[48:51], s65 offen offset:1024 lds\n"
"v_mfma_f32_16x16x32_bf16 v[204:207], a[240:243], a[172:175], v[204:207]\n"
"v_cmp_eq_u32 vcc, 0, v7\n"
"s_add_u32 s66, s66, 16384\n"
"v_mfma_f32_16x16x32_bf16 v[216:219], v[240:243], v[32:35], v[216:219]\n"
"v_cndmask_b32 v4, v224, 0, vcc\n"
"s_add_u32 s69, s57, 15040512\n"
"v_mfma_f32_16x16x32_bf16 v[220:223], v[240:243], v[40:43], v[220:223]\n"
"v_exp_f32 v192, v192\n"
"v_mfma_f32_16x16x32_bf16 v[64:67], a[244:247], v[32:35], v[64:67]\n"
"v_exp_f32 v193, v193\n"
"v_mfma_f32_16x16x32_bf16 v[72:75], a[244:247], v[40:43], v[72:75]\n"
"buffer_load_dwordx4 v18, s[48:51], s65 offen offset:2048 lds\n"
"v_mfma_f32_16x16x32_bf16 v[68:71], a[176:179], v[32:35], v[68:71]\n"
"v_exp_f32 v200, v200\n"
"v_mfma_f32_16x16x32_bf16 v[76:79], a[176:179], v[40:43], v[76:79]\n"
"v_exp_f32 v201, v201\n"
"v_mfma_f32_16x16x32_bf16 v[80:83], a[180:183], v[32:35], v[80:83]\n"
"v_exp_f32 v194, v194\n"
"v_mfma_f32_16x16x32_bf16 v[88:91], a[180:183], v[40:43], v[88:91]\n"
"buffer_load_dwordx4 v19, s[48:51], s65 offen offset:3072 lds\n"
"v_mfma_f32_16x16x32_bf16 v[84:87], a[184:187], v[32:35], v[84:87]\n"
"s_add_u32 m0, s61, 81920\n"
"v_cvt_pk_bf16_f32 v192, v192, v193\n"
"v_mfma_f32_16x16x32_bf16 v[92:95], a[184:187], v[40:43], v[92:95]\n"
"v_exp_f32 v195, v195\n"
"v_mfma_f32_16x16x32_bf16 v[96:99], a[188:191], v[32:35], v[96:99]\n"
"v_exp_f32 v202, v202\n"
"v_mfma_f32_16x16x32_bf16 v[104:107], a[188:191], v[40:43], v[104:107]\n"
"s_waitcnt vmcnt(10)\n"
"s_barrier\n"
"ds_read_b128 a[236:239], v12 offset:0\n"
"v_mfma_f32_16x16x32_bf16 v[100:103], a[192:195], v[32:35], v[100:103]\n"
"buffer_load_dwordx4 v20, s[40:43], s67 offen offset:0 lds\n"
"ds_read_b128 a[240:243], v12 offset:8192\n"
"v_mfma_f32_16x16x32_bf16 v[108:111], a[192:195], v[40:43], v[108:111]\n"
"ds_read_b128 a[176:179], v13 offset:8192\n"
"s_waitcnt lgkmcnt(3)\n"
"v_mfma_f32_16x16x32_bf16 v[112:115], a[196:199], v[32:35], v[112:115]\n"
"ds_read_b128 a[244:247], v13 offset:0\n"
"v_cvt_pk_bf16_f32 v200, v200, v201\n"
"v_mfma_f32_16x16x32_bf16 v[120:123], a[196:199], v[40:43], v[120:123]\n"
"ds_read_b128 a[180:183], v14 offset:0\n"
"v_cvt_pk_bf16_f32 v193, v194, v195\n"
"v_mfma_f32_16x16x32_bf16 v[116:119], a[200:203], v[32:35], v[116:119]\n"
"buffer_load_dwordx4 v21, s[40:43], s67 offen offset:1024 lds\n"
"v_mfma_f32_16x16x32_bf16 v[124:127], a[200:203], v[40:43], v[124:127]\n"
"ds_read_b128 a[184:187], v14 offset:8192\n"
"s_add_u32 s65, s65, 229376\n"
"v_mfma_f32_16x16x32_bf16 v[128:131], a[204:207], v[32:35], v[128:131]\n"
"ds_read_b128 a[188:191], v15 offset:0\n"
"v_mfma_f32_16x16x32_bf16 v[136:139], a[204:207], v[40:43], v[136:139]\n"
"ds_read_b128 a[192:195], v15 offset:8192\n"
"v_mfma_f32_16x16x32_bf16 v[132:135], a[208:211], v[32:35], v[132:135]\n"
"buffer_load_dwordx4 v22, s[40:43], s67 offen offset:2048 lds\n"
"ds_read_b128 a[196:199], v12 offset:256\n"
"v_mfma_f32_16x16x32_bf16 v[140:143], a[208:211], v[40:43], v[140:143]\n"
"v_exp_f32 v203, v203\n"
"v_mfma_f32_16x16x32_bf16 v[144:147], a[212:215], v[32:35], v[144:147]\n"
"ds_read_b128 a[200:203], v12 offset:8448\n"
"v_cvt_pk_bf16_f32 v201, v202, v203\n"
"v_mfma_f32_16x16x32_bf16 v[152:155], a[212:215], v[40:43], v[152:155]\n"
"ds_read_b128 a[204:207], v13 offset:256\n"
"v_mfma_f32_16x16x32_bf16 v[148:151], a[216:219], v[32:35], v[148:151]\n"
"buffer_load_dwordx4 v23, s[40:43], s67 offen offset:3072 lds\n"
"v_mfma_f32_16x16x32_bf16 v[156:159], a[216:219], v[40:43], v[156:159]\n"
"ds_read_b128 a[208:211], v13 offset:8448\n"
"s_add_u32 m0, s61, 147456\n"
"v_mfma_f32_16x16x32_bf16 v[160:163], a[220:223], v[32:35], v[160:163]\n"
"ds_read_b128 a[212:215], v14 offset:256\n"
"s_add_u32 s67, s67, 16384\n"
"v_mfma_f32_16x16x32_bf16 v[168:171], a[220:223], v[40:43], v[168:171]\n"
"ds_read_b128 a[216:219], v14 offset:8448\n"
"v_mfma_f32_16x16x32_bf16 v[164:167], a[224:227], v[32:35], v[164:167]\n"
"ds_read_b128 a[220:223], v15 offset:256\n"
"v_mfma_f32_16x16x32_bf16 v[172:175], a[224:227], v[40:43], v[172:175]\n"
"buffer_load_dwordx4 v24, s[36:39], s66 offen offset:0 lds\n"
"v_mfma_f32_16x16x32_bf16 v[176:179], a[228:231], v[32:35], v[176:179]\n"
"ds_read_b128 a[224:227], v15 offset:8448\n"
"v_mfma_f32_16x16x32_bf16 v[184:187], a[228:231], v[40:43], v[184:187]\n"
"ds_read_b64_tr_b16 a[228:229], v8 offset:0\n"
"ds_read_b64_tr_b16 a[230:231], v8 offset:2048\n"
"v_mfma_f32_16x16x32_bf16 v[180:183], a[232:235], v[32:35], v[180:183]\n"
"v_exp_f32 v196, v196\n"
"v_mfma_f32_16x16x32_bf16 v[188:191], a[232:235], v[40:43], v[188:191]\n"
"ds_read_b64_tr_b16 a[232:233], v9 offset:0\n"
"s_waitcnt lgkmcnt(8)\n"
"v_mfma_f32_16x16x32_bf16 v[32:35], a[236:239], a[112:115], v[208:211]\n"
"buffer_load_dwordx4 v25, s[36:39], s66 offen offset:1024 lds\n"
"v_mfma_f32_16x16x32_bf16 v[40:43], a[236:239], a[116:119], v[212:215]\n"
"ds_read_b64_tr_b16 a[234:235], v9 offset:2048\n"
"ds_read_b64_tr_b16 a[236:237], v10 offset:0\n"
"v_mfma_f32_16x16x32_bf16 v[36:39], a[240:243], a[112:115], v[208:211]\n"
"ds_read_b64_tr_b16 a[238:239], v10 offset:2048\n"
"v_mfma_f32_16x16x32_bf16 v[44:47], a[240:243], a[116:119], v[212:215]\n"
"ds_read_b64_tr_b16 a[240:241], v11 offset:0\n"
"ds_read_b64_tr_b16 a[242:243], v11 offset:2048\n"
"v_mfma_f32_16x16x32_bf16 v[32:35], a[244:247], a[120:123], v[32:35]\n"
"v_exp_f32 v197, v197\n"
"v_mfma_f32_16x16x32_bf16 v[40:43], a[244:247], a[124:127], v[40:43]\n"
"s_waitcnt vmcnt(11)\n"
"buffer_store_dwordx2 v[246:247], v4, s[20:23], s78 offen sc1\n"
"v_mfma_f32_16x16x32_bf16 v[36:39], a[176:179], a[120:123], v[36:39]\n"
"ds_read_b64_tr_b16 a[244:245], v8 offset:4096\n"
"ds_read_b64_tr_b16 a[246:247], v8 offset:6144\n"
"v_mfma_f32_16x16x32_bf16 v[44:47], a[176:179], a[124:127], v[44:47]\n"
"ds_read_b64_tr_b16 a[176:177], v9 offset:4096\n"
"ds_read_b64_tr_b16 a[178:179], v9 offset:6144\n"
"v_mfma_f32_16x16x32_bf16 v[32:35], a[180:183], a[128:131], v[32:35]\n"
"v_exp_f32 v198, v198\n"
"v_mfma_f32_16x16x32_bf16 v[40:43], a[180:183], a[132:135], v[40:43]\n"
"ds_read_b64_tr_b16 a[180:181], v10 offset:4096\n"
"ds_read_b64_tr_b16 a[182:183], v10 offset:6144\n"
"v_mfma_f32_16x16x32_bf16 v[36:39], a[184:187], a[128:131], v[36:39]\n"
"buffer_load_dwordx4 v26, s[36:39], s66 offen offset:2048 lds\n"
"v_mfma_f32_16x16x32_bf16 v[44:47], a[184:187], a[132:135], v[44:47]\n"
"ds_read_b64_tr_b16 a[184:185], v11 offset:4096\n"
"ds_read_b64_tr_b16 a[186:187], v11 offset:6144\n"
"v_mfma_f32_16x16x32_bf16 v[32:35], a[188:191], a[136:139], v[32:35]\n"
"v_exp_f32 v199, v199\n"
"v_mfma_f32_16x16x32_bf16 v[40:43], a[188:191], a[140:143], v[40:43]\n"
"ds_read_b64_tr_b16 a[188:189], v8 offset:8192\n"
"ds_read_b64_tr_b16 a[190:191], v8 offset:10240\n"
"v_mfma_f32_16x16x32_bf16 v[36:39], a[192:195], a[136:139], v[36:39]\n"
"v_exp_f32 v204, v204\n"
"v_mfma_f32_16x16x32_bf16 v[44:47], a[192:195], a[140:143], v[44:47]\n"
"buffer_load_dwordx4 v27, s[36:39], s66 offen offset:3072 lds\n"
"v_mfma_f32_16x16x32_bf16 v[32:35], a[196:199], a[144:147], v[32:35]\n"
"ds_read_b64_tr_b16 a[192:193], v9 offset:8192\n"
"ds_read_b64_tr_b16 a[194:195], v9 offset:10240\n"
"v_mfma_f32_16x16x32_bf16 v[40:43], a[196:199], a[148:151], v[40:43]\n"
"v_exp_f32 v205, v205\n"
"v_mfma_f32_16x16x32_bf16 v[36:39], a[200:203], a[144:147], v[36:39]\n"
"ds_read_b64_tr_b16 a[196:197], v10 offset:8192\n"
"ds_read_b64_tr_b16 a[198:199], v10 offset:10240\n"
"v_mfma_f32_16x16x32_bf16 v[44:47], a[200:203], a[148:151], v[44:47]\n"
"s_add_u32 m0, s61, 32768\n"
"v_cvt_pk_bf16_f32 v194, v196, v197\n"
"v_mfma_f32_16x16x32_bf16 v[32:35], a[204:207], a[152:155], v[32:35]\n"
"buffer_load_dwordx4 v16, s[48:51], s65 offen offset:0 lds\n"
"v_mfma_f32_16x16x32_bf16 v[40:43], a[204:207], a[156:159], v[40:43]\n"
"v_cvt_pk_bf16_f32 v195, v198, v199\n"
"s_waitcnt lgkmcnt(6)\n"
"v_mfma_f32_16x16x32_bf16 v[36:39], a[208:211], a[152:155], v[36:39]\n"
"v_exp_f32 v206, v206\n"
"v_mfma_f32_16x16x32_bf16 v[44:47], a[208:211], a[156:159], v[44:47]\n"
"v_exp_f32 v207, v207\n"
"v_mfma_f32_16x16x32_bf16 v[32:35], a[212:215], a[160:163], v[32:35]\n"
"v_cvt_pk_bf16_f32 v202, v204, v205\n"
"v_cvt_pk_bf16_f32 v203, v206, v207\n"
"v_mfma_f32_16x16x32_bf16 v[40:43], a[212:215], a[164:167], v[40:43]\n"
"buffer_load_dwordx4 v17, s[48:51], s65 offen offset:1024 lds\n"
"v_mfma_f32_16x16x32_bf16 v[36:39], a[216:219], a[160:163], v[36:39]\n"
"ds_read_b64_tr_b16 a[200:201], v11 offset:8192\n"
"ds_read_b64_tr_b16 a[202:203], v11 offset:10240\n"
"v_mfma_f32_16x16x32_bf16 v[44:47], a[216:219], a[164:167], v[44:47]\n"
"ds_read_b64_tr_b16 a[204:205], v8 offset:12288\n"
"ds_read_b64_tr_b16 a[206:207], v8 offset:14336\n"
"v_mfma_f32_16x16x32_bf16 v[32:35], a[220:223], a[168:171], v[32:35]\n"
"ds_read_b64_tr_b16 a[208:209], v9 offset:12288\n"
"ds_read_b64_tr_b16 a[210:211], v9 offset:14336\n"
"v_mfma_f32_16x16x32_bf16 v[40:43], a[220:223], a[172:175], v[40:43]\n"
"ds_read_b64_tr_b16 a[212:213], v10 offset:12288\n"
"ds_read_b64_tr_b16 a[214:215], v10 offset:14336\n"
"v_mfma_f32_16x16x32_bf16 v[36:39], a[224:227], a[168:171], v[36:39]\n"
"buffer_load_dwordx4 v18, s[48:51], s65 offen offset:2048 lds\n"
"v_mfma_f32_16x16x32_bf16 v[44:47], a[224:227], a[172:175], v[44:47]\n"
"ds_read_b64_tr_b16 a[216:217], v11 offset:12288\n"
"ds_read_b64_tr_b16 a[218:219], v11 offset:14336\n"
"v_mfma_f32_16x16x32_bf16 v[216:219], v[240:243], v[192:195], v[216:219]\n"
"v_exp_f32 v32, v32\n"
"v_mfma_f32_16x16x32_bf16 v[220:223], v[240:243], v[200:203], v[220:223]\n"
"v_exp_f32 v33, v33\n"
"v_mfma_f32_16x16x32_bf16 v[64:67], a[228:231], v[192:195], v[64:67]\n"
"s_add_u32 s66, s66, 16384\n"
"v_cvt_pk_bf16_f32 v32, v32, v33\n"
"v_mfma_f32_16x16x32_bf16 v[72:75], a[228:231], v[200:203], v[72:75]\n"
"buffer_load_dwordx4 v19, s[48:51], s65 offen offset:3072 lds\n"
"v_mfma_f32_16x16x32_bf16 v[68:71], a[232:235], v[192:195], v[68:71]\n"
"s_add_u32 m0, s61, 98304\n"
"s_sub_u32 s65, s65, 229248\n"
"v_mfma_f32_16x16x32_bf16 v[76:79], a[232:235], v[200:203], v[76:79]\n"
"v_exp_f32 v40, v40\n"
"v_mfma_f32_16x16x32_bf16 v[80:83], a[236:239], v[192:195], v[80:83]\n"
"v_exp_f32 v41, v41\n"
"v_mfma_f32_16x16x32_bf16 v[88:91], a[236:239], v[200:203], v[88:91]\n"
"v_exp_f32 v34, v34\n"
"v_mfma_f32_16x16x32_bf16 v[84:87], a[240:243], v[192:195], v[84:87]\n"
"v_exp_f32 v35, v35\n"
"v_mfma_f32_16x16x32_bf16 v[92:95], a[240:243], v[200:203], v[92:95]\n"
"v_cvt_pk_bf16_f32 v40, v40, v41\n"
"v_cvt_pk_bf16_f32 v33, v34, v35\n"
"v_mfma_f32_16x16x32_bf16 v[96:99], a[244:247], v[192:195], v[96:99]\n"
"v_exp_f32 v42, v42\n"
"v_mfma_f32_16x16x32_bf16 v[104:107], a[244:247], v[200:203], v[104:107]\n"
"s_waitcnt vmcnt(9)\n"
"s_barrier\n"
"ds_read_b128 a[220:223], v12 offset:16384\n"
"v_mfma_f32_16x16x32_bf16 v[100:103], a[176:179], v[192:195], v[100:103]\n"
"buffer_load_dwordx4 v20, s[40:43], s67 offen offset:0 lds\n"
"ds_read_b128 a[224:227], v12 offset:24576\n"
"v_mfma_f32_16x16x32_bf16 v[108:111], a[176:179], v[200:203], v[108:111]\n"
"ds_read_b128 a[228:231], v13 offset:16384\n"
"v_mfma_f32_16x16x32_bf16 v[112:115], a[180:183], v[192:195], v[112:115]\n"
"ds_read_b128 a[232:235], v13 offset:24576\n"
"v_mfma_f32_16x16x32_bf16 v[120:123], a[180:183], v[200:203], v[120:123]\n"
"ds_read_b128 a[236:239], v14 offset:16384\n"
"v_mfma_f32_16x16x32_bf16 v[116:119], a[184:187], v[192:195], v[116:119]\n"
"ds_read_b128 a[240:243], v14 offset:24576\n"
"v_mfma_f32_16x16x32_bf16 v[124:127], a[184:187], v[200:203], v[124:127]\n"
"ds_read_b128 a[244:247], v15 offset:16384\n"
"s_waitcnt lgkmcnt(7)\n"
"v_mfma_f32_16x16x32_bf16 v[128:131], a[188:191], v[192:195], v[128:131]\n"
"buffer_load_dwordx4 v21, s[40:43], s67 offen offset:1024 lds\n"
"v_mfma_f32_16x16x32_bf16 v[136:139], a[188:191], v[200:203], v[136:139]\n"
"ds_read_b128 a[176:179], v15 offset:24576\n"
"v_mfma_f32_16x16x32_bf16 v[132:135], a[192:195], v[192:195], v[132:135]\n"
"ds_read_b128 a[180:183], v12 offset:16640\n"
"v_mfma_f32_16x16x32_bf16 v[140:143], a[192:195], v[200:203], v[140:143]\n"
"ds_read_b128 a[184:187], v12 offset:24832\n"
"v_mfma_f32_16x16x32_bf16 v[144:147], a[196:199], v[192:195], v[144:147]\n"
"buffer_load_dwordx4 v22, s[40:43], s67 offen offset:2048 lds\n"
"v_mfma_f32_16x16x32_bf16 v[152:155], a[196:199], v[200:203], v[152:155]\n"
"ds_read_b128 a[188:191], v13 offset:16640\n"
"v_mfma_f32_16x16x32_bf16 v[148:151], a[200:203], v[192:195], v[148:151]\n"
"ds_read_b128 a[192:195], v13 offset:24832\n"
"v_mfma_f32_16x16x32_bf16 v[156:159], a[200:203], v[200:203], v[156:159]\n"
"ds_read_b128 a[196:199], v14 offset:16640\n"
"v_mfma_f32_16x16x32_bf16 v[160:163], a[204:207], v[192:195], v[160:163]\n"
"buffer_load_dwordx4 v23, s[40:43], s67 offen offset:3072 lds\n"
"v_mfma_f32_16x16x32_bf16 v[168:171], a[204:207], v[200:203], v[168:171]\n"
"ds_read_b128 a[200:203], v14 offset:24832\n"
"s_add_u32 m0, s61, 114688\n"
"v_mfma_f32_16x16x32_bf16 v[164:167], a[208:211], v[192:195], v[164:167]\n"
"ds_read_b128 a[204:207], v15 offset:16640\n"
"s_add_u32 s67, s67, 16384\n"
"v_mfma_f32_16x16x32_bf16 v[172:175], a[208:211], v[200:203], v[172:175]\n"
"ds_read_b128 a[208:211], v15 offset:24832\n"
"v_mfma_f32_16x16x32_bf16 v[176:179], a[212:215], v[192:195], v[176:179]\n"
"v_exp_f32 v43, v43\n"
"v_mfma_f32_16x16x32_bf16 v[184:187], a[212:215], v[200:203], v[184:187]\n"
"buffer_load_dwordx4 v24, s[36:39], s66 offen offset:0 lds\n"
"v_mfma_f32_16x16x32_bf16 v[180:183], a[216:219], v[192:195], v[180:183]\n"
"ds_read_b64_tr_b16 a[212:213], v8 offset:16384\n"
"ds_read_b64_tr_b16 a[214:215], v8 offset:18432\n"
"v_mfma_f32_16x16x32_bf16 v[188:191], a[216:219], v[200:203], v[188:191]\n"
"ds_read_b64_tr_b16 a[216:217], v9 offset:16384\n"
"s_waitcnt lgkmcnt(8)\n"
"v_mfma_f32_16x16x32_bf16 v[192:195], a[220:223], a[112:115], v[208:211]\n"
"ds_read_b64_tr_b16 a[218:219], v9 offset:18432\n"
"v_cvt_pk_bf16_f32 v41, v42, v43\n"
"v_mfma_f32_16x16x32_bf16 v[200:203], a[220:223], a[116:119], v[212:215]\n"
"ds_read_b64_tr_b16 a[220:221], v10 offset:16384\n"
"ds_read_b64_tr_b16 a[222:223], v10 offset:18432\n"
"v_mfma_f32_16x16x32_bf16 v[196:199], a[224:227], a[112:115], v[208:211]\n"
"buffer_load_dwordx4 v25, s[36:39], s66 offen offset:1024 lds\n"
"v_mfma_f32_16x16x32_bf16 v[204:207], a[224:227], a[116:119], v[212:215]\n"
"ds_read_b64_tr_b16 a[224:225], v11 offset:16384\n"
"ds_read_b64_tr_b16 a[226:227], v11 offset:18432\n"
"v_mfma_f32_16x16x32_bf16 v[192:195], a[228:231], a[120:123], v[192:195]\n"
"v_exp_f32 v36, v36\n"
"v_mfma_f32_16x16x32_bf16 v[200:203], a[228:231], a[124:127], v[200:203]\n"
"ds_read_b64_tr_b16 a[228:229], v8 offset:20480\n"
"ds_read_b64_tr_b16 a[230:231], v8 offset:22528\n"
"v_mfma_f32_16x16x32_bf16 v[196:199], a[232:235], a[120:123], v[196:199]\n"
"v_exp_f32 v37, v37\n"
"v_mfma_f32_16x16x32_bf16 v[204:207], a[232:235], a[124:127], v[204:207]\n"
"buffer_load_dwordx4 v26, s[36:39], s66 offen offset:2048 lds\n"
"ds_read_b64_tr_b16 a[232:233], v9 offset:20480\n"
"ds_read_b64_tr_b16 a[234:235], v9 offset:22528\n"
"v_mfma_f32_16x16x32_bf16 v[192:195], a[236:239], a[128:131], v[192:195]\n"
"v_exp_f32 v38, v38\n"
"v_mfma_f32_16x16x32_bf16 v[200:203], a[236:239], a[132:135], v[200:203]\n"
"ds_read_b64_tr_b16 a[236:237], v10 offset:20480\n"
"ds_read_b64_tr_b16 a[238:239], v10 offset:22528\n"
"v_mfma_f32_16x16x32_bf16 v[196:199], a[240:243], a[128:131], v[196:199]\n"
"v_exp_f32 v39, v39\n"
"v_mfma_f32_16x16x32_bf16 v[204:207], a[240:243], a[132:135], v[204:207]\n"
"ds_read_b64_tr_b16 a[240:241], v11 offset:20480\n"
"ds_read_b64_tr_b16 a[242:243], v11 offset:22528\n"
"v_mfma_f32_16x16x32_bf16 v[192:195], a[244:247], a[136:139], v[192:195]\n"
"buffer_load_dwordx4 v27, s[36:39], s66 offen offset:3072 lds\n"
"v_mfma_f32_16x16x32_bf16 v[200:203], a[244:247], a[140:143], v[200:203]\n"
"ds_read_b64_tr_b16 a[246:247], v8 offset:26624\n"
"ds_read_b64_tr_b16 a[244:245], v8 offset:24576\n"
"v_mfma_f32_16x16x32_bf16 v[196:199], a[176:179], a[136:139], v[196:199]\n"
"v_exp_f32 v44, v44\n"
"v_mfma_f32_16x16x32_bf16 v[204:207], a[176:179], a[140:143], v[204:207]\n"
"ds_read_b64_tr_b16 a[176:177], v9 offset:24576\n"
"ds_read_b64_tr_b16 a[178:179], v9 offset:26624\n"
"v_mfma_f32_16x16x32_bf16 v[192:195], a[180:183], a[144:147], v[192:195]\n"
"s_add_u32 m0, s61, 49152\n"
"v_cvt_pk_bf16_f32 v34, v36, v37\n"
"v_mfma_f32_16x16x32_bf16 v[200:203], a[180:183], a[148:151], v[200:203]\n"
"buffer_load_dwordx4 v16, s[48:51], s65 offen offset:0 lds\n"
"v_mfma_f32_16x16x32_bf16 v[196:199], a[184:187], a[144:147], v[196:199]\n"
"v_exp_f32 v45, v45\n"
"v_mfma_f32_16x16x32_bf16 v[204:207], a[184:187], a[148:151], v[204:207]\n"
"v_exp_f32 v46, v46\n"
"v_mfma_f32_16x16x32_bf16 v[192:195], a[188:191], a[152:155], v[192:195]\n"
"v_exp_f32 v47, v47\n"
"v_mfma_f32_16x16x32_bf16 v[200:203], a[188:191], a[156:159], v[200:203]\n"
"ds_read_b64_tr_b16 a[180:181], v10 offset:24576\n"
"s_waitcnt lgkmcnt(5)\n"
"v_mfma_f32_16x16x32_bf16 v[196:199], a[192:195], a[152:155], v[196:199]\n"
"buffer_load_dwordx4 v17, s[48:51], s65 offen offset:1024 lds\n"
"v_mfma_f32_16x16x32_bf16 v[204:207], a[192:195], a[156:159], v[204:207]\n"
"ds_read_b64_tr_b16 a[182:183], v10 offset:26624\n"
"v_cvt_pk_bf16_f32 v35, v38, v39\n"
"v_mfma_f32_16x16x32_bf16 v[192:195], a[196:199], a[160:163], v[192:195]\n"
"v_cvt_pk_bf16_f32 v42, v44, v45\n"
"v_cvt_pk_bf16_f32 v43, v46, v47\n"
"v_mfma_f32_16x16x32_bf16 v[200:203], a[196:199], a[164:167], v[200:203]\n"
"ds_read_b64_tr_b16 a[184:185], v11 offset:24576\n"
"ds_read_b64_tr_b16 a[186:187], v11 offset:26624\n"
"v_mfma_f32_16x16x32_bf16 v[196:199], a[200:203], a[160:163], v[196:199]\n"
"ds_read_b64_tr_b16 a[188:189], v8 offset:28672\n"
"ds_read_b64_tr_b16 a[190:191], v8 offset:30720\n"
"v_mfma_f32_16x16x32_bf16 v[204:207], a[200:203], a[164:167], v[204:207]\n"
"buffer_load_dwordx4 v18, s[48:51], s65 offen offset:2048 lds\n"
"v_mfma_f32_16x16x32_bf16 v[192:195], a[204:207], a[168:171], v[192:195]\n"
"ds_read_b64_tr_b16 a[192:193], v9 offset:28672\n"
"ds_read_b64_tr_b16 a[194:195], v9 offset:30720\n"
"v_mfma_f32_16x16x32_bf16 v[200:203], a[204:207], a[172:175], v[200:203]\n"
"ds_read_b64_tr_b16 a[196:197], v10 offset:28672\n"
"ds_read_b64_tr_b16 a[198:199], v10 offset:30720\n"
"v_mfma_f32_16x16x32_bf16 v[196:199], a[208:211], a[168:171], v[196:199]\n"
"ds_read_b64_tr_b16 a[200:201], v11 offset:28672\n"
"ds_read_b64_tr_b16 a[202:203], v11 offset:30720\n"
"v_mfma_f32_16x16x32_bf16 v[204:207], a[208:211], a[172:175], v[204:207]\n"
"v_exp_f32 v192, v192\n"
"v_mfma_f32_16x16x32_bf16 v[216:219], v[240:243], v[32:35], v[216:219]\n"
"buffer_load_dwordx4 v19, s[48:51], s65 offen offset:3072 lds\n"
"v_mfma_f32_16x16x32_bf16 v[220:223], v[240:243], v[40:43], v[220:223]\n"
"s_add_u32 m0, s61, 65536\n"
"s_add_u32 s65, s65, 229376\n"
"v_mfma_f32_16x16x32_bf16 v[64:67], a[212:215], v[32:35], v[64:67]\n"
"v_exp_f32 v193, v193\n"
"v_mfma_f32_16x16x32_bf16 v[72:75], a[212:215], v[40:43], v[72:75]\n"
"v_exp_f32 v200, v200\n"
"v_mfma_f32_16x16x32_bf16 v[68:71], a[216:219], v[32:35], v[68:71]\n"
"v_exp_f32 v201, v201\n"
"v_mfma_f32_16x16x32_bf16 v[76:79], a[216:219], v[40:43], v[76:79]\n"
"v_cvt_pk_bf16_f32 v192, v192, v193\n"
"v_cvt_pk_bf16_f32 v200, v200, v201\n"
"v_mfma_f32_16x16x32_bf16 v[80:83], a[220:223], v[32:35], v[80:83]\n"
"v_exp_f32 v194, v194\n"
"v_mfma_f32_16x16x32_bf16 v[88:91], a[220:223], v[40:43], v[88:91]\n"
"v_exp_f32 v195, v195\n"
"v_mfma_f32_16x16x32_bf16 v[84:87], a[224:227], v[32:35], v[84:87]\n"
"v_exp_f32 v202, v202\n"
"v_mfma_f32_16x16x32_bf16 v[92:95], a[224:227], v[40:43], v[92:95]\n"
"v_exp_f32 v203, v203\n"
"v_mfma_f32_16x16x32_bf16 v[96:99], a[228:231], v[32:35], v[96:99]\n"
"v_cvt_pk_bf16_f32 v193, v194, v195\n"
"v_cvt_pk_bf16_f32 v201, v202, v203\n"
"v_mfma_f32_16x16x32_bf16 v[104:107], a[228:231], v[40:43], v[104:107]\n"
"s_waitcnt vmcnt(8)\n"
"s_barrier\n"
"ds_read_b128 a[204:207], v12 offset:32768\n"
"v_mfma_f32_16x16x32_bf16 v[100:103], a[232:235], v[32:35], v[100:103]\n"
"buffer_load_dwordx4 v20, s[40:43], s67 offen offset:0 lds\n"
"ds_read_b128 a[208:211], v12 offset:40960\n"
"v_mfma_f32_16x16x32_bf16 v[108:111], a[232:235], v[40:43], v[108:111]\n"
"ds_read_b128 a[212:215], v13 offset:32768\n"
"s_add_u32 s66, s66, 16384\n"
"v_mfma_f32_16x16x32_bf16 v[112:115], a[236:239], v[32:35], v[112:115]\n"
"ds_read_b128 a[216:219], v13 offset:40960\n"
"v_mfma_f32_16x16x32_bf16 v[120:123], a[236:239], v[40:43], v[120:123]\n"
"ds_read_b128 a[220:223], v14 offset:32768\n"
"v_mfma_f32_16x16x32_bf16 v[116:119], a[240:243], v[32:35], v[116:119]\n"
"buffer_load_dwordx4 v21, s[40:43], s67 offen offset:1024 lds\n"
"v_mfma_f32_16x16x32_bf16 v[124:127], a[240:243], v[40:43], v[124:127]\n"
"ds_read_b128 a[224:227], v14 offset:40960\n"
"s_waitcnt lgkmcnt(6)\n"
"v_mfma_f32_16x16x32_bf16 v[128:131], a[244:247], v[32:35], v[128:131]\n"
"ds_read_b128 a[228:231], v15 offset:32768\n"
"v_mfma_f32_16x16x32_bf16 v[136:139], a[244:247], v[40:43], v[136:139]\n"
"ds_read_b128 a[232:235], v15 offset:40960\n"
"v_mfma_f32_16x16x32_bf16 v[132:135], a[176:179], v[32:35], v[132:135]\n"
"buffer_load_dwordx4 v22, s[40:43], s67 offen offset:2048 lds\n"
"v_mfma_f32_16x16x32_bf16 v[140:143], a[176:179], v[40:43], v[140:143]\n"
"buffer_load_dwordx2 v[238:239], v244, s[20:23], s52 offen offset:0 sc1\n"
"v_mfma_f32_16x16x32_bf16 v[144:147], a[180:183], v[32:35], v[144:147]\n"
"ds_read_b128 a[236:239], v12 offset:33024\n"
"v_mfma_f32_16x16x32_bf16 v[152:155], a[180:183], v[40:43], v[152:155]\n"
"ds_read_b128 a[240:243], v12 offset:41216\n"
"v_mfma_f32_16x16x32_bf16 v[148:151], a[184:187], v[32:35], v[148:151]\n"
"ds_read_b128 a[244:247], v13 offset:33024\n"
"v_mfma_f32_16x16x32_bf16 v[156:159], a[184:187], v[40:43], v[156:159]\n"
"buffer_load_dwordx4 v23, s[40:43], s67 offen offset:3072 lds\n"
"s_add_u32 m0, s61, 0\n"
"ds_read_b128 a[176:179], v13 offset:41216\n"
"v_mfma_f32_16x16x32_bf16 v[160:163], a[188:191], v[32:35], v[160:163]\n"
"ds_read_b128 a[180:183], v14 offset:33024\n"
"s_add_u32 s67, s67, 16384\n"
"v_mfma_f32_16x16x32_bf16 v[168:171], a[188:191], v[40:43], v[168:171]\n"
"ds_read_b128 a[184:187], v14 offset:41216\n"
"v_mfma_f32_16x16x32_bf16 v[164:167], a[192:195], v[32:35], v[164:167]\n"
"ds_read_b128 a[188:191], v15 offset:33024\n"
"v_mfma_f32_16x16x32_bf16 v[172:175], a[192:195], v[40:43], v[172:175]\n"
"buffer_load_dwordx4 v16, s[48:51], s65 offen offset:0 lds\n"
"v_mfma_f32_16x16x32_bf16 v[176:179], a[196:199], v[32:35], v[176:179]\n"
"v_exp_f32 v196, v196\n"
"v_mfma_f32_16x16x32_bf16 v[184:187], a[196:199], v[40:43], v[184:187]\n"
"ds_read_b128 a[192:195], v15 offset:41216\n"
"v_mfma_f32_16x16x32_bf16 v[180:183], a[200:203], v[32:35], v[180:183]\n"
"ds_read_b64_tr_b16 a[196:197], v8 offset:32768\n"
"ds_read_b64_tr_b16 a[198:199], v8 offset:34816\n"
"v_mfma_f32_16x16x32_bf16 v[188:191], a[200:203], v[40:43], v[188:191]\n"
"buffer_load_dwordx4 v17, s[48:51], s65 offen offset:1024 lds\n"
"s_waitcnt lgkmcnt(8)\n"
"v_mfma_f32_16x16x32_bf16 v[32:35], a[204:207], a[112:115], v[208:211]\n"
"ds_read_b64_tr_b16 a[200:201], v9 offset:32768\n"
"ds_read_b64_tr_b16 a[202:203], v9 offset:34816\n"
"v_mfma_f32_16x16x32_bf16 v[40:43], a[204:207], a[116:119], v[212:215]\n"
"ds_read_b64_tr_b16 a[204:205], v10 offset:32768\n"
"ds_read_b64_tr_b16 a[206:207], v10 offset:34816\n"
"v_mfma_f32_16x16x32_bf16 v[36:39], a[208:211], a[112:115], v[208:211]\n"
"v_exp_f32 v197, v197\n"
"v_mfma_f32_16x16x32_bf16 v[44:47], a[208:211], a[116:119], v[212:215]\n"
"ds_read_b64_tr_b16 a[208:209], v11 offset:32768\n"
"ds_read_b64_tr_b16 a[210:211], v11 offset:34816\n"
"v_mfma_f32_16x16x32_bf16 v[32:35], a[212:215], a[120:123], v[32:35]\n"
"buffer_load_dwordx4 v18, s[48:51], s65 offen offset:2048 lds\n"
"v_mfma_f32_16x16x32_bf16 v[40:43], a[212:215], a[124:127], v[40:43]\n"
"ds_read_b64_tr_b16 a[212:213], v8 offset:36864\n"
"ds_read_b64_tr_b16 a[214:215], v8 offset:38912\n"
"v_mfma_f32_16x16x32_bf16 v[36:39], a[216:219], a[120:123], v[36:39]\n"
"v_exp_f32 v198, v198\n"
"v_mfma_f32_16x16x32_bf16 v[44:47], a[216:219], a[124:127], v[44:47]\n"
"ds_read_b64_tr_b16 a[216:217], v9 offset:36864\n"
"ds_read_b64_tr_b16 a[218:219], v9 offset:38912\n"
"v_mfma_f32_16x16x32_bf16 v[32:35], a[220:223], a[128:131], v[32:35]\n"
"v_exp_f32 v199, v199\n"
"v_mfma_f32_16x16x32_bf16 v[40:43], a[220:223], a[132:135], v[40:43]\n"
"buffer_load_dwordx4 v19, s[48:51], s65 offen offset:3072 lds\n"
"v_mfma_f32_16x16x32_bf16 v[36:39], a[224:227], a[128:131], v[36:39]\n"
"ds_read_b64_tr_b16 a[220:221], v10 offset:36864\n"
"ds_read_b64_tr_b16 a[222:223], v10 offset:38912\n"
"v_mfma_f32_16x16x32_bf16 v[44:47], a[224:227], a[132:135], v[44:47]\n"
"ds_read_b64_tr_b16 a[226:227], v11 offset:38912\n"
"ds_read_b64_tr_b16 a[224:225], v11 offset:36864\n"
"v_mfma_f32_16x16x32_bf16 v[32:35], a[228:231], a[136:139], v[32:35]\n"
"v_exp_f32 v204, v204\n"
"v_mfma_f32_16x16x32_bf16 v[40:43], a[228:231], a[140:143], v[40:43]\n"
"s_add_u32 m0, s61, 131072\n"
"ds_read_b64_tr_b16 a[228:229], v8 offset:40960\n"
"v_mfma_f32_16x16x32_bf16 v[36:39], a[232:235], a[136:139], v[36:39]\n"
"ds_read_b64_tr_b16 a[230:231], v8 offset:43008\n"
"v_cvt_pk_bf16_f32 v194, v196, v197\n"
"v_mfma_f32_16x16x32_bf16 v[44:47], a[232:235], a[140:143], v[44:47]\n"
"v_exp_f32 v205, v205\n"
"v_mfma_f32_16x16x32_bf16 v[32:35], a[236:239], a[144:147], v[32:35]\n"
"v_exp_f32 v206, v206\n"
"v_mfma_f32_16x16x32_bf16 v[40:43], a[236:239], a[148:151], v[40:43]\n"
"v_exp_f32 v207, v207\n"
"v_mfma_f32_16x16x32_bf16 v[36:39], a[240:243], a[144:147], v[36:39]\n"
"ds_read_b64_tr_b16 a[232:233], v9 offset:40960\n"
"ds_read_b64_tr_b16 a[234:235], v9 offset:43008\n"
"v_mfma_f32_16x16x32_bf16 v[44:47], a[240:243], a[148:151], v[44:47]\n"
"ds_read_b64_tr_b16 a[236:237], v10 offset:40960\n"
"s_waitcnt lgkmcnt(9)\n"
"v_mfma_f32_16x16x32_bf16 v[32:35], a[244:247], a[152:155], v[32:35]\n"
"ds_read_b64_tr_b16 a[238:239], v10 offset:43008\n"
"v_cvt_pk_bf16_f32 v195, v198, v199\n"
"v_mfma_f32_16x16x32_bf16 v[40:43], a[244:247], a[156:159], v[40:43]\n"
"s_waitcnt vmcnt(5)\n"
"v_cmp_ne_u64 vcc, s[70:71], v[238:239]\n"
"v_mfma_f32_16x16x32_bf16 v[36:39], a[176:179], a[152:155], v[36:39]\n"
"s_sub_u32 s65, s65, 114816\n"
"s_cbranch_vccnz L_spiny_%=\n"
"L_spinrety_%=:\n"
"v_mfma_f32_16x16x32_bf16 v[44:47], a[176:179], a[156:159], v[44:47]\n"
"buffer_load_dwordx4 v16, s[48:51], s65 offen offset:0 lds\n"
"v_mfma_f32_16x16x32_bf16 v[32:35], a[180:183], a[160:163], v[32:35]\n"
"v_cvt_pk_bf16_f32 v202, v204, v205\n"
"v_cvt_pk_bf16_f32 v203, v206, v207\n"
"v_mfma_f32_16x16x32_bf16 v[40:43], a[180:183], a[164:167], v[40:43]\n"
"ds_read_b64_tr_b16 a[240:241], v11 offset:40960\n"
"ds_read_b64_tr_b16 a[242:243], v11 offset:43008\n"
"v_mfma_f32_16x16x32_bf16 v[36:39], a[184:187], a[160:163], v[36:39]\n"
"ds_read_b64_tr_b16 a[244:245], v8 offset:45056\n"
"ds_read_b64_tr_b16 a[246:247], v8 offset:47104\n"
"v_mfma_f32_16x16x32_bf16 v[44:47], a[184:187], a[164:167], v[44:47]\n"
"ds_read_b64_tr_b16 a[176:177], v9 offset:45056\n"
"ds_read_b64_tr_b16 a[178:179], v9 offset:47104\n"
"v_mfma_f32_16x16x32_bf16 v[32:35], a[188:191], a[168:171], v[32:35]\n"
"buffer_load_dwordx4 a[4:7], v1, s[20:23], s74 offen offset:1024 sc1\n"
"v_mfma_f32_16x16x32_bf16 v[40:43], a[188:191], a[172:175], v[40:43]\n"
"ds_read_b64_tr_b16 a[180:181], v10 offset:45056\n"
"ds_read_b64_tr_b16 a[182:183], v10 offset:47104\n"
"v_mfma_f32_16x16x32_bf16 v[36:39], a[192:195], a[168:171], v[36:39]\n"
"ds_read_b64_tr_b16 a[184:185], v11 offset:45056\n"
"ds_read_b64_tr_b16 a[186:187], v11 offset:47104\n"
"v_mfma_f32_16x16x32_bf16 v[44:47], a[192:195], a[172:175], v[44:47]\n"
"v_exp_f32 v32, v32\n"
"v_mfma_f32_16x16x32_bf16 v[216:219], v[240:243], v[192:195], v[216:219]\n"
"v_exp_f32 v33, v33\n"
"v_mfma_f32_16x16x32_bf16 v[220:223], v[240:243], v[200:203], v[220:223]\n"
"buffer_load_dwordx4 v17, s[48:51], s65 offen offset:1024 lds\n"
"v_mfma_f32_16x16x32_bf16 v[64:67], a[196:199], v[192:195], v[64:67]\n"
"v_exp_f32 v40, v40\n"
"v_mfma_f32_16x16x32_bf16 v[72:75], a[196:199], v[200:203], v[72:75]\n"
"v_exp_f32 v41, v41\n"
"v_mfma_f32_16x16x32_bf16 v[68:71], a[200:203], v[192:195], v[68:71]\n"
"v_exp_f32 v34, v34\n"
"v_mfma_f32_16x16x32_bf16 v[76:79], a[200:203], v[200:203], v[76:79]\n"
"buffer_load_dwordx4 v18, s[48:51], s65 offen offset:2048 lds\n"
"v_mfma_f32_16x16x32_bf16 v[80:83], a[204:207], v[192:195], v[80:83]\n"
"v_exp_f32 v35, v35\n"
"v_mfma_f32_16x16x32_bf16 v[88:91], a[204:207], v[200:203], v[88:91]\n"
"v_cvt_pk_bf16_f32 v32, v32, v33\n"
"v_cvt_pk_bf16_f32 v40, v40, v41\n"
"v_mfma_f32_16x16x32_bf16 v[84:87], a[208:211], v[192:195], v[84:87]\n"
"buffer_load_dwordx4 a[8:11], v1, s[20:23], s74 offen offset:2048 sc1\n"
"v_mfma_f32_16x16x32_bf16 v[92:95], a[208:211], v[200:203], v[92:95]\n"
"v_exp_f32 v42, v42\n"
"v_mfma_f32_16x16x32_bf16 v[96:99], a[212:215], v[192:195], v[96:99]\n"
"v_exp_f32 v43, v43\n"
"v_mfma_f32_16x16x32_bf16 v[104:107], a[212:215], v[200:203], v[104:107]\n"
"s_waitcnt vmcnt(9)\n"
"s_barrier\n"
"ds_read_b128 a[188:191], v12 offset:0\n"
"v_mfma_f32_16x16x32_bf16 v[100:103], a[216:219], v[192:195], v[100:103]\n"
"buffer_load_dwordx4 v19, s[48:51], s65 offen offset:3072 lds\n"
"s_add_u32 m0, s61, 81920\n"
"ds_read_b128 a[192:195], v12 offset:8192\n"
"v_mfma_f32_16x16x32_bf16 v[108:111], a[216:219], v[200:203], v[108:111]\n"
"buffer_load_dwordx4 a[12:15], v1, s[20:23], s74 offen offset:3072 sc1\n"
"ds_read_b128 a[196:199], v13 offset:0\n"
"s_waitcnt lgkmcnt(3)\n"
"v_mfma_f32_16x16x32_bf16 v[112:115], a[220:223], v[192:195], v[112:115]\n"
"ds_read_b128 a[200:203], v13 offset:8192\n"
"s_add_u32 s74, s74, s83\n"
"v_mfma_f32_16x16x32_bf16 v[120:123], a[220:223], v[200:203], v[120:123]\n"
"ds_read_b128 a[204:207], v14 offset:0\n"
"v_cvt_pk_bf16_f32 v33, v34, v35\n"
"v_mfma_f32_16x16x32_bf16 v[116:119], a[224:227], v[192:195], v[116:119]\n"
"ds_read_b128 a[208:211], v14 offset:8192\n"
"v_cvt_pk_bf16_f32 v41, v42, v43\n"
"v_mfma_f32_16x16x32_bf16 v[124:127], a[224:227], v[200:203], v[124:127]\n"
"buffer_load_dwordx4 v20, s[40:43], s67 offen offset:0 lds\n"
"v_mfma_f32_16x16x32_bf16 v[128:131], a[228:231], v[192:195], v[128:131]\n"
"buffer_load_dwordx4 a[16:19], v1, s[20:23], s75 offen offset:0 sc1\n"
"ds_read_b128 a[212:215], v15 offset:0\n"
"v_mfma_f32_16x16x32_bf16 v[136:139], a[228:231], v[200:203], v[136:139]\n"
"ds_read_b128 a[216:219], v15 offset:8192\n"
"s_add_u32 s65, s65, 229376\n"
"v_mfma_f32_16x16x32_bf16 v[132:135], a[232:235], v[192:195], v[132:135]\n"
"ds_read_b128 a[220:223], v12 offset:256\n"
"v_mfma_f32_16x16x32_bf16 v[140:143], a[232:235], v[200:203], v[140:143]\n"
"ds_read_b128 a[224:227], v12 offset:8448\n"
"v_mfma_f32_16x16x32_bf16 v[144:147], a[236:239], v[192:195], v[144:147]\n"
"buffer_load_dwordx4 v21, s[40:43], s67 offen offset:1024 lds\n"
"v_mfma_f32_16x16x32_bf16 v[152:155], a[236:239], v[200:203], v[152:155]\n"
"buffer_load_dwordx4 a[20:23], v1, s[20:23], s75 offen offset:1024 sc1\n"
"ds_read_b128 a[228:231], v13 offset:256\n"
"v_mfma_f32_16x16x32_bf16 v[148:151], a[240:243], v[192:195], v[148:151]\n"
"ds_read_b128 a[232:235], v13 offset:8448\n"
"v_mfma_f32_16x16x32_bf16 v[156:159], a[240:243], v[200:203], v[156:159]\n"
"ds_read_b128 a[236:239], v14 offset:256\n"
"v_mfma_f32_16x16x32_bf16 v[160:163], a[244:247], v[192:195], v[160:163]\n"
"ds_read_b128 a[240:243], v14 offset:8448\n"
"v_mfma_f32_16x16x32_bf16 v[168:171], a[244:247], v[200:203], v[168:171]\n"
"buffer_load_dwordx4 v22, s[40:43], s67 offen offset:2048 lds\n"
"ds_read_b128 a[244:247], v15 offset:256\n"
"v_mfma_f32_16x16x32_bf16 v[164:167], a[176:179], v[192:195], v[164:167]\n"
"buffer_load_dwordx4 a[24:27], v1, s[20:23], s75 offen offset:2048 sc1\n"
"v_mfma_f32_16x16x32_bf16 v[172:175], a[176:179], v[200:203], v[172:175]\n"
"ds_read_b128 a[176:179], v15 offset:8448\n"
"v_mfma_f32_16x16x32_bf16 v[176:179], a[180:183], v[192:195], v[176:179]\n"
"v_exp_f32 v36, v36\n"
"v_mfma_f32_16x16x32_bf16 v[184:187], a[180:183], v[200:203], v[184:187]\n"
"ds_read_b64_tr_b16 a[180:181], v8 offset:0\n"
"ds_read_b64_tr_b16 a[182:183], v8 offset:2048\n"
"v_mfma_f32_16x16x32_bf16 v[180:183], a[184:187], v[192:195], v[180:183]\n"
"buffer_load_dwordx4 v23, s[40:43], s67 offen offset:3072 lds\n"
"v_mfma_f32_16x16x32_bf16 v[188:191], a[184:187], v[200:203], v[188:191]\n"
"buffer_load_dwordx4 a[28:31], v1, s[20:23], s75 offen offset:3072 sc1\n"
"s_waitcnt lgkmcnt(7)\n"
"v_mfma_f32_16x16x32_bf16 v[192:195], a[188:191], a[112:115], v[208:211]\n"
"ds_read_b64_tr_b16 a[184:185], v9 offset:0\n"
"ds_read_b64_tr_b16 a[186:187], v9 offset:2048\n"
"v_mfma_f32_16x16x32_bf16 v[200:203], a[188:191], a[116:119], v[212:215]\n"
"s_add_u32 s75, s75, s83\n"
"ds_read_b64_tr_b16 a[188:189], v10 offset:0\n"
"v_mfma_f32_16x16x32_bf16 v[196:199], a[192:195], a[112:115], v[208:211]\n"
"ds_read_b64_tr_b16 a[190:191], v10 offset:2048\n"
"s_sub_u32 s83, 15040512, s83\n"
"v_mfma_f32_16x16x32_bf16 v[204:207], a[192:195], a[116:119], v[212:215]\n"
"buffer_load_dwordx4 a[36:39], v1, s[20:23], s74 offen offset:1024 sc1\n"
"v_mfma_f32_16x16x32_bf16 v[192:195], a[196:199], a[120:123], v[192:195]\n"
"ds_read_b64_tr_b16 a[192:193], v11 offset:0\n"
"ds_read_b64_tr_b16 a[194:195], v11 offset:2048\n"
"v_mfma_f32_16x16x32_bf16 v[200:203], a[196:199], a[124:127], v[200:203]\n"
"ds_read_b64_tr_b16 a[196:197], v8 offset:4096\n"
"ds_read_b64_tr_b16 a[198:199], v8 offset:6144\n"
"v_mfma_f32_16x16x32_bf16 v[196:199], a[200:203], a[120:123], v[196:199]\n"
"v_exp_f32 v37, v37\n"
"v_mfma_f32_16x16x32_bf16 v[204:207], a[200:203], a[124:127], v[204:207]\n"
"buffer_load_dwordx4 a[40:43], v1, s[20:23], s74 offen offset:2048 sc1\n"
"ds_read_b64_tr_b16 a[200:201], v9 offset:4096\n"
"v_mfma_f32_16x16x32_bf16 v[192:195], a[204:207], a[128:131], v[192:195]\n"
"ds_read_b64_tr_b16 a[202:203], v9 offset:6144\n"
"v_cvt_pk_bf16_f32 v34, v36, v37\n"
"v_mfma_f32_16x16x32_bf16 v[200:203], a[204:207], a[132:135], v[200:203]\n"
"ds_read_b64_tr_b16 a[204:205], v10 offset:4096\n"
"ds_read_b64_tr_b16 a[206:207], v10 offset:6144\n"
"v_mfma_f32_16x16x32_bf16 v[196:199], a[208:211], a[128:131], v[196:199]\n"
"v_exp_f32 v38, v38\n"
"v_mfma_f32_16x16x32_bf16 v[204:207], a[208:211], a[132:135], v[204:207]\n"
"buffer_load_dwordx4 a[44:47], v1, s[20:23], s74 offen offset:3072 sc1\n"
"v_mfma_f32_16x16x32_bf16 v[192:195], a[212:215], a[136:139], v[192:195]\n"
"ds_read_b64_tr_b16 a[208:209], v11 offset:4096\n"
"ds_read_b64_tr_b16 a[210:211], v11 offset:6144\n"
"v_mfma_f32_16x16x32_bf16 v[200:203], a[212:215], a[140:143], v[200:203]\n"
"ds_read_b64_tr_b16 a[212:213], v8 offset:8192\n"
"ds_read_b64_tr_b16 a[214:215], v8 offset:10240\n"
"v_mfma_f32_16x16x32_bf16 v[196:199], a[216:219], a[136:139], v[196:199]\n"
"v_exp_f32 v39, v39\n"
"v_mfma_f32_16x16x32_bf16 v[204:207], a[216:219], a[140:143], v[204:207]\n"
"buffer_load_dwordx4 a[48:51], v1, s[20:23], s75 offen offset:0 sc1\n"
"v_mfma_f32_16x16x32_bf16 v[192:195], a[220:223], a[144:147], v[192:195]\n"
"v_exp_f32 v44, v44\n"
"v_mfma_f32_16x16x32_bf16 v[200:203], a[220:223], a[148:151], v[200:203]\n"
"v_exp_f32 v45, v45\n"
"v_mfma_f32_16x16x32_bf16 v[196:199], a[224:227], a[144:147], v[196:199]\n"
"v_exp_f32 v46, v46\n"
"v_mfma_f32_16x16x32_bf16 v[204:207], a[224:227], a[148:151], v[204:207]\n"
"buffer_load_dwordx4 a[52:55], v1, s[20:23], s75 offen offset:1024 sc1\n"
"v_mfma_f32_16x16x32_bf16 v[192:195], a[228:231], a[152:155], v[192:195]\n"
"v_exp_f32 v47, v47\n"
"v_mfma_f32_16x16x32_bf16 v[200:203], a[228:231], a[156:159], v[200:203]\n"
"ds_read_b64_tr_b16 a[216:217], v9 offset:8192\n"
"s_waitcnt lgkmcnt(5)\n"
"v_mfma_f32_16x16x32_bf16 v[196:199], a[232:235], a[152:155], v[196:199]\n"
"ds_read_b64_tr_b16 a[218:219], v9 offset:10240\n"
"ds_read_b64_tr_b16 a[220:221], v10 offset:8192\n"
"v_mfma_f32_16x16x32_bf16 v[204:207], a[232:235], a[156:159], v[204:207]\n"
"buffer_load_dwordx4 a[56:59], v1, s[20:23], s75 offen offset:2048 sc1\n"
"v_mfma_f32_16x16x32_bf16 v[192:195], a[236:239], a[160:163], v[192:195]\n"
"ds_read_b64_tr_b16 a[222:223], v10 offset:10240\n"
"v_cvt_pk_bf16_f32 v35, v38, v39\n"
"v_mfma_f32_16x16x32_bf16 v[200:203], a[236:239], a[164:167], v[200:203]\n"
"v_cvt_pk_bf16_f32 v42, v44, v45\n"
"v_cvt_pk_bf16_f32 v43, v46, v47\n"
"v_mfma_f32_16x16x32_bf16 v[196:199], a[240:243], a[160:163], v[196:199]\n"
"ds_read_b64_tr_b16 a[224:225], v11 offset:8192\n"
"ds_read_b64_tr_b16 a[226:227], v11 offset:10240\n"
"v_mfma_f32_16x16x32_bf16 v[204:207], a[240:243], a[164:167], v[204:207]\n"
"buffer_load_dwordx4 a[60:63], v1, s[20:23], s75 offen offset:3072 sc1\n"
"v_mfma_f32_16x16x32_bf16 v[192:195], a[244:247], a[168:171], v[192:195]\n"
"ds_read_b64_tr_b16 a[228:229], v8 offset:12288\n"
"ds_read_b64_tr_b16 a[230:231], v8 offset:14336\n"
"v_mfma_f32_16x16x32_bf16 v[200:203], a[244:247], a[172:175], v[200:203]\n"
"ds_read_b64_tr_b16 a[232:233], v9 offset:12288\n"
"ds_read_b64_tr_b16 a[234:235], v9 offset:14336\n"
"v_mfma_f32_16x16x32_bf16 v[196:199], a[176:179], a[168:171], v[196:199]\n"
"ds_read_b64_tr_b16 a[236:237], v10 offset:12288\n"
"ds_read_b64_tr_b16 a[238:239], v10 offset:14336\n"
"v_mfma_f32_16x16x32_bf16 v[204:207], a[176:179], a[172:175], v[204:207]\n"
"s_add_u32 m0, s61, 147456\n"
"ds_read_b64_tr_b16 a[240:241], v11 offset:12288\n"
"v_mfma_f32_16x16x32_bf16 v[216:219], v[240:243], v[32:35], v[216:219]\n"
"buffer_load_dwordx4 v16, s[48:51], s65 offen offset:0 lds\n"
"v_exp_f32 v192, v192\n"
"v_mfma_f32_16x16x32_bf16 v[220:223], v[240:243], v[40:43], v[220:223]\n"
"v_exp_f32 v193, v193\n"
"v_mfma_f32_16x16x32_bf16 v[64:67], a[180:183], v[32:35], v[64:67]\n"
"v_exp_f32 v200, v200\n"
"v_mfma_f32_16x16x32_bf16 v[72:75], a[180:183], v[40:43], v[72:75]\n"
"v_exp_f32 v201, v201\n"
"v_mfma_f32_16x16x32_bf16 v[68:71], a[184:187], v[32:35], v[68:71]\n"
"v_exp_f32 v194, v194\n"
"v_mfma_f32_16x16x32_bf16 v[76:79], a[184:187], v[40:43], v[76:79]\n"
"buffer_load_dwordx4 v17, s[48:51], s65 offen offset:1024 lds\n"
"v_mfma_f32_16x16x32_bf16 v[80:83], a[188:191], v[32:35], v[80:83]\n"
"ds_read_b64_tr_b16 a[242:243], v11 offset:14336\n"
"v_cvt_pk_bf16_f32 v192, v192, v193\n"
"v_mfma_f32_16x16x32_bf16 v[88:91], a[188:191], v[40:43], v[88:91]\n"
"v_exp_f32 v195, v195\n"
"v_mfma_f32_16x16x32_bf16 v[84:87], a[192:195], v[32:35], v[84:87]\n"
"v_cvt_pk_bf16_f32 v200, v200, v201\n"
"v_cvt_pk_bf16_f32 v193, v194, v195\n"
"v_mfma_f32_16x16x32_bf16 v[92:95], a[192:195], v[40:43], v[92:95]\n"
"v_exp_f32 v202, v202\n"
"v_mfma_f32_16x16x32_bf16 v[96:99], a[196:199], v[32:35], v[96:99]\n"
"buffer_load_dwordx4 v18, s[48:51], s65 offen offset:2048 lds\n"
"v_exp_f32 v203, v203\n"
"v_mfma_f32_16x16x32_bf16 v[104:107], a[196:199], v[40:43], v[104:107]\n"
"v_exp_f32 v196, v196\n"
"v_mfma_f32_16x16x32_bf16 v[100:103], a[200:203], v[32:35], v[100:103]\n"
"v_exp_f32 v197, v197\n"
"v_mfma_f32_16x16x32_bf16 v[108:111], a[200:203], v[40:43], v[108:111]\n"
"s_waitcnt vmcnt(11)\n"
"s_barrier\n"
"ds_read_b64_tr_b16 a[244:245], v8 offset:16384\n"
"ds_read_b64_tr_b16 a[246:247], v8 offset:18432\n"
"v_mfma_f32_16x16x32_bf16 v[112:115], a[204:207], v[32:35], v[112:115]\n"
"ds_read_b64_tr_b16 a[176:177], v9 offset:16384\n"
"ds_read_b64_tr_b16 a[178:179], v9 offset:18432\n"
"v_exp_f32 v198, v198\n"
"v_mfma_f32_16x16x32_bf16 v[120:123], a[204:207], v[40:43], v[120:123]\n"
"ds_read_b64_tr_b16 a[192:193], v9 offset:20480\n"
"ds_read_b64_tr_b16 a[180:181], v10 offset:16384\n"
"s_waitcnt lgkmcnt(7)\n"
"v_mfma_f32_16x16x32_bf16 v[116:119], a[208:211], v[32:35], v[116:119]\n"
"buffer_load_dwordx4 v19, s[48:51], s65 offen offset:3072 lds\n"
"ds_read_b64_tr_b16 a[182:183], v10 offset:18432\n"
"ds_read_b64_tr_b16 a[194:195], v9 offset:22528\n"
"v_exp_f32 v199, v199\n"
"v_mfma_f32_16x16x32_bf16 v[124:127], a[208:211], v[40:43], v[124:127]\n"
"ds_read_b64_tr_b16 a[184:185], v11 offset:16384\n"
"v_exp_f32 v204, v204\n"
"ds_read_b64_tr_b16 a[186:187], v11 offset:18432\n"
"v_mfma_f32_16x16x32_bf16 v[128:131], a[212:215], v[32:35], v[128:131]\n"
"v_exp_f32 v205, v205\n"
"ds_read_b64_tr_b16 a[188:189], v8 offset:20480\n"
"v_mfma_f32_16x16x32_bf16 v[136:139], a[212:215], v[40:43], v[136:139]\n"
"v_cvt_pk_bf16_f32 v201, v202, v203\n"
"ds_read_b64_tr_b16 a[190:191], v8 offset:22528\n"
"ds_read_b64_tr_b16 a[196:197], v10 offset:20480\n"
"v_mfma_f32_16x16x32_bf16 v[132:135], a[216:219], v[32:35], v[132:135]\n"
"s_add_u32 m0, s61, 65536\n"
"v_cvt_pk_bf16_f32 v194, v196, v197\n"
"ds_read_b64_tr_b16 a[198:199], v10 offset:22528\n"
"v_mfma_f32_16x16x32_bf16 v[140:143], a[216:219], v[40:43], v[140:143]\n"
"v_exp_f32 v206, v206\n"
"v_mfma_f32_16x16x32_bf16 v[144:147], a[220:223], v[32:35], v[144:147]\n"
"v_exp_f32 v207, v207\n"
"v_mfma_f32_16x16x32_bf16 v[152:155], a[220:223], v[40:43], v[152:155]\n"
"v_cvt_pk_bf16_f32 v195, v198, v199\n"
"v_cvt_pk_bf16_f32 v202, v204, v205\n"
"v_mfma_f32_16x16x32_bf16 v[148:151], a[224:227], v[32:35], v[148:151]\n"
"v_cvt_pk_bf16_f32 v203, v206, v207\n"
"ds_read_b64_tr_b16 a[200:201], v11 offset:20480\n"
"v_mfma_f32_16x16x32_bf16 v[156:159], a[224:227], v[40:43], v[156:159]\n"
"ds_read_b64_tr_b16 a[202:203], v11 offset:22528\n"
"ds_read_b64_tr_b16 a[204:205], v8 offset:24576\n"
"v_mfma_f32_16x16x32_bf16 v[216:219], v[240:243], v[192:195], v[216:219]\n"
"ds_read_b64_tr_b16 a[206:207], v8 offset:26624\n"
"ds_read_b64_tr_b16 a[208:209], v9 offset:24576\n"
"v_mfma_f32_16x16x32_bf16 v[220:223], v[240:243], v[200:203], v[220:223]\n"
"ds_read_b64_tr_b16 a[210:211], v9 offset:26624\n"
"ds_read_b64_tr_b16 a[212:213], v10 offset:24576\n"
"v_mfma_f32_16x16x32_bf16 v[160:163], a[228:231], v[32:35], v[160:163]\n"
"ds_read_b64_tr_b16 a[214:215], v10 offset:26624\n"
"ds_read_b64_tr_b16 a[216:217], v11 offset:24576\n"
"v_mfma_f32_16x16x32_bf16 v[168:171], a[228:231], v[40:43], v[168:171]\n"
"ds_read_b64_tr_b16 a[218:219], v11 offset:26624\n"
"s_add_u32 s74, s74, s83\n"
"v_mfma_f32_16x16x32_bf16 v[164:167], a[232:235], v[32:35], v[164:167]\n"
"s_add_u32 s75, s75, s83\n"
"v_add_f32 v225, v216, v220\n"
"v_mfma_f32_16x16x32_bf16 v[172:175], a[232:235], v[40:43], v[172:175]\n"
"v_cmp_ngt_f32 vcc, 0x53800000, v225\n"
"ds_read_b64_tr_b16 a[220:221], v8 offset:28672\n"
"v_mfma_f32_16x16x32_bf16 v[176:179], a[236:239], v[32:35], v[176:179]\n"
"ds_read_b64_tr_b16 a[222:223], v8 offset:30720\n"
"ds_read_b64_tr_b16 a[224:225], v9 offset:28672\n"
"v_mfma_f32_16x16x32_bf16 v[184:187], a[236:239], v[40:43], v[184:187]\n"
"ds_read_b64_tr_b16 a[226:227], v9 offset:30720\n"
"s_waitcnt lgkmcnt(14)\n"
"v_mfma_f32_16x16x32_bf16 v[180:183], a[240:243], v[32:35], v[180:183]\n"
"ds_read_b64_tr_b16 a[228:229], v10 offset:28672\n"
"ds_read_b64_tr_b16 a[230:231], v10 offset:30720\n"
"v_mfma_f32_16x16x32_bf16 v[188:191], a[240:243], v[40:43], v[188:191]\n"
"s_cbranch_vccnz L_slow_%=\n"
"L_slowret_%=:\n"
"v_rcp_f32 v250, v216\n"
"v_mfma_f32_16x16x32_bf16 v[64:67], a[244:247], v[192:195], v[64:67]\n"
"v_and_b32 v225, 15, v7\n"
"v_lshrrev_b32 v224, 4, v7\n"
"v_mfma_f32_16x16x32_bf16 v[72:75], a[244:247], v[200:203], v[72:75]\n"
"v_rcp_f32 v251, v220\n"
"v_mfma_f32_16x16x32_bf16 v[68:71], a[176:179], v[192:195], v[68:71]\n"
"v_lshlrev_b32 v224, 2, v224\n"
"v_sub_u32 v224, v225, v224\n"
"v_mfma_f32_16x16x32_bf16 v[76:79], a[176:179], v[200:203], v[76:79]\n"
"v_mul_f32 v64, v64, v250\n"
"v_mul_f32 v65, v65, v250\n"
"v_mul_f32 v66, v66, v250\n"
"v_mul_f32 v67, v67, v250\n"
"v_cvt_pk_bf16_f32 v64, v64, v65\n"
"ds_read_b64_tr_b16 a[232:233], v11 offset:28672\n"
"ds_read_b64_tr_b16 a[234:235], v11 offset:30720\n"
"v_mfma_f32_16x16x32_bf16 v[80:83], a[180:183], v[192:195], v[80:83]\n"
"v_mov_b32 v226, 0x3f80\n"
"v_cvt_pk_bf16_f32 v65, v66, v67\n"
"v_mul_f32 v72, v72, v251\n"
"v_mul_f32 v73, v73, v251\n"
"v_mul_f32 v74, v74, v251\n"
"v_mul_f32 v75, v75, v251\n"
"v_cvt_pk_bf16_f32 v72, v72, v73\n"
"v_cvt_pk_bf16_f32 v73, v74, v75\n"
"v_mfma_f32_16x16x32_bf16 v[88:91], a[180:183], v[200:203], v[88:91]\n"
"v_cmp_eq_u32 vcc, 0, v224\n"
"v_mul_f32 v68, v68, v250\n"
"v_mul_f32 v69, v69, v250\n"
"v_mul_f32 v70, v70, v250\n"
"v_mul_f32 v71, v71, v250\n"
"v_cvt_pk_bf16_f32 v66, v68, v69\n"
"v_cvt_pk_bf16_f32 v67, v70, v71\n"
"ds_write_b128 v2, v[64:67] offset:0\n"
"v_mfma_f32_16x16x32_bf16 v[84:87], a[184:187], v[192:195], v[84:87]\n"
"v_mul_f32 v76, v76, v251\n"
"v_mul_f32 v77, v77, v251\n"
"v_mul_f32 v78, v78, v251\n"
"v_mul_f32 v79, v79, v251\n"
"v_cvt_pk_bf16_f32 v74, v76, v77\n"
"v_cvt_pk_bf16_f32 v75, v78, v79\n"
"v_mfma_f32_16x16x32_bf16 v[92:95], a[184:187], v[200:203], v[92:95]\n"
"v_cndmask_b32 v234, 0, v226, vcc\n"
"ds_write_b128 v2, v[72:75] offset:1024\n"
"v_mul_f32 v80, v80, v250\n"
"v_mul_f32 v81, v81, v250\n"
"v_mul_f32 v82, v82, v250\n"
"v_mul_f32 v83, v83, v250\n"
"v_cvt_pk_bf16_f32 v80, v80, v81\n"
"v_mfma_f32_16x16x32_bf16 v[96:99], a[188:191], v[192:195], v[96:99]\n"
"v_cvt_pk_bf16_f32 v81, v82, v83\n"
"v_mul_f32 v88, v88, v251\n"
"v_mul_f32 v89, v89, v251\n"
"v_mul_f32 v90, v90, v251\n"
"v_mul_f32 v91, v91, v251\n"
"v_cvt_pk_bf16_f32 v88, v88, v89\n"
"v_cvt_pk_bf16_f32 v89, v90, v91\n"
"v_mfma_f32_16x16x32_bf16 v[104:107], a[188:191], v[200:203], v[104:107]\n"
"v_mov_b32 v227, 0x3f800000\n"
"v_cmp_eq_u32 vcc, 1, v224\n"
"v_mul_f32 v84, v84, v250\n"
"v_mul_f32 v85, v85, v250\n"
"v_mul_f32 v86, v86, v250\n"
"v_mul_f32 v87, v87, v250\n"
"v_cvt_pk_bf16_f32 v82, v84, v85\n"
"v_cvt_pk_bf16_f32 v83, v86, v87\n"
"ds_write_b128 v2, v[80:83] offset:4096\n"
"v_mfma_f32_16x16x32_bf16 v[100:103], a[192:195], v[192:195], v[100:103]\n"
"v_mul_f32 v92, v92, v251\n"
"v_mul_f32 v93, v93, v251\n"
"v_mul_f32 v94, v94, v251\n"
"v_mul_f32 v95, v95, v251\n"
"v_cvt_pk_bf16_f32 v90, v92, v93\n"
"v_cvt_pk_bf16_f32 v91, v94, v95\n"
"v_mfma_f32_16x16x32_bf16 v[108:111], a[192:195], v[200:203], v[108:111]\n"
"ds_write_b128 v2, v[88:91] offset:5120\n"
"v_mul_f32 v96, v96, v250\n"
"v_mul_f32 v97, v97, v250\n"
"v_mul_f32 v98, v98, v250\n"
"v_mul_f32 v99, v99, v250\n"
"v_cvt_pk_bf16_f32 v96, v96, v97\n"
"v_cvt_pk_bf16_f32 v97, v98, v99\n"
"v_mfma_f32_16x16x32_bf16 v[112:115], a[196:199], v[192:195], v[112:115]\n"
"v_cndmask_b32 v234, v234, v227, vcc\n"
"v_mul_f32 v104, v104, v251\n"
"v_mul_f32 v105, v105, v251\n"
"v_mul_f32 v106, v106, v251\n"
"v_mul_f32 v107, v107, v251\n"
"v_cvt_pk_bf16_f32 v104, v104, v105\n"
"v_cvt_pk_bf16_f32 v105, v106, v107\n"
"v_mfma_f32_16x16x32_bf16 v[120:123], a[196:199], v[200:203], v[120:123]\n"
"v_mul_f32 v100, v100, v250\n"
"v_mul_f32 v101, v101, v250\n"
"v_mul_f32 v102, v102, v250\n"
"v_mul_f32 v103, v103, v250\n"
"v_cvt_pk_bf16_f32 v98, v100, v101\n"
"v_cvt_pk_bf16_f32 v99, v102, v103\n"
"ds_write_b128 v2, v[96:99] offset:8192\n"
"s_waitcnt lgkmcnt(5)\n"
"v_mfma_f32_16x16x32_bf16 v[116:119], a[200:203], v[192:195], v[116:119]\n"
"v_cmp_eq_u32 vcc, 2, v224\n"
"v_mul_f32 v108, v108, v251\n"
"v_mul_f32 v109, v109, v251\n"
"v_mul_f32 v110, v110, v251\n"
"v_mul_f32 v111, v111, v251\n"
"v_cvt_pk_bf16_f32 v106, v108, v109\n"
"v_cvt_pk_bf16_f32 v107, v110, v111\n"
"v_mfma_f32_16x16x32_bf16 v[124:127], a[200:203], v[200:203], v[124:127]\n"
"ds_write_b128 v2, v[104:107] offset:9216\n"
"v_mul_f32 v112, v112, v250\n"
"v_mul_f32 v113, v113, v250\n"
"v_mul_f32 v114, v114, v250\n"
"v_mul_f32 v115, v115, v250\n"
"v_cvt_pk_bf16_f32 v112, v112, v113\n"
"v_cvt_pk_bf16_f32 v113, v114, v115\n"
"v_mfma_f32_16x16x32_bf16 v[128:131], a[204:207], v[192:195], v[128:131]\n"
"v_cndmask_b32 v235, 0, v226, vcc\n"
"v_mul_f32 v120, v120, v251\n"
"v_mul_f32 v121, v121, v251\n"
"v_mul_f32 v122, v122, v251\n"
"v_mul_f32 v123, v123, v251\n"
"v_cvt_pk_bf16_f32 v120, v120, v121\n"
"v_cvt_pk_bf16_f32 v121, v122, v123\n"
"v_mfma_f32_16x16x32_bf16 v[136:139], a[204:207], v[200:203], v[136:139]\n"
"v_mul_f32 v116, v116, v250\n"
"v_mul_f32 v117, v117, v250\n"
"v_mul_f32 v118, v118, v250\n"
"v_mul_f32 v119, v119, v250\n"
"v_cvt_pk_bf16_f32 v114, v116, v117\n"
"v_cvt_pk_bf16_f32 v115, v118, v119\n"
"ds_write_b128 v2, v[112:115] offset:12288\n"
"v_mfma_f32_16x16x32_bf16 v[132:135], a[208:211], v[192:195], v[132:135]\n"
"v_cmp_eq_u32 vcc, 3, v224\n"
"v_mul_f32 v124, v124, v251\n"
"v_mul_f32 v125, v125, v251\n"
"v_mul_f32 v126, v126, v251\n"
"v_mul_f32 v127, v127, v251\n"
"v_cvt_pk_bf16_f32 v122, v124, v125\n"
"v_cvt_pk_bf16_f32 v123, v126, v127\n"
"v_mfma_f32_16x16x32_bf16 v[140:143], a[208:211], v[200:203], v[140:143]\n"
"ds_write_b128 v2, v[120:123] offset:13312\n"
"v_mov_b32 v236, 0\n"
"v_mov_b32 v237, 0\n"
"v_mfma_f32_16x16x32_bf16 v[144:147], a[212:215], v[192:195], v[144:147]\n"
"v_mov_b32 v232, 0\n"
"v_cndmask_b32 v235, v235, v227, vcc\n"
"v_mul_f32 v137, v137, v251\n"
"v_mfma_f32_16x16x32_bf16 v[152:155], a[212:215], v[200:203], v[152:155]\n"
"v_mov_b32 v233, 0\n"
"v_mul_f32 v128, v128, v250\n"
"v_mul_f32 v138, v138, v251\n"
"v_mfma_f32_16x16x32_bf16 v[148:151], a[216:219], v[192:195], v[148:151]\n"
"v_mul_f32 v129, v129, v250\n"
"v_mul_f32 v136, v136, v251\n"
"v_mul_f32 v139, v139, v251\n"
"v_cvt_pk_bf16_f32 v136, v136, v137\n"
"v_mfma_f32_16x16x32_bf16 v[156:159], a[216:219], v[200:203], v[156:159]\n"
"v_mul_f32 v130, v130, v250\n"
"v_mul_f32 v144, v144, v250\n"
"v_mfma_f32_16x16x32_bf16 v[160:163], a[220:223], v[192:195], v[160:163]\n"
"v_mul_f32 v131, v131, v250\n"
"v_mul_f32 v145, v145, v250\n"
"v_cvt_pk_bf16_f32 v144, v144, v145\n"
"v_mfma_f32_16x16x32_bf16 v[168:171], a[220:223], v[200:203], v[168:171]\n"
"v_cvt_pk_bf16_f32 v128, v128, v129\n"
"v_mul_f32 v152, v152, v251\n"
"v_mul_f32 v154, v154, v251\n"
"v_mul_f32 v155, v155, v251\n"
"v_mfma_f32_16x16x32_bf16 v[164:167], a[224:227], v[192:195], v[164:167]\n"
"v_mul_f32 v132, v132, v250\n"
"v_mul_f32 v146, v146, v250\n"
"v_mul_f32 v153, v153, v251\n"
"v_cvt_pk_bf16_f32 v152, v152, v153\n"
"v_mfma_f32_16x16x32_bf16 v[172:175], a[224:227], v[200:203], v[172:175]\n"
"v_mul_f32 v133, v133, v250\n"
"v_mul_f32 v134, v134, v250\n"
"v_mul_f32 v147, v147, v250\n"
"v_mfma_f32_16x16x32_bf16 v[176:179], a[228:231], v[192:195], v[176:179]\n"
"v_mul_f32 v135, v135, v250\n"
"v_cvt_pk_bf16_f32 v129, v130, v131\n"
"v_mul_f32 v169, v169, v251\n"
"v_mfma_f32_16x16x32_bf16 v[184:187], a[228:231], v[200:203], v[184:187]\n"
"v_mul_f32 v162, v162, v250\n"
"s_waitcnt lgkmcnt(0)\n"
"s_barrier\n"
"ds_read_b128 a[156:159], v245 offset:23552\n"
"v_mfma_f32_16x16x32_bf16 v[180:183], a[232:235], v[192:195], v[180:183]\n"
"ds_read_b128 a[160:163], v245 offset:24576\n"
"v_mul_f32 v160, v160, v250\n"
"v_mul_f32 v163, v163, v250\n"
"v_mfma_f32_16x16x32_bf16 v[188:191], a[232:235], v[200:203], v[188:191]\n"
"ds_read_b128 a[164:167], v245 offset:25600\n"
"v_mul_f32 v161, v161, v250\n"
"v_cvt_pk_bf16_f32 v160, v160, v161\n"
"v_mfma_f32_16x16x32_bf16 v[192:195], v[234:237], v[252:255], 0\n"
"ds_read_b128 a[168:171], v245 offset:26624\n"
"v_mul_f32 v140, v140, v251\n"
"v_mfma_f32_16x16x32_bf16 v[196:199], v[232:235], v[252:255], 0\n"
"ds_read_b128 a[172:175], v245 offset:27648\n"
"v_mul_f32 v141, v141, v251\n"
"v_mfma_f32_16x16x32_bf16 v[192:195], v[234:237], a[4:7], v[192:195]\n"
"ds_read_b128 a[176:179], v245 offset:28672\n"
"v_mul_f32 v142, v142, v251\n"
"v_mfma_f32_16x16x32_bf16 v[196:199], v[232:235], a[4:7], v[196:199]\n"
"ds_read_b128 a[92:95], v245 offset:7168\n"
"v_mul_f32 v143, v143, v251\n"
"v_mfma_f32_16x16x32_bf16 v[192:195], v[234:237], a[8:11], v[192:195]\n"
"ds_read_b128 a[180:183], v245 offset:29696\n"
"v_cvt_pk_bf16_f32 v137, v138, v139\n"
"v_mul_f32 v148, v148, v250\n"
"v_mfma_f32_16x16x32_bf16 v[196:199], v[232:235], a[8:11], v[196:199]\n"
"ds_read_b128 a[184:187], v245 offset:30720\n"
"v_mul_f32 v149, v149, v250\n"
"v_mul_f32 v150, v150, v250\n"
"v_mfma_f32_16x16x32_bf16 v[192:195], v[234:237], a[12:15], v[192:195]\n"
"ds_read_b128 a[96:99], v245 offset:8192\n"
"v_mul_f32 v151, v151, v250\n"
"v_cvt_pk_bf16_f32 v145, v146, v147\n"
"v_mul_f32 v176, v176, v250\n"
"v_mfma_f32_16x16x32_bf16 v[196:199], v[232:235], a[12:15], v[196:199]\n"
"ds_read_b128 a[188:191], v245 offset:31744\n"
"v_mul_f32 v177, v177, v250\n"
"v_mfma_f32_16x16x32_bf16 v[192:195], v[234:237], a[16:19], v[192:195]\n"
"ds_read_b128 a[64:67], v245 offset:0\n"
"v_mul_f32 v156, v156, v251\n"
"v_mfma_f32_16x16x32_bf16 v[196:199], v[232:235], a[16:19], v[196:199]\n"
"ds_read_b64_tr_b16 v[40:41], v30 offset:24576\n"
"ds_read_b64_tr_b16 v[32:33], v30 offset:16384\n"
"v_mul_f32 v157, v157, v251\n"
"v_mfma_f32_16x16x32_bf16 v[192:195], v[234:237], a[20:23], v[192:195]\n"
"ds_read_b64_tr_b16 v[42:43], v30 offset:26624\n"
"ds_read_b64_tr_b16 v[34:35], v30 offset:18432\n"
"v_cvt_pk_bf16_f32 v153, v154, v155\n"
"v_mfma_f32_16x16x32_bf16 v[196:199], v[232:235], a[20:23], v[196:199]\n"
"ds_read_b128 a[100:103], v245 offset:9216\n"
"v_mul_f32 v168, v168, v251\n"
"v_mul_f32 v170, v170, v251\n"
"v_mul_f32 v171, v171, v251\n"
"v_mfma_f32_16x16x32_bf16 v[192:195], v[234:237], a[24:27], v[192:195]\n"
"ds_read_b128 a[68:71], v245 offset:1024\n"
"v_mul_f32 v158, v158, v251\n"
"v_cvt_pk_bf16_f32 v168, v168, v169\n"
"v_mfma_f32_16x16x32_bf16 v[196:199], v[232:235], a[24:27], v[196:199]\n"
"ds_read_b128 a[104:107], v245 offset:10240\n"
"v_mul_f32 v159, v159, v251\n"
"v_mul_f32 v164, v164, v250\n"
"s_waitcnt vmcnt(11)\n"
"v_mfma_f32_16x16x32_bf16 v[192:195], v[234:237], a[28:31], v[192:195]\n"
"ds_read_b128 a[72:75], v245 offset:2048\n"
"v_mul_f32 v165, v165, v250\n"
"v_cvt_pk_bf16_f32 v161, v162, v163\n"
"v_mfma_f32_16x16x32_bf16 v[196:199], v[232:235], a[28:31], v[196:199]\n"
"ds_read_b128 a[76:79], v245 offset:3072\n"
"v_mul_f32 v166, v166, v250\n"
"v_mfma_f32_16x16x32_bf16 v[200:203], v[234:237], v[228:231], 0\n"
"ds_read_b64_tr_b16 v[48:49], v30 offset:32768\n"
"ds_read_b64_tr_b16 v[50:51], v30 offset:34816\n"
"v_mul_f32 v167, v167, v250\n"
"v_mfma_f32_16x16x32_bf16 v[204:207], v[232:235], v[228:231], 0\n"
"ds_read_b128 a[80:83], v245 offset:4096\n"
"v_mul_f32 v178, v178, v250\n"
"v_mul_f32 v179, v179, v250\n"
"s_waitcnt vmcnt(10)\n"
"v_mfma_f32_16x16x32_bf16 v[200:203], v[234:237], a[36:39], v[200:203]\n"
"ds_read_b128 a[108:111], v245 offset:11264\n"
"v_cvt_pk_bf16_f32 v176, v176, v177\n"
"v_mfma_f32_16x16x32_bf16 v[204:207], v[232:235], a[36:39], v[204:207]\n"
"ds_read_b128 a[112:115], v245 offset:12288\n"
"s_waitcnt vmcnt(9)\n"
"v_mfma_f32_16x16x32_bf16 v[200:203], v[234:237], a[40:43], v[200:203]\n"
"ds_read_b64_tr_b16 v[36:37], v30 offset:20480\n"
"ds_read_b64_tr_b16 v[38:39], v30 offset:22528\n"
"v_mul_f32 v184, v184, v251\n"
"v_mul_f32 v185, v185, v251\n"
"v_mfma_f32_16x16x32_bf16 v[204:207], v[232:235], a[40:43], v[204:207]\n"
"ds_read_b64_tr_b16 v[44:45], v30 offset:28672\n"
"ds_read_b64_tr_b16 v[46:47], v30 offset:30720\n"
"v_cvt_pk_bf16_f32 v130, v132, v133\n"
"v_cvt_pk_bf16_f32 v131, v134, v135\n"
"s_waitcnt vmcnt(8)\n"
"v_mfma_f32_16x16x32_bf16 v[200:203], v[234:237], a[44:47], v[200:203]\n"
"ds_read_b128 a[84:87], v245 offset:5120\n"
"v_cvt_pk_bf16_f32 v138, v140, v141\n"
"v_cvt_pk_bf16_f32 v139, v142, v143\n"
"v_mfma_f32_16x16x32_bf16 v[204:207], v[232:235], a[44:47], v[204:207]\n"
"ds_read_b128 a[116:119], v245 offset:13312\n"
"ds_read_b128 a[88:91], v245 offset:6144\n"
"ds_read_b128 a[120:123], v245 offset:14336\n"
"ds_read_b128 a[124:127], v245 offset:15360\n"
"ds_read_b128 a[128:131], v245 offset:16384\n"
"ds_read_b128 a[132:135], v245 offset:17408\n"
"ds_read_b128 a[136:139], v245 offset:18432\n"
"ds_read_b128 a[140:143], v245 offset:19456\n"
"ds_read_b128 a[144:147], v245 offset:20480\n"
"ds_read_b128 a[148:151], v245 offset:21504\n"
"ds_read_b128 a[152:155], v245 offset:22528\n"
"v_cvt_pk_bf16_f32 v146, v148, v149\n"
"v_cvt_pk_bf16_f32 v147, v150, v151\n"
"s_waitcnt vmcnt(7)\n"
"v_mfma_f32_16x16x32_bf16 v[200:203], v[234:237], a[48:51], v[200:203]\n"
"v_mul_f32 v172, v172, v251\n"
"v_mul_f32 v173, v173, v251\n"
"v_mfma_f32_16x16x32_bf16 v[204:207], v[232:235], a[48:51], v[204:207]\n"
"v_mul_f32 v174, v174, v251\n"
"v_mul_f32 v175, v175, v251\n"
"s_waitcnt vmcnt(6)\n"
"v_mfma_f32_16x16x32_bf16 v[200:203], v[234:237], a[52:55], v[200:203]\n"
"v_cvt_pk_bf16_f32 v154, v156, v157\n"
"v_cvt_pk_bf16_f32 v169, v170, v171\n"
"v_mfma_f32_16x16x32_bf16 v[204:207], v[232:235], a[52:55], v[204:207]\n"
"v_cvt_pk_bf16_f32 v155, v158, v159\n"
"v_cvt_pk_bf16_f32 v162, v164, v165\n"
"s_waitcnt vmcnt(5)\n"
"v_mfma_f32_16x16x32_bf16 v[200:203], v[234:237], a[56:59], v[200:203]\n"
"v_cvt_pk_bf16_f32 v163, v166, v167\n"
"v_mul_f32 v180, v180, v250\n"
"v_mfma_f32_16x16x32_bf16 v[204:207], v[232:235], a[56:59], v[204:207]\n"
"ds_read_b64_tr_b16 v[52:53], v30 offset:36864\n"
"s_waitcnt vmcnt(4)\n"
"v_mfma_f32_16x16x32_bf16 v[200:203], v[234:237], a[60:63], v[200:203]\n"
"ds_read_b64_tr_b16 v[54:55], v30 offset:38912\n"
"ds_read_b64_tr_b16 v[56:57], v30 offset:40960\n"
"v_mul_f32 v181, v181, v250\n"
"v_mfma_f32_16x16x32_bf16 v[204:207], v[232:235], a[60:63], v[204:207]\n"
"ds_read_b64_tr_b16 v[58:59], v30 offset:43008\n"
"ds_read_b64_tr_b16 v[60:61], v30 offset:45056\n"
"v_cvt_pk_bf16_f32 v177, v178, v179\n"
"v_mul_f32 v186, v186, v251\n"
"s_waitcnt lgkmcnt(15)\n"
"v_mfma_f32_16x16x32_bf16 v[64:67], v[32:35], a[64:67], 0\n"
"ds_read_b64_tr_b16 v[62:63], v30 offset:47104\n"
"v_mul_f32 v187, v187, v251\n"
"v_cvt_pk_bf16_f32 v184, v184, v185\n"
"v_mfma_f32_16x16x32_bf16 v[68:71], v[32:35], a[68:71], 0\n"
"v_mul_f32 v182, v182, v250\n"
"v_mul_f32 v183, v183, v250\n"
"s_waitcnt lgkmcnt(6)\n"
"s_barrier\n"
"ds_write_b128 v2, v[128:131] offset:0\n"
"v_mfma_f32_16x16x32_bf16 v[72:75], v[32:35], a[72:75], 0\n"
"v_cvt_pk_bf16_f32 v170, v172, v173\n"
"v_cvt_pk_bf16_f32 v171, v174, v175\n"
"v_mfma_f32_16x16x32_bf16 v[76:79], v[32:35], a[76:79], 0\n"
"ds_write_b128 v2, v[136:139] offset:1024\n"
"s_sub_u32 s65, s65, 229248\n"
"v_mfma_f32_16x16x32_bf16 v[64:67], v[36:39], a[80:83], v[64:67]\n"
"buffer_load_dwordx4 v16, s[48:51], s65 offen offset:0 lds\n"
"v_mul_f32 v188, v188, v251\n"
"v_mfma_f32_16x16x32_bf16 v[68:71], v[36:39], a[84:87], v[68:71]\n"
"v_mul_f32 v189, v189, v251\n"
"v_cvt_pk_bf16_f32 v178, v180, v181\n"
"v_mfma_f32_16x16x32_bf16 v[72:75], v[36:39], a[88:91], v[72:75]\n"
"ds_write_b128 v2, v[144:147] offset:4096\n"
"v_cvt_pk_bf16_f32 v185, v186, v187\n"
"v_mfma_f32_16x16x32_bf16 v[76:79], v[36:39], a[92:95], v[76:79]\n"
"v_cvt_pk_bf16_f32 v179, v182, v183\n"
"v_mul_f32 v190, v190, v251\n"
"v_mfma_f32_16x16x32_bf16 v[64:67], v[40:43], a[96:99], v[64:67]\n"
"buffer_load_dwordx4 v17, s[48:51], s65 offen offset:1024 lds\n"
"v_mul_f32 v191, v191, v251\n"
"v_mfma_f32_16x16x32_bf16 v[68:71], v[40:43], a[100:103], v[68:71]\n"
"ds_write_b128 v2, v[152:155] offset:5120\n"
"s_barrier\n"
"v_mfma_f32_16x16x32_bf16 v[72:75], v[40:43], a[104:107], v[72:75]\n"
"ds_read_b64_tr_b16 v[32:33], v30 offset:49152\n"
"ds_read_b64_tr_b16 v[34:35], v30 offset:51200\n"
"v_cvt_pk_bf16_f32 v186, v188, v189\n"
"v_mfma_f32_16x16x32_bf16 v[76:79], v[40:43], a[108:111], v[76:79]\n"
"ds_read_b64_tr_b16 v[38:39], v30 offset:55296\n"
"v_cvt_pk_bf16_f32 v187, v190, v191\n"
"ds_read_b64_tr_b16 v[36:37], v30 offset:53248\n"
"v_mfma_f32_16x16x32_bf16 v[64:67], v[44:47], a[112:115], v[64:67]\n"
"buffer_load_dwordx4 v18, s[48:51], s65 offen offset:2048 lds\n"
"v_mfma_f32_16x16x32_bf16 v[68:71], v[44:47], a[116:119], v[68:71]\n"
"ds_read_b64_tr_b16 v[40:41], v30 offset:57344\n"
"v_accvgpr_read_b32 v164, a252\n"
"v_mfma_f32_16x16x32_bf16 v[72:75], v[44:47], a[120:123], v[72:75]\n"
"ds_write_b128 v2, v[160:163] offset:8192\n"
"v_accvgpr_read_b32 v160, a248\n"
"v_mfma_f32_16x16x32_bf16 v[76:79], v[44:47], a[124:127], v[76:79]\n"
"ds_write_b128 v2, v[168:171] offset:9216\n"
"ds_write_b128 v2, v[176:179] offset:12288\n"
"ds_write_b128 v2, v[184:187] offset:13312\n"
"v_mfma_f32_16x16x32_bf16 v[64:67], v[48:51], a[128:131], v[64:67]\n"
"buffer_load_dwordx4 v19, s[48:51], s65 offen offset:3072 lds\n"
"v_mfma_f32_16x16x32_bf16 v[68:71], v[48:51], a[132:135], v[68:71]\n"
"s_add_u32 s65, s65, 229376\n"
"s_add_u32 m0, s61, 81920\n"
"v_mfma_f32_16x16x32_bf16 v[72:75], v[48:51], a[136:139], v[72:75]\n"
"v_accvgpr_read_b32 v161, a249\n"
"v_accvgpr_read_b32 v162, a250\n"
"v_mfma_f32_16x16x32_bf16 v[76:79], v[48:51], a[140:143], v[76:79]\n"
"buffer_load_dwordx4 v16, s[48:51], s65 offen offset:0 lds\n"
"s_waitcnt lgkmcnt(9)\n"
"v_mfma_f32_16x16x32_bf16 v[64:67], v[52:55], a[144:147], v[64:67]\n"
"v_accvgpr_read_b32 v163, a251\n"
"v_accvgpr_read_b32 v165, a253\n"
"v_mfma_f32_16x16x32_bf16 v[68:71], v[52:55], a[148:151], v[68:71]\n"
"v_accvgpr_read_b32 v166, a254\n"
"v_accvgpr_read_b32 v167, a255\n"
"v_mfma_f32_16x16x32_bf16 v[72:75], v[52:55], a[152:155], v[72:75]\n"
"buffer_load_dwordx4 v17, s[48:51], s65 offen offset:1024 lds\n"
"v_mfma_f32_16x16x32_bf16 v[76:79], v[52:55], a[156:159], v[76:79]\n"
"s_cmp_ge_u32 s28, 0x40000000\n"
"s_cselect_b64 vcc, -1, 0\n"
"v_mfma_f32_16x16x32_bf16 v[64:67], v[56:59], a[160:163], v[64:67]\n"
"v_mul_f32 v225, 0xc1800000, v193\n"
"v_mul_f32 v224, 0xc1800000, v192\n"
"v_mfma_f32_16x16x32_bf16 v[68:71], v[56:59], a[164:167], v[68:71]\n"
"buffer_load_dwordx4 v18, s[48:51], s65 offen offset:2048 lds\n"
"s_waitcnt lgkmcnt(0)\n"
"s_barrier\n"
"v_mfma_f32_16x16x32_bf16 v[72:75], v[56:59], a[168:171], v[72:75]\n"
"ds_read_b64_tr_b16 v[42:43], v30 offset:59392\n"
"ds_read_b64_tr_b16 v[44:45], v30 offset:61440\n"
"v_mfma_f32_16x16x32_bf16 v[76:79], v[56:59], a[172:175], v[76:79]\n"
"ds_read_b64_tr_b16 v[46:47], v30 offset:63488\n"
"ds_read_b64_tr_b16 v[48:49], v30 offset:0\n"
"v_mfma_f32_16x16x32_bf16 v[64:67], v[60:63], a[176:179], v[64:67]\n"
"buffer_load_dwordx4 v19, s[48:51], s65 offen offset:3072 lds\n"
"v_mfma_f32_16x16x32_bf16 v[68:71], v[60:63], a[180:183], v[68:71]\n"
"ds_read_b64_tr_b16 v[50:51], v30 offset:2048\n"
"ds_read_b64_tr_b16 v[52:53], v30 offset:4096\n"
"v_mfma_f32_16x16x32_bf16 v[72:75], v[60:63], a[184:187], v[72:75]\n"
"ds_read_b64_tr_b16 v[54:55], v30 offset:6144\n"
"s_sub_u32 s65, s65, 343936\n"
"v_mfma_f32_16x16x32_bf16 v[76:79], v[60:63], a[188:191], v[76:79]\n"
"s_add_u32 m0, s61, 16384\n"
"ds_read_b64_tr_b16 v[56:57], v30 offset:8192\n"
"ds_read_b64_tr_b16 v[58:59], v30 offset:10240\n"
"v_mfma_f32_16x16x32_bf16 v[80:83], v[32:35], a[64:67], 0\n"
"ds_read_b64_tr_b16 v[60:61], v30 offset:12288\n"
"ds_read_b64_tr_b16 v[62:63], v30 offset:14336\n"
"v_mfma_f32_16x16x32_bf16 v[84:87], v[32:35], a[68:71], 0\n"
"ds_read_b128 a[192:195], v245 offset:0\n"
"v_mul_f32 v226, 0xc1800000, v194\n"
"v_mfma_f32_16x16x32_bf16 v[88:91], v[32:35], a[72:75], 0\n"
"ds_read_b128 a[196:199], v245 offset:1024\n"
"v_mul_f32 v227, 0xc1800000, v195\n"
"v_mfma_f32_16x16x32_bf16 v[92:95], v[32:35], a[76:79], 0\n"
"ds_read_b128 a[200:203], v245 offset:2048\n"
"s_lshr_b32 s81, s70, 14\n"
"v_mfma_f32_16x16x32_bf16 v[80:83], v[36:39], a[80:83], v[80:83]\n"
"ds_read_b128 a[204:207], v245 offset:3072\n"
"v_mul_f32 v193, 0x41317218, v193\n"
"v_mfma_f32_16x16x32_bf16 v[84:87], v[36:39], a[84:87], v[84:87]\n"
"ds_read_b128 a[208:211], v245 offset:4096\n"
"s_and_b32 s81, s81, 0x3fff\n"
"v_mfma_f32_16x16x32_bf16 v[88:91], v[36:39], a[88:91], v[88:91]\n"
"ds_read_b128 a[212:215], v245 offset:5120\n"
"v_mul_f32 v192, 0x41317218, v192\n"
"v_mfma_f32_16x16x32_bf16 v[92:95], v[36:39], a[92:95], v[92:95]\n"
"ds_read_b128 a[216:219], v245 offset:6144\n"
"s_waitcnt lgkmcnt(12)\n"
"v_mfma_f32_16x16x32_bf16 v[80:83], v[40:43], a[96:99], v[80:83]\n"
"ds_read_b128 a[220:223], v245 offset:7168\n"
"v_mul_f32 v194, 0x41317218, v194\n"
"v_mfma_f32_16x16x32_bf16 v[84:87], v[40:43], a[100:103], v[84:87]\n"
"ds_read_b128 a[224:227], v245 offset:8192\n"
"s_waitcnt vmcnt(8)\n"
"s_barrier\n"
"v_mfma_f32_16x16x32_bf16 v[88:91], v[40:43], a[104:107], v[88:91]\n"
"buffer_load_dwordx4 v16, s[48:51], s65 offen offset:0 lds\n"
"ds_read_b64_tr_b16 v[32:33], v3 offset:0\n"
"ds_read_b64_tr_b16 v[34:35], v3 offset:2048\n"
"v_mfma_f32_16x16x32_bf16 v[92:95], v[40:43], a[108:111], v[92:95]\n"
"ds_read_b128 a[228:231], v245 offset:9216\n"
"s_and_b32 s80, s70, 0x3fff\n"
"v_mfma_f32_16x16x32_bf16 v[80:83], v[44:47], a[112:115], v[80:83]\n"
"ds_read_b128 a[232:235], v245 offset:10240\n"
"s_lshl_b32 s81, s81, 16\n"
"v_mfma_f32_16x16x32_bf16 v[84:87], v[44:47], a[116:119], v[84:87]\n"
"buffer_load_dwordx4 v17, s[48:51], s65 offen offset:1024 lds\n"
"ds_read_b64_tr_b16 v[36:37], v3 offset:4096\n"
"v_mfma_f32_16x16x32_bf16 v[88:91], v[44:47], a[120:123], v[88:91]\n"
"ds_read_b128 a[236:239], v245 offset:11264\n"
"v_mul_f32 v195, 0x41317218, v195\n"
"v_mfma_f32_16x16x32_bf16 v[92:95], v[44:47], a[124:127], v[92:95]\n"
"ds_read_b64_tr_b16 v[38:39], v3 offset:6144\n"
"ds_read_b64_tr_b16 v[40:41], v3 offset:8192\n"
"v_mfma_f32_16x16x32_bf16 v[80:83], v[48:51], a[128:131], v[80:83]\n"
"buffer_load_dwordx4 v18, s[48:51], s65 offen offset:2048 lds\n"
"v_mfma_f32_16x16x32_bf16 v[84:87], v[48:51], a[132:135], v[84:87]\n"
"ds_read_b128 a[240:243], v245 offset:12288\n"
"s_or_b32 s66, s80, s81\n"
"v_mfma_f32_16x16x32_bf16 v[88:91], v[48:51], a[136:139], v[88:91]\n"
"ds_read_b64_tr_b16 v[42:43], v3 offset:10240\n"
"ds_read_b64_tr_b16 v[44:45], v3 offset:12288\n"
"v_mfma_f32_16x16x32_bf16 v[92:95], v[48:51], a[140:143], v[92:95]\n"
"buffer_load_dwordx4 v19, s[48:51], s65 offen offset:3072 lds\n"
"s_add_u32 s65, s65, 229376\n"
"s_add_u32 m0, s61, 32768\n"
"s_waitcnt lgkmcnt(11)\n"
"v_mfma_f32_16x16x32_bf16 v[80:83], v[52:55], a[144:147], v[80:83]\n"
"ds_read_b64_tr_b16 v[46:47], v3 offset:14336\n"
"s_add_u32 s67, s67, 16384\n"
"v_mfma_f32_16x16x32_bf16 v[84:87], v[52:55], a[148:151], v[84:87]\n"
"ds_read_b128 a[244:247], v245 offset:13312\n"
"v_mov_b32 v217, s66\n"
"v_mfma_f32_16x16x32_bf16 v[88:91], v[52:55], a[152:155], v[88:91]\n"
"buffer_load_dwordx4 v16, s[48:51], s65 offen offset:0 lds\n"
"ds_read_b64_tr_b16 v[48:49], v3 offset:16384\n"
"v_mfma_f32_16x16x32_bf16 v[92:95], v[52:55], a[156:159], v[92:95]\n"
"ds_read_b128 a[248:251], v245 offset:14336\n"
"v_mov_b32 v219, s66\n"
"v_mfma_f32_16x16x32_bf16 v[80:83], v[56:59], a[160:163], v[80:83]\n"
"ds_read_b128 a[252:255], v245 offset:15360\n"
"v_mov_b32 v221, s66\n"
"v_mfma_f32_16x16x32_bf16 v[84:87], v[56:59], a[164:167], v[84:87]\n"
"buffer_load_dwordx4 v17, s[48:51], s65 offen offset:1024 lds\n"
"ds_read_b64_tr_b16 v[50:51], v3 offset:18432\n"
"v_mfma_f32_16x16x32_bf16 v[88:91], v[56:59], a[168:171], v[88:91]\n"
"ds_read_b128 a[0:3], v245 offset:16384\n"
"v_mov_b32 v223, s66\n"
"v_mfma_f32_16x16x32_bf16 v[92:95], v[56:59], a[172:175], v[92:95]\n"
"ds_read_b128 a[4:7], v245 offset:17408\n"
"v_mov_b32 v229, s66\n"
"v_mfma_f32_16x16x32_bf16 v[80:83], v[60:63], a[176:179], v[80:83]\n"
"buffer_load_dwordx4 v18, s[48:51], s65 offen offset:2048 lds\n"
"v_mfma_f32_16x16x32_bf16 v[84:87], v[60:63], a[180:183], v[84:87]\n"
"ds_read_b128 a[8:11], v245 offset:18432\n"
"v_mov_b32 v231, s66\n"
"v_mfma_f32_16x16x32_bf16 v[88:91], v[60:63], a[184:187], v[88:91]\n"
"ds_read_b128 a[12:15], v245 offset:19456\n"
"v_mov_b32 v240, v234\n"
"v_mfma_f32_16x16x32_bf16 v[92:95], v[60:63], a[188:191], v[92:95]\n"
"buffer_load_dwordx4 v19, s[48:51], s65 offen offset:3072 lds\n"
"s_waitcnt lgkmcnt(8)\n"
"v_mfma_f32_16x16x32_bf16 v[64:67], v[32:35], a[192:195], v[64:67]\n"
"ds_read_b128 a[16:19], v245 offset:20480\n"
"s_sub_u32 s65, s65, 114688\n"
"v_mfma_f32_16x16x32_bf16 v[68:71], v[32:35], a[196:199], v[68:71]\n"
"ds_read_b64_tr_b16 v[52:53], v3 offset:20480\n"
"ds_read_b64_tr_b16 v[54:55], v3 offset:22528\n"
"v_mfma_f32_16x16x32_bf16 v[72:75], v[32:35], a[200:203], v[72:75]\n"
"s_add_u32 m0, s61, 49152\n"
"ds_read_b128 a[20:23], v245 offset:21504\n"
"v_mfma_f32_16x16x32_bf16 v[76:79], v[32:35], a[204:207], v[76:79]\n"
"ds_read_b128 a[24:27], v245 offset:22528\n"
"v_mov_b32 v241, 0\n"
"v_mfma_f32_16x16x32_bf16 v[64:67], v[36:39], a[208:211], v[64:67]\n"
"ds_read_b128 a[28:31], v245 offset:23552\n"
"v_mov_b32 v242, v235\n"
"v_mfma_f32_16x16x32_bf16 v[68:71], v[36:39], a[212:215], v[68:71]\n"
"ds_read_b128 a[32:35], v245 offset:24576\n"
"v_mov_b32 v243, 0\n"
"v_mfma_f32_16x16x32_bf16 v[72:75], v[36:39], a[216:219], v[72:75]\n"
"ds_read_b64_tr_b16 v[56:57], v3 offset:24576\n"
"ds_read_b64_tr_b16 v[58:59], v3 offset:26624\n"
"v_mfma_f32_16x16x32_bf16 v[76:79], v[36:39], a[220:223], v[76:79]\n"
"ds_read_b128 a[36:39], v245 offset:25600\n"
"v_mfma_f32_16x16x32_bf16 v[64:67], v[40:43], a[224:227], v[64:67]\n"
"ds_read_b128 a[40:43], v245 offset:26624\n"
"v_mfma_f32_16x16x32_bf16 v[68:71], v[40:43], a[228:231], v[68:71]\n"
"ds_read_b128 a[44:47], v245 offset:27648\n"
"s_waitcnt vmcnt(8)\n"
"s_barrier\n"
"v_mfma_f32_16x16x32_bf16 v[72:75], v[40:43], a[232:235], v[72:75]\n"
"buffer_load_dwordx4 v16, s[48:51], s65 offen offset:0 lds\n"
"ds_read_b128 a[48:51], v245 offset:28672\n"
"v_mfma_f32_16x16x32_bf16 v[76:79], v[40:43], a[236:239], v[76:79]\n"
"ds_read_b64_tr_b16 v[60:61], v3 offset:28672\n"
"ds_read_b64_tr_b16 v[62:63], v3 offset:30720\n"
"v_mfma_f32_16x16x32_bf16 v[64:67], v[44:47], a[240:243], v[64:67]\n"
"ds_read_b128 a[52:55], v245 offset:29696\n"
"v_mfma_f32_16x16x32_bf16 v[68:71], v[44:47], a[244:247], v[68:71]\n"
"ds_read_b128 a[56:59], v245 offset:30720\n"
"s_waitcnt lgkmcnt(12)\n"
"v_mfma_f32_16x16x32_bf16 v[72:75], v[44:47], a[248:251], v[72:75]\n"
"buffer_load_dwordx4 v17, s[48:51], s65 offen offset:1024 lds\n"
"ds_read_b64_tr_b16 v[32:33], v31 offset:0\n"
"v_mfma_f32_16x16x32_bf16 v[76:79], v[44:47], a[252:255], v[76:79]\n"
"ds_read_b128 a[60:63], v245 offset:31744\n"
"v_mfma_f32_16x16x32_bf16 v[64:67], v[48:51], a[0:3], v[64:67]\n"
"ds_read_b64_tr_b16 v[34:35], v31 offset:2048\n"
"ds_read_b64_tr_b16 v[36:37], v31 offset:4096\n"
"v_mfma_f32_16x16x32_bf16 v[68:71], v[48:51], a[4:7], v[68:71]\n"
"buffer_load_dwordx4 v18, s[48:51], s65 offen offset:2048 lds\n"
"ds_read_b64_tr_b16 v[38:39], v31 offset:6144\n"
"v_mfma_f32_16x16x32_bf16 v[72:75], v[48:51], a[8:11], v[72:75]\n"
"ds_read_b64_tr_b16 v[40:41], v31 offset:8192\n"
"ds_read_b64_tr_b16 v[42:43], v31 offset:10240\n"
"v_mfma_f32_16x16x32_bf16 v[76:79], v[48:51], a[12:15], v[76:79]\n"
"ds_read_b64_tr_b16 v[44:45], v31 offset:12288\n"
"ds_read_b64_tr_b16 v[46:47], v31 offset:14336\n"
"v_mfma_f32_16x16x32_bf16 v[64:67], v[52:55], a[16:19], v[64:67]\n"
"buffer_load_dwordx4 v19, s[48:51], s65 offen offset:3072 lds\n"
"s_add_u32 s65, s65, 229376\n"
"s_add_u32 m0, s61, 0\n"
"v_mfma_f32_16x16x32_bf16 v[68:71], v[52:55], a[20:23], v[68:71]\n"
"ds_read_b64_tr_b16 v[48:49], v31 offset:16384\n"
"v_mfma_f32_16x16x32_bf16 v[72:75], v[52:55], a[24:27], v[72:75]\n"
"ds_read_b64_tr_b16 v[50:51], v31 offset:18432\n"
"s_waitcnt lgkmcnt(12)\n"
"v_mfma_f32_16x16x32_bf16 v[76:79], v[52:55], a[28:31], v[76:79]\n"
"buffer_load_dwordx4 v16, s[48:51], s65 offen offset:0 lds\n"
"v_mfma_f32_16x16x32_bf16 v[64:67], v[56:59], a[32:35], v[64:67]\n"
"ds_read_b64_tr_b16 v[52:53], v31 offset:20480\n"
"ds_read_b64_tr_b16 v[54:55], v31 offset:22528\n"
"v_mfma_f32_16x16x32_bf16 v[68:71], v[56:59], a[36:39], v[68:71]\n"
"v_exp_f32 v225, v225\n"
"v_mfma_f32_16x16x32_bf16 v[72:75], v[56:59], a[40:43], v[72:75]\n"
"buffer_load_dwordx4 v17, s[48:51], s65 offen offset:1024 lds\n"
"v_mfma_f32_16x16x32_bf16 v[76:79], v[56:59], a[44:47], v[76:79]\n"
"ds_read_b64_tr_b16 v[56:57], v31 offset:24576\n"
"ds_read_b64_tr_b16 v[58:59], v31 offset:26624\n"
"v_mfma_f32_16x16x32_bf16 v[64:67], v[60:63], a[48:51], v[64:67]\n"
"v_exp_f32 v224, v224\n"
"v_mfma_f32_16x16x32_bf16 v[68:71], v[60:63], a[52:55], v[68:71]\n"
"buffer_load_dwordx4 v18, s[48:51], s65 offen offset:2048 lds\n"
"s_waitcnt lgkmcnt(6)\n"
"v_mfma_f32_16x16x32_bf16 v[72:75], v[60:63], a[56:59], v[72:75]\n"
"v_exp_f32 v226, v226\n"
"v_mfma_f32_16x16x32_bf16 v[76:79], v[60:63], a[60:63], v[76:79]\n"
"ds_read_b64_tr_b16 v[60:61], v31 offset:28672\n"
"ds_read_b64_tr_b16 v[62:63], v31 offset:30720\n"
"v_mfma_f32_16x16x32_bf16 v[80:83], v[32:35], a[192:195], v[80:83]\n"
"buffer_load_dwordx4 v19, s[48:51], s65 offen offset:3072 lds\n"
"v_mfma_f32_16x16x32_bf16 v[84:87], v[32:35], a[196:199], v[84:87]\n"
"v_cvt_pk_bf16_f32 v64, v64, v65\n"
"v_cvt_pk_bf16_f32 v65, v66, v67\n"
"v_mfma_f32_16x16x32_bf16 v[88:91], v[32:35], a[200:203], v[88:91]\n"
"v_cvt_pk_bf16_f32 v66, v68, v69\n"
"v_cvt_pk_bf16_f32 v72, v72, v73\n"
"v_mfma_f32_16x16x32_bf16 v[92:95], v[32:35], a[204:207], v[92:95]\n"
"s_add_u32 m0, s61, 98304\n"
"v_cvt_pk_bf16_f32 v67, v70, v71\n"
"v_mfma_f32_16x16x32_bf16 v[80:83], v[36:39], a[208:211], v[80:83]\n"
"buffer_store_dwordx4 v[64:67], v1, s[20:23], s68 offen sc1\n"
"v_mfma_f32_16x16x32_bf16 v[84:87], v[36:39], a[212:215], v[84:87]\n"
"s_sub_u32 s65, s65, 343936\n"
"v_cvt_pk_bf16_f32 v73, v74, v75\n"
"v_mfma_f32_16x16x32_bf16 v[88:91], v[36:39], a[216:219], v[88:91]\n"
"v_cvt_pk_bf16_f32 v74, v76, v77\n"
"v_cvt_pk_bf16_f32 v75, v78, v79\n"
"v_mfma_f32_16x16x32_bf16 v[92:95], v[36:39], a[220:223], v[92:95]\n"
"v_cndmask_b32 v208, v208, v64, vcc\n"
"v_cndmask_b32 v209, v209, v65, vcc\n"
"v_mfma_f32_16x16x32_bf16 v[80:83], v[40:43], a[224:227], v[80:83]\n"
"buffer_store_dwordx4 v[72:75], v1, s[20:23], s69 offen sc1\n"
"v_mfma_f32_16x16x32_bf16 v[84:87], v[40:43], a[228:231], v[84:87]\n"
"s_waitcnt vmcnt(10)\n"
"s_barrier\n"
"ds_read_b64_tr_b16 v[32:33], v30 offset:16384\n"
"ds_read_b64_tr_b16 v[34:35], v30 offset:18432\n"
"v_mfma_f32_16x16x32_bf16 v[88:91], v[40:43], a[232:235], v[88:91]\n"
"ds_read_b64_tr_b16 v[36:37], v30 offset:20480\n"
"ds_read_b64_tr_b16 v[38:39], v30 offset:22528\n"
"v_mfma_f32_16x16x32_bf16 v[92:95], v[40:43], a[236:239], v[92:95]\n"
"buffer_load_dwordx4 v16, s[48:51], s65 offen offset:0 lds\n"
"v_mfma_f32_16x16x32_bf16 v[80:83], v[44:47], a[240:243], v[80:83]\n"
"ds_read_b64_tr_b16 v[40:41], v30 offset:24576\n"
"ds_read_b64_tr_b16 v[42:43], v30 offset:26624\n"
"v_mfma_f32_16x16x32_bf16 v[84:87], v[44:47], a[244:247], v[84:87]\n"
"v_cndmask_b32 v210, v210, v66, vcc\n"
"v_cndmask_b32 v211, v211, v67, vcc\n"
"v_mfma_f32_16x16x32_bf16 v[88:91], v[44:47], a[248:251], v[88:91]\n"
"buffer_load_dwordx4 v17, s[48:51], s65 offen offset:1024 lds\n"
"v_mfma_f32_16x16x32_bf16 v[92:95], v[44:47], a[252:255], v[92:95]\n"
"ds_read_b64_tr_b16 v[44:45], v30 offset:28672\n"
"s_waitcnt lgkmcnt(7)\n"
"v_mfma_f32_16x16x32_bf16 v[80:83], v[48:51], a[0:3], v[80:83]\n"
"ds_read_b64_tr_b16 v[46:47], v30 offset:30720\n"
"s_add_u32 s68, s29, 15048704\n"
"v_mfma_f32_16x16x32_bf16 v[84:87], v[48:51], a[4:7], v[84:87]\n"
"buffer_load_dwordx4 v18, s[48:51], s65 offen offset:2048 lds\n"
"v_mfma_f32_16x16x32_bf16 v[88:91], v[48:51], a[8:11], v[88:91]\n"
"s_cmp_ge_u32 s57, 0x40000000\n"
"s_cselect_b64 vcc, -1, 0\n"
"v_mfma_f32_16x16x32_bf16 v[92:95], v[48:51], a[12:15], v[92:95]\n"
"ds_read_b64_tr_b16 v[48:49], v30 offset:32768\n"
"ds_read_b64_tr_b16 v[50:51], v30 offset:34816\n"
"v_mfma_f32_16x16x32_bf16 v[80:83], v[52:55], a[16:19], v[80:83]\n"
"buffer_load_dwordx4 v19, s[48:51], s65 offen offset:3072 lds\n"
"s_add_u32 s65, s65, 229376\n"
"s_add_u32 m0, s61, 114688\n"
"v_mfma_f32_16x16x32_bf16 v[84:87], v[52:55], a[20:23], v[84:87]\n"
"v_cndmask_b32 v208, v208, v72, vcc\n"
"v_cndmask_b32 v209, v209, v73, vcc\n"
"v_mfma_f32_16x16x32_bf16 v[88:91], v[52:55], a[24:27], v[88:91]\n"
"v_cndmask_b32 v210, v210, v74, vcc\n"
"v_cndmask_b32 v211, v211, v75, vcc\n"
"v_mfma_f32_16x16x32_bf16 v[92:95], v[52:55], a[28:31], v[92:95]\n"
"buffer_load_dwordx4 v16, s[48:51], s65 offen offset:0 lds\n"
"v_mfma_f32_16x16x32_bf16 v[80:83], v[56:59], a[32:35], v[80:83]\n"
"ds_read_b64_tr_b16 v[52:53], v30 offset:36864\n"
"ds_read_b64_tr_b16 v[54:55], v30 offset:38912\n"
"v_mfma_f32_16x16x32_bf16 v[84:87], v[56:59], a[36:39], v[84:87]\n"
"s_add_u32 s69, s58, 15048704\n"
"v_add_f32 v225, 1.0, v225\n"
"v_mfma_f32_16x16x32_bf16 v[88:91], v[56:59], a[40:43], v[88:91]\n"
"buffer_load_dwordx4 v17, s[48:51], s65 offen offset:1024 lds\n"
"v_mfma_f32_16x16x32_bf16 v[92:95], v[56:59], a[44:47], v[92:95]\n"
"ds_read_b64_tr_b16 v[56:57], v30 offset:40960\n"
"ds_read_b64_tr_b16 v[58:59], v30 offset:43008\n"
"v_mfma_f32_16x16x32_bf16 v[80:83], v[60:63], a[48:51], v[80:83]\n"
"s_cmp_ge_u32 s29, 0x40000000\n"
"s_cselect_b64 vcc, -1, 0\n"
"v_mfma_f32_16x16x32_bf16 v[84:87], v[60:63], a[52:55], v[84:87]\n"
"buffer_load_dwordx4 v18, s[48:51], s65 offen offset:2048 lds\n"
"v_mfma_f32_16x16x32_bf16 v[88:91], v[60:63], a[56:59], v[88:91]\n"
"v_exp_f32 v227, v227\n"
"v_mfma_f32_16x16x32_bf16 v[92:95], v[60:63], a[60:63], v[92:95]\n"
"ds_read_b64_tr_b16 v[60:61], v30 offset:45056\n"
"s_waitcnt lgkmcnt(5)\n"
"v_mfma_f32_16x16x32_bf16 v[64:67], v[32:35], a[64:67], 0\n"
"buffer_load_dwordx4 v19, s[48:51], s65 offen offset:3072 lds\n"
"v_mfma_f32_16x16x32_bf16 v[68:71], v[32:35], a[68:71], 0\n"
"ds_read_b64_tr_b16 v[62:63], v30 offset:47104\n"
"v_cvt_pk_bf16_f32 v80, v80, v81\n"
"v_mfma_f32_16x16x32_bf16 v[72:75], v[32:35], a[72:75], 0\n"
"v_cvt_pk_bf16_f32 v81, v82, v83\n"
"v_cvt_pk_bf16_f32 v88, v88, v89\n"
"v_mfma_f32_16x16x32_bf16 v[76:79], v[32:35], a[76:79], 0\n"
"s_add_u32 m0, s61, 131072\n"
"v_cvt_pk_bf16_f32 v82, v84, v85\n"
"v_mfma_f32_16x16x32_bf16 v[64:67], v[36:39], a[80:83], v[64:67]\n"
"v_cvt_pk_bf16_f32 v83, v86, v87\n"
"s_sub_u32 s65, s65, 114688\n"
"v_mfma_f32_16x16x32_bf16 v[68:71], v[36:39], a[84:87], v[68:71]\n"
"buffer_store_dwordx4 v[80:83], v1, s[20:23], s68 offen sc1\n"
"v_mfma_f32_16x16x32_bf16 v[72:75], v[36:39], a[88:91], v[72:75]\n"
"v_cvt_pk_bf16_f32 v89, v90, v91\n"
"v_cvt_pk_bf16_f32 v90, v92, v93\n"
"v_mfma_f32_16x16x32_bf16 v[76:79], v[36:39], a[92:95], v[76:79]\n"
"v_cvt_pk_bf16_f32 v91, v94, v95\n"
"v_cndmask_b32 v208, v208, v80, vcc\n"
"v_mfma_f32_16x16x32_bf16 v[64:67], v[40:43], a[96:99], v[64:67]\n"
"v_cndmask_b32 v209, v209, v81, vcc\n"
"v_cndmask_b32 v210, v210, v82, vcc\n"
"v_mfma_f32_16x16x32_bf16 v[68:71], v[40:43], a[100:103], v[68:71]\n"
"buffer_load_dwordx4 v16, s[48:51], s65 offen offset:0 lds\n"
"s_waitcnt vmcnt(12)\n"
"s_barrier\n"
"ds_read_b64_tr_b16 v[32:33], v30 offset:49152\n"
"ds_read_b64_tr_b16 v[34:35], v30 offset:51200\n"
"v_mfma_f32_16x16x32_bf16 v[72:75], v[40:43], a[104:107], v[72:75]\n"
"ds_read_b64_tr_b16 v[36:37], v30 offset:53248\n"
"v_cndmask_b32 v211, v211, v83, vcc\n"
"v_mfma_f32_16x16x32_bf16 v[76:79], v[40:43], a[108:111], v[76:79]\n"
"ds_read_b64_tr_b16 v[38:39], v30 offset:55296\n"
"ds_read_b64_tr_b16 v[40:41], v30 offset:57344\n"
"v_mfma_f32_16x16x32_bf16 v[64:67], v[44:47], a[112:115], v[64:67]\n"
"ds_read_b64_tr_b16 v[42:43], v30 offset:59392\n"
"s_add_u32 s68, s30, 15056896\n"
"v_mfma_f32_16x16x32_bf16 v[68:71], v[44:47], a[116:119], v[68:71]\n"
"buffer_load_dwordx4 v17, s[48:51], s65 offen offset:1024 lds\n"
"v_mfma_f32_16x16x32_bf16 v[72:75], v[44:47], a[120:123], v[72:75]\n"
"s_cmp_ge_u32 s58, 0x40000000\n"
"s_cselect_b64 vcc, -1, 0\n"
"v_mfma_f32_16x16x32_bf16 v[76:79], v[44:47], a[124:127], v[76:79]\n"
"ds_read_b64_tr_b16 v[44:45], v30 offset:61440\n"
"ds_read_b64_tr_b16 v[46:47], v30 offset:63488\n"
"v_mfma_f32_16x16x32_bf16 v[64:67], v[48:51], a[128:131], v[64:67]\n"
"buffer_store_dwordx4 v[88:91], v1, s[20:23], s69 offen sc1\n"
"v_mfma_f32_16x16x32_bf16 v[68:71], v[48:51], a[132:135], v[68:71]\n"
"v_cndmask_b32 v208, v208, v88, vcc\n"
"v_cndmask_b32 v209, v209, v89, vcc\n"
"v_mfma_f32_16x16x32_bf16 v[72:75], v[48:51], a[136:139], v[72:75]\n"
"v_cndmask_b32 v210, v210, v90, vcc\n"
"v_cndmask_b32 v211, v211, v91, vcc\n"
"v_mfma_f32_16x16x32_bf16 v[76:79], v[48:51], a[140:143], v[76:79]\n"
"buffer_load_dwordx4 v18, s[48:51], s65 offen offset:2048 lds\n"
"s_waitcnt lgkmcnt(6)\n"
"v_mfma_f32_16x16x32_bf16 v[64:67], v[52:55], a[144:147], v[64:67]\n"
"ds_read_b64_tr_b16 v[48:49], v30 offset:0\n"
"ds_read_b64_tr_b16 v[50:51], v30 offset:2048\n"
"v_mfma_f32_16x16x32_bf16 v[68:71], v[52:55], a[148:151], v[68:71]\n"
"s_add_u32 s69, s59, 15056896\n"
"v_add_f32 v224, 1.0, v224\n"
"v_mfma_f32_16x16x32_bf16 v[72:75], v[52:55], a[152:155], v[72:75]\n"
"buffer_load_dwordx4 v19, s[48:51], s65 offen offset:3072 lds\n"
"s_add_u32 s65, s65, 229376\n"
"s_add_u32 m0, s61, 147456\n"
"v_mfma_f32_16x16x32_bf16 v[76:79], v[52:55], a[156:159], v[76:79]\n"
"ds_read_b64_tr_b16 v[52:53], v30 offset:4096\n"
"ds_read_b64_tr_b16 v[54:55], v30 offset:6144\n"
"v_mfma_f32_16x16x32_bf16 v[64:67], v[56:59], a[160:163], v[64:67]\n"
"s_cmp_ge_u32 s30, 0x40000000\n"
"s_cselect_b64 vcc, -1, 0\n"
"v_mfma_f32_16x16x32_bf16 v[68:71], v[56:59], a[164:167], v[68:71]\n"
"buffer_load_dwordx4 v16, s[48:51], s65 offen offset:0 lds\n"
"v_mfma_f32_16x16x32_bf16 v[72:75], v[56:59], a[168:171], v[72:75]\n"
"v_add_f32 v226, 1.0, v226\n"
"v_add_f32 v227, 1.0, v227\n"
"v_mfma_f32_16x16x32_bf16 v[76:79], v[56:59], a[172:175], v[76:79]\n"
"ds_read_b64_tr_b16 v[56:57], v30 offset:8192\n"
"ds_read_b64_tr_b16 v[58:59], v30 offset:10240\n"
"v_mfma_f32_16x16x32_bf16 v[64:67], v[60:63], a[176:179], v[64:67]\n"
"buffer_load_dwordx4 v17, s[48:51], s65 offen offset:1024 lds\n"
"v_mfma_f32_16x16x32_bf16 v[68:71], v[60:63], a[180:183], v[68:71]\n"
"v_rcp_f32 v225, v225\n"
"v_mfma_f32_16x16x32_bf16 v[72:75], v[60:63], a[184:187], v[72:75]\n"
"v_rcp_f32 v224, v224\n"
"v_mfma_f32_16x16x32_bf16 v[76:79], v[60:63], a[188:191], v[76:79]\n"
"buffer_load_dwordx4 v18, s[48:51], s65 offen offset:2048 lds\n"
"v_mfma_f32_16x16x32_bf16 v[64:67], v[32:35], a[192:195], v[64:67]\n"
"ds_read_b64_tr_b16 v[60:61], v30 offset:12288\n"
"ds_read_b64_tr_b16 v[62:63], v30 offset:14336\n"
"v_mfma_f32_16x16x32_bf16 v[68:71], v[32:35], a[196:199], v[68:71]\n"
"v_rcp_f32 v226, v226\n"
"v_mfma_f32_16x16x32_bf16 v[72:75], v[32:35], a[200:203], v[72:75]\n"
"buffer_load_dwordx4 v19, s[48:51], s65 offen offset:3072 lds\n"
"v_mfma_f32_16x16x32_bf16 v[76:79], v[32:35], a[204:207], v[76:79]\n"
"s_sub_u32 s65, s65, 343936\n"
"s_waitcnt lgkmcnt(4)\n"
"v_mfma_f32_16x16x32_bf16 v[64:67], v[36:39], a[208:211], v[64:67]\n"
"s_add_u32 m0, s61, 65536\n"
"v_mul_f32 v193, v193, v225\n"
"v_mfma_f32_16x16x32_bf16 v[68:71], v[36:39], a[212:215], v[68:71]\n"
"v_rcp_f32 v227, v227\n"
"v_mfma_f32_16x16x32_bf16 v[72:75], v[36:39], a[216:219], v[72:75]\n"
"buffer_load_dwordx4 v16, s[48:51], s65 offen offset:0 lds\n"
"v_mfma_f32_16x16x32_bf16 v[76:79], v[36:39], a[220:223], v[76:79]\n"
"v_mul_f32 v192, v192, v224\n"
"v_mul_f32 v194, v194, v226\n"
"v_mfma_f32_16x16x32_bf16 v[64:67], v[40:43], a[224:227], v[64:67]\n"
"s_cmp_eq_u32 s34, 3\n"
"s_cselect_b32 s82, 0x40000000, 0\n"
"v_mfma_f32_16x16x32_bf16 v[68:71], v[40:43], a[228:231], v[68:71]\n"
"buffer_load_dwordx4 v17, s[48:51], s65 offen offset:1024 lds\n"
"s_waitcnt vmcnt(12)\n"
"s_barrier\n"
"ds_read_b64_tr_b16 v[32:33], v31 offset:32768\n"
"ds_read_b64_tr_b16 v[34:35], v31 offset:34816\n"
"v_mfma_f32_16x16x32_bf16 v[72:75], v[40:43], a[232:235], v[72:75]\n"
"ds_read_b64_tr_b16 v[36:37], v31 offset:36864\n"
"ds_read_b64_tr_b16 v[38:39], v31 offset:38912\n"
"v_mfma_f32_16x16x32_bf16 v[76:79], v[40:43], a[236:239], v[76:79]\n"
"ds_read_b64_tr_b16 v[40:41], v31 offset:40960\n"
"v_mul_f32 v225, 0xc1800000, v197\n"
"v_mfma_f32_16x16x32_bf16 v[64:67], v[44:47], a[240:243], v[64:67]\n"
"buffer_load_dwordx4 v18, s[48:51], s65 offen offset:2048 lds\n"
"v_mfma_f32_16x16x32_bf16 v[68:71], v[44:47], a[244:247], v[68:71]\n"
"ds_read_b64_tr_b16 v[42:43], v31 offset:43008\n"
"v_mul_f32 v195, v195, v227\n"
"v_mfma_f32_16x16x32_bf16 v[72:75], v[44:47], a[248:251], v[72:75]\n"
"v_mul_f32 v224, 0xc1800000, v196\n"
"v_mul_f32 v226, 0xc1800000, v198\n"
"v_mfma_f32_16x16x32_bf16 v[76:79], v[44:47], a[252:255], v[76:79]\n"
"buffer_load_dwordx4 v19, s[48:51], s65 offen offset:3072 lds\n"
"s_add_u32 s65, s65, 229376\n"
"s_add_u32 m0, s61, 81920\n"
"v_mfma_f32_16x16x32_bf16 v[64:67], v[48:51], a[0:3], v[64:67]\n"
"ds_read_b64_tr_b16 v[44:45], v31 offset:45056\n"
"ds_read_b64_tr_b16 v[46:47], v31 offset:47104\n"
"v_mfma_f32_16x16x32_bf16 v[68:71], v[48:51], a[4:7], v[68:71]\n"
"s_add_u32 s80, s74, s82\n"
"v_mul_f32 v227, 0xc1800000, v199\n"
"v_mfma_f32_16x16x32_bf16 v[72:75], v[48:51], a[8:11], v[72:75]\n"
"buffer_load_dwordx4 v16, s[48:51], s65 offen offset:0 lds\n"
"v_mfma_f32_16x16x32_bf16 v[76:79], v[48:51], a[12:15], v[76:79]\n"
"ds_read_b64_tr_b16 v[48:49], v31 offset:49152\n"
"ds_read_b64_tr_b16 v[50:51], v31 offset:51200\n"
"v_mfma_f32_16x16x32_bf16 v[64:67], v[52:55], a[16:19], v[64:67]\n"
"v_exp_f32 v225, v225\n"
"v_mfma_f32_16x16x32_bf16 v[68:71], v[52:55], a[20:23], v[68:71]\n"
"buffer_load_dwordx4 v17, s[48:51], s65 offen offset:1024 lds\n"
"v_mfma_f32_16x16x32_bf16 v[72:75], v[52:55], a[24:27], v[72:75]\n"
"v_exp_f32 v224, v224\n"
"v_mfma_f32_16x16x32_bf16 v[76:79], v[52:55], a[28:31], v[76:79]\n"
"ds_read_b64_tr_b16 v[52:53], v31 offset:53248\n"
"s_waitcnt lgkmcnt(5)\n"
"v_mfma_f32_16x16x32_bf16 v[64:67], v[56:59], a[32:35], v[64:67]\n"
"buffer_load_dwordx4 v18, s[48:51], s65 offen offset:2048 lds\n"
"v_mfma_f32_16x16x32_bf16 v[68:71], v[56:59], a[36:39], v[68:71]\n"
"ds_read_b64_tr_b16 v[54:55], v31 offset:55296\n"
"v_add_f32 v225, 1.0, v225\n"
"v_mfma_f32_16x16x32_bf16 v[72:75], v[56:59], a[40:43], v[72:75]\n"
"v_exp_f32 v226, v226\n"
"v_mfma_f32_16x16x32_bf16 v[76:79], v[56:59], a[44:47], v[76:79]\n"
"buffer_load_dwordx4 v19, s[48:51], s65 offen offset:3072 lds\n"
"v_mfma_f32_16x16x32_bf16 v[64:67], v[60:63], a[48:51], v[64:67]\n"
"ds_read_b64_tr_b16 v[56:57], v31 offset:57344\n"
"ds_read_b64_tr_b16 v[58:59], v31 offset:59392\n"
"v_mfma_f32_16x16x32_bf16 v[68:71], v[60:63], a[52:55], v[68:71]\n"
"s_sub_u32 s65, s65, 114688\n"
"s_add_u32 m0, s61, 16384\n"
"v_mfma_f32_16x16x32_bf16 v[72:75], v[60:63], a[56:59], v[72:75]\n"
"v_exp_f32 v227, v227\n"
"v_mfma_f32_16x16x32_bf16 v[76:79], v[60:63], a[60:63], v[76:79]\n"
"buffer_load_dwordx4 v16, s[48:51], s65 offen offset:0 lds\n"
"v_cvt_pk_bf16_f32 v64, v64, v65\n"
"v_cvt_pk_bf16_f32 v65, v66, v67\n"
"v_mfma_f32_16x16x32_bf16 v[80:83], v[32:35], a[64:67], 0\n"
"v_cvt_pk_bf16_f32 v66, v68, v69\n"
"v_cvt_pk_bf16_f32 v67, v70, v71\n"
"v_mfma_f32_16x16x32_bf16 v[84:87], v[32:35], a[68:71], 0\n"
"v_cvt_pk_bf16_f32 v72, v72, v73\n"
"v_cvt_pk_bf16_f32 v73, v74, v75\n"
"v_cvt_pk_bf16_f32 v74, v76, v77\n"
"v_cvt_pk_bf16_f32 v75, v78, v79\n"
"v_mfma_f32_16x16x32_bf16 v[88:91], v[32:35], a[72:75], 0\n"
"buffer_store_dwordx4 v[64:67], v1, s[20:23], s68 offen sc1\n"
"v_mfma_f32_16x16x32_bf16 v[92:95], v[32:35], a[76:79], 0\n"
"ds_read_b64_tr_b16 v[60:61], v31 offset:61440\n"
"ds_read_b64_tr_b16 v[62:63], v31 offset:63488\n"
"v_mfma_f32_16x16x32_bf16 v[80:83], v[36:39], a[80:83], v[80:83]\n"
"v_cndmask_b32 v208, v208, v64, vcc\n"
"v_cndmask_b32 v209, v209, v65, vcc\n"
"v_mfma_f32_16x16x32_bf16 v[84:87], v[36:39], a[84:87], v[84:87]\n"
"buffer_store_dwordx4 v[72:75], v1, s[20:23], s69 offen sc1\n"
"v_mfma_f32_16x16x32_bf16 v[88:91], v[36:39], a[88:91], v[88:91]\n"
"v_cndmask_b32 v210, v210, v66, vcc\n"
"v_cndmask_b32 v211, v211, v67, vcc\n"
"v_mfma_f32_16x16x32_bf16 v[92:95], v[36:39], a[92:95], v[92:95]\n"
"s_cmp_ge_u32 s59, 0x40000000\n"
"s_cselect_b64 vcc, -1, 0\n"
"v_mfma_f32_16x16x32_bf16 v[80:83], v[40:43], a[96:99], v[80:83]\n"
"v_cndmask_b32 v208, v208, v72, vcc\n"
"v_cndmask_b32 v209, v209, v73, vcc\n"
"v_mfma_f32_16x16x32_bf16 v[84:87], v[40:43], a[100:103], v[84:87]\n"
"buffer_load_dwordx4 v17, s[48:51], s65 offen offset:1024 lds\n"
"s_waitcnt vmcnt(12)\n"
"s_barrier\n"
"ds_read_b64_tr_b16 v[32:33], v3 offset:0\n"
"ds_read_b64_tr_b16 v[34:35], v3 offset:2048\n"
"v_mfma_f32_16x16x32_bf16 v[88:91], v[40:43], a[104:107], v[88:91]\n"
"ds_read_b64_tr_b16 v[36:37], v3 offset:4096\n"
"v_cndmask_b32 v210, v210, v74, vcc\n"
"v_mfma_f32_16x16x32_bf16 v[92:95], v[40:43], a[108:111], v[92:95]\n"
"ds_read_b64_tr_b16 v[38:39], v3 offset:6144\n"
"s_waitcnt lgkmcnt(6)\n"
"v_mfma_f32_16x16x32_bf16 v[80:83], v[44:47], a[112:115], v[80:83]\n"
"ds_read_b64_tr_b16 v[40:41], v3 offset:8192\n"
"ds_read_b64_tr_b16 v[42:43], v3 offset:10240\n"
"v_mfma_f32_16x16x32_bf16 v[84:87], v[44:47], a[116:119], v[84:87]\n"
"buffer_load_dwordx4 v18, s[48:51], s65 offen offset:2048 lds\n"
"v_mfma_f32_16x16x32_bf16 v[88:91], v[44:47], a[120:123], v[88:91]\n"
"v_cndmask_b32 v211, v211, v75, vcc\n"
"s_add_u32 s68, s31, 15065088\n"
"v_mfma_f32_16x16x32_bf16 v[92:95], v[44:47], a[124:127], v[92:95]\n"
"ds_read_b64_tr_b16 v[44:45], v3 offset:12288\n"
"ds_read_b64_tr_b16 v[46:47], v3 offset:14336\n"
"v_mfma_f32_16x16x32_bf16 v[80:83], v[48:51], a[128:131], v[80:83]\n"
"s_add_u32 s69, s33, 15065088\n"
"v_add_f32 v224, 1.0, v224\n"
"v_mfma_f32_16x16x32_bf16 v[84:87], v[48:51], a[132:135], v[84:87]\n"
"buffer_load_dwordx4 v19, s[48:51], s65 offen offset:3072 lds\n"
"v_mfma_f32_16x16x32_bf16 v[88:91], v[48:51], a[136:139], v[88:91]\n"
"s_add_u32 s65, s65, 229376\n"
"s_add_u32 m0, s61, 32768\n"
"v_mfma_f32_16x16x32_bf16 v[92:95], v[48:51], a[140:143], v[92:95]\n"
"ds_read_b64_tr_b16 v[48:49], v3 offset:16384\n"
"ds_read_b64_tr_b16 v[50:51], v3 offset:18432\n"
"v_mfma_f32_16x16x32_bf16 v[80:83], v[52:55], a[144:147], v[80:83]\n"
"s_cmp_ge_u32 s31, 0x40000000\n"
"s_cselect_b64 vcc, -1, 0\n"
"v_mfma_f32_16x16x32_bf16 v[84:87], v[52:55], a[148:151], v[84:87]\n"
"buffer_load_dwordx4 v16, s[48:51], s65 offen offset:0 lds\n"
"v_mfma_f32_16x16x32_bf16 v[88:91], v[52:55], a[152:155], v[88:91]\n"
"v_add_f32 v226, 1.0, v226\n"
"v_mul_f32 v197, 0x41317218, v197\n"
"v_mfma_f32_16x16x32_bf16 v[92:95], v[52:55], a[156:159], v[92:95]\n"
"ds_read_b64_tr_b16 v[52:53], v3 offset:20480\n"
"ds_read_b64_tr_b16 v[54:55], v3 offset:22528\n"
"v_mfma_f32_16x16x32_bf16 v[80:83], v[56:59], a[160:163], v[80:83]\n"
"buffer_load_dwordx4 v17, s[48:51], s65 offen offset:1024 lds\n"
"v_mfma_f32_16x16x32_bf16 v[84:87], v[56:59], a[164:167], v[84:87]\n"
"v_rcp_f32 v225, v225\n"
"v_mfma_f32_16x16x32_bf16 v[88:91], v[56:59], a[168:171], v[88:91]\n"
"v_add_f32 v227, 1.0, v227\n"
"v_mul_f32 v196, 0x41317218, v196\n"
"v_mfma_f32_16x16x32_bf16 v[92:95], v[56:59], a[172:175], v[92:95]\n"
"buffer_load_dwordx4 v18, s[48:51], s65 offen offset:2048 lds\n"
"s_waitcnt lgkmcnt(4)\n"
"v_mfma_f32_16x16x32_bf16 v[80:83], v[60:63], a[176:179], v[80:83]\n"
"ds_read_b64_tr_b16 v[56:57], v3 offset:24576\n"
"ds_read_b64_tr_b16 v[58:59], v3 offset:26624\n"
"v_mfma_f32_16x16x32_bf16 v[84:87], v[60:63], a[180:183], v[84:87]\n"
"v_rcp_f32 v224, v224\n"
"v_mfma_f32_16x16x32_bf16 v[88:91], v[60:63], a[184:187], v[88:91]\n"
"buffer_load_dwordx4 v19, s[48:51], s65 offen offset:3072 lds\n"
"v_mfma_f32_16x16x32_bf16 v[92:95], v[60:63], a[188:191], v[92:95]\n"
"ds_read_b64_tr_b16 v[60:61], v3 offset:28672\n"
"ds_read_b64_tr_b16 v[62:63], v3 offset:30720\n"
"v_mfma_f32_16x16x32_bf16 v[80:83], v[32:35], a[192:195], v[80:83]\n"
"s_sub_u32 s65, s65, 343936\n"
"s_add_u32 m0, s61, 49152\n"
"v_mfma_f32_16x16x32_bf16 v[84:87], v[32:35], a[196:199], v[84:87]\n"
"v_rcp_f32 v226, v226\n"
"v_mfma_f32_16x16x32_bf16 v[88:91], v[32:35], a[200:203], v[88:91]\n"
"buffer_load_dwordx4 v16, s[48:51], s65 offen offset:0 lds\n"
"v_mfma_f32_16x16x32_bf16 v[92:95], v[32:35], a[204:207], v[92:95]\n"
"v_mul_f32 v198, 0x41317218, v198\n"
"s_add_u32 s81, s75, s82\n"
"v_mfma_f32_16x16x32_bf16 v[80:83], v[36:39], a[208:211], v[80:83]\n"
"v_rcp_f32 v227, v227\n"
"v_mfma_f32_16x16x32_bf16 v[84:87], v[36:39], a[212:215], v[84:87]\n"
"v_mul_f32 v197, v197, v225\n"
"v_mul_f32 v199, 0x41317218, v199\n"
"v_mfma_f32_16x16x32_bf16 v[88:91], v[36:39], a[216:219], v[88:91]\n"
"buffer_load_dwordx4 v17, s[48:51], s65 offen offset:1024 lds\n"
"v_mfma_f32_16x16x32_bf16 v[92:95], v[36:39], a[220:223], v[92:95]\n"
"v_mul_f32 v196, v196, v224\n"
"v_mul_f32 v198, v198, v226\n"
"v_mfma_f32_16x16x32_bf16 v[80:83], v[40:43], a[224:227], v[80:83]\n"
"v_mul_f32 v199, v199, v227\n"
"v_mul_f32 v225, 0xc1800000, v201\n"
"v_mfma_f32_16x16x32_bf16 v[84:87], v[40:43], a[228:231], v[84:87]\n"
"s_waitcnt vmcnt(12)\n"
"s_barrier\n"
"ds_read_b64_tr_b16 v[32:33], v31 offset:0\n"
"ds_read_b64_tr_b16 v[34:35], v31 offset:2048\n"
"v_mfma_f32_16x16x32_bf16 v[88:91], v[40:43], a[232:235], v[88:91]\n"
"buffer_load_dwordx4 v18, s[48:51], s65 offen offset:2048 lds\n"
"ds_read_b64_tr_b16 v[36:37], v31 offset:4096\n"
"ds_read_b64_tr_b16 v[38:39], v31 offset:6144\n"
"v_mfma_f32_16x16x32_bf16 v[92:95], v[40:43], a[236:239], v[92:95]\n"
"v_mul_f32 v224, 0xc1800000, v200\n"
"v_mul_f32 v226, 0xc1800000, v202\n"
"v_mfma_f32_16x16x32_bf16 v[80:83], v[44:47], a[240:243], v[80:83]\n"
"ds_read_b64_tr_b16 v[40:41], v31 offset:8192\n"
"ds_read_b64_tr_b16 v[42:43], v31 offset:10240\n"
"v_mfma_f32_16x16x32_bf16 v[84:87], v[44:47], a[244:247], v[84:87]\n"
"v_mul_f32 v227, 0xc1800000, v203\n"
"v_mul_f32 v201, 0x41317218, v201\n"
"v_mfma_f32_16x16x32_bf16 v[88:91], v[44:47], a[248:251], v[88:91]\n"
"buffer_load_dwordx4 v19, s[48:51], s65 offen offset:3072 lds\n"
"v_mfma_f32_16x16x32_bf16 v[92:95], v[44:47], a[252:255], v[92:95]\n"
"s_add_u32 s65, s65, 229376\n"
"s_waitcnt lgkmcnt(6)\n"
"v_mfma_f32_16x16x32_bf16 v[80:83], v[48:51], a[0:3], v[80:83]\n"
"s_add_u32 m0, s61, 0\n"
"ds_read_b64_tr_b16 v[44:45], v31 offset:12288\n"
"v_mfma_f32_16x16x32_bf16 v[84:87], v[48:51], a[4:7], v[84:87]\n"
"ds_read_b64_tr_b16 v[46:47], v31 offset:14336\n"
"v_mul_f32 v200, 0x41317218, v200\n"
"v_mfma_f32_16x16x32_bf16 v[88:91], v[48:51], a[8:11], v[88:91]\n"
"buffer_load_dwordx4 v16, s[48:51], s65 offen offset:0 lds\n"
"v_mfma_f32_16x16x32_bf16 v[92:95], v[48:51], a[12:15], v[92:95]\n"
"ds_read_b64_tr_b16 v[48:49], v31 offset:16384\n"
"ds_read_b64_tr_b16 v[50:51], v31 offset:18432\n"
"v_mfma_f32_16x16x32_bf16 v[80:83], v[52:55], a[16:19], v[80:83]\n"
"v_exp_f32 v225, v225\n"
"v_mfma_f32_16x16x32_bf16 v[84:87], v[52:55], a[20:23], v[84:87]\n"
"s_waitcnt vmcnt(12)\n"
"buffer_store_dwordx2 v[246:247], v4, s[20:23], s78 offen offset:256 sc1\n"
"v_mfma_f32_16x16x32_bf16 v[88:91], v[52:55], a[24:27], v[88:91]\n"
"v_exp_f32 v224, v224\n"
"v_mfma_f32_16x16x32_bf16 v[92:95], v[52:55], a[28:31], v[92:95]\n"
"ds_read_b64_tr_b16 v[52:53], v31 offset:20480\n"
"ds_read_b64_tr_b16 v[54:55], v31 offset:22528\n"
"v_mfma_f32_16x16x32_bf16 v[80:83], v[56:59], a[32:35], v[80:83]\n"
"buffer_load_dwordx4 v17, s[48:51], s65 offen offset:1024 lds\n"
"v_mfma_f32_16x16x32_bf16 v[84:87], v[56:59], a[36:39], v[84:87]\n"
"v_exp_f32 v226, v226\n"
"v_mfma_f32_16x16x32_bf16 v[88:91], v[56:59], a[40:43], v[88:91]\n"
"v_exp_f32 v227, v227\n"
"v_mfma_f32_16x16x32_bf16 v[92:95], v[56:59], a[44:47], v[92:95]\n"
"buffer_load_dwordx4 v18, s[48:51], s65 offen offset:2048 lds\n"
"v_mfma_f32_16x16x32_bf16 v[80:83], v[60:63], a[48:51], v[80:83]\n"
"ds_read_b64_tr_b16 v[56:57], v31 offset:24576\n"
"ds_read_b64_tr_b16 v[58:59], v31 offset:26624\n"
"v_mfma_f32_16x16x32_bf16 v[84:87], v[60:63], a[52:55], v[84:87]\n"
"v_add_f32 v225, 1.0, v225\n"
"v_add_f32 v224, 1.0, v224\n"
"v_mfma_f32_16x16x32_bf16 v[88:91], v[60:63], a[56:59], v[88:91]\n"
"buffer_load_dwordx4 v19, s[48:51], s65 offen offset:3072 lds\n"
"v_mfma_f32_16x16x32_bf16 v[92:95], v[60:63], a[60:63], v[92:95]\n"
"ds_read_b64_tr_b16 v[60:61], v31 offset:28672\n"
"s_waitcnt lgkmcnt(5)\n"
"v_mfma_f32_16x16x32_bf16 v[64:67], v[32:35], a[64:67], 0\n"
"v_cvt_pk_bf16_f32 v80, v80, v81\n"
"ds_read_b64_tr_b16 v[62:63], v31 offset:30720\n"
"v_mfma_f32_16x16x32_bf16 v[68:71], v[32:35], a[68:71], 0\n"
"s_add_u32 m0, s61, 98304\n"
"v_cvt_pk_bf16_f32 v81, v82, v83\n"
"v_mfma_f32_16x16x32_bf16 v[72:75], v[32:35], a[72:75], 0\n"
"v_cvt_pk_bf16_f32 v88, v88, v89\n"
"v_cvt_pk_bf16_f32 v82, v84, v85\n"
"v_mfma_f32_16x16x32_bf16 v[76:79], v[32:35], a[76:79], 0\n"
"v_cvt_pk_bf16_f32 v83, v86, v87\n"
"s_sub_u32 s65, s65, 114688\n"
"v_mfma_f32_16x16x32_bf16 v[64:67], v[36:39], a[80:83], v[64:67]\n"
"buffer_store_dwordx4 v[80:83], v1, s[20:23], s68 offen sc1\n"
"v_mfma_f32_16x16x32_bf16 v[68:71], v[36:39], a[84:87], v[68:71]\n"
"v_cvt_pk_bf16_f32 v89, v90, v91\n"
"v_cvt_pk_bf16_f32 v90, v92, v93\n"
"v_mfma_f32_16x16x32_bf16 v[72:75], v[36:39], a[88:91], v[72:75]\n"
"v_cvt_pk_bf16_f32 v91, v94, v95\n"
"v_cndmask_b32 v208, v208, v80, vcc\n"
"v_mfma_f32_16x16x32_bf16 v[76:79], v[36:39], a[92:95], v[76:79]\n"
"v_cndmask_b32 v209, v209, v81, vcc\n"
"v_cndmask_b32 v210, v210, v82, vcc\n"
"v_mfma_f32_16x16x32_bf16 v[64:67], v[40:43], a[96:99], v[64:67]\n"
"buffer_load_dwordx4 v16, s[48:51], s65 offen offset:0 lds\n"
"v_mfma_f32_16x16x32_bf16 v[68:71], v[40:43], a[100:103], v[68:71]\n"
"s_waitcnt vmcnt(11)\n"
"s_barrier\n"
"ds_read_b64_tr_b16 v[32:33], v30 offset:16384\n"
"ds_read_b64_tr_b16 v[34:35], v30 offset:18432\n"
"v_mfma_f32_16x16x32_bf16 v[72:75], v[40:43], a[104:107], v[72:75]\n"
"ds_read_b64_tr_b16 v[36:37], v30 offset:20480\n"
"ds_read_b64_tr_b16 v[38:39], v30 offset:22528\n"
"v_mfma_f32_16x16x32_bf16 v[76:79], v[40:43], a[108:111], v[76:79]\n"
"ds_read_b64_tr_b16 v[40:41], v30 offset:24576\n"
"ds_read_b64_tr_b16 v[42:43], v30 offset:26624\n"
"v_mfma_f32_16x16x32_bf16 v[64:67], v[44:47], a[112:115], v[64:67]\n"
"buffer_load_dwordx4 v17, s[48:51], s65 offen offset:1024 lds\n"
"v_mfma_f32_16x16x32_bf16 v[68:71], v[44:47], a[116:119], v[68:71]\n"
"v_cndmask_b32 v211, v211, v83, vcc\n"
"s_add_u32 s68, s28, 15073280\n"
"v_mfma_f32_16x16x32_bf16 v[72:75], v[44:47], a[120:123], v[72:75]\n"
"s_cmp_ge_u32 s33, 0x40000000\n"
"s_cselect_b64 vcc, -1, 0\n"
"v_mfma_f32_16x16x32_bf16 v[76:79], v[44:47], a[124:127], v[76:79]\n"
"buffer_store_dwordx4 v[88:91], v1, s[20:23], s69 offen sc1\n"
"v_mfma_f32_16x16x32_bf16 v[64:67], v[48:51], a[128:131], v[64:67]\n"
"ds_read_b64_tr_b16 v[44:45], v30 offset:28672\n"
"ds_read_b64_tr_b16 v[46:47], v30 offset:30720\n"
"v_mfma_f32_16x16x32_bf16 v[68:71], v[48:51], a[132:135], v[68:71]\n"
"v_cndmask_b32 v208, v208, v88, vcc\n"
"v_cndmask_b32 v209, v209, v89, vcc\n"
"v_mfma_f32_16x16x32_bf16 v[72:75], v[48:51], a[136:139], v[72:75]\n"
"buffer_load_dwordx4 v18, s[48:51], s65 offen offset:2048 lds\n"
"v_mfma_f32_16x16x32_bf16 v[76:79], v[48:51], a[140:143], v[76:79]\n"
"ds_read_b64_tr_b16 v[48:49], v30 offset:32768\n"
"s_waitcnt lgkmcnt(7)\n"
"v_mfma_f32_16x16x32_bf16 v[64:67], v[52:55], a[144:147], v[64:67]\n"
"ds_read_b64_tr_b16 v[50:51], v30 offset:34816\n"
"v_cndmask_b32 v210, v210, v90, vcc\n"
"v_mfma_f32_16x16x32_bf16 v[68:71], v[52:55], a[148:151], v[68:71]\n"
"buffer_load_dwordx4 v19, s[48:51], s65 offen offset:3072 lds\n"
"s_add_u32 s65, s65, 229376\n"
"s_add_u32 m0, s61, 114688\n"
"v_mfma_f32_16x16x32_bf16 v[72:75], v[52:55], a[152:155], v[72:75]\n"
"v_cndmask_b32 v211, v211, v91, vcc\n"
"s_add_u32 s69, s57, 15073280\n"
"v_mfma_f32_16x16x32_bf16 v[76:79], v[52:55], a[156:159], v[76:79]\n"
"ds_read_b64_tr_b16 v[52:53], v30 offset:36864\n"
"ds_read_b64_tr_b16 v[54:55], v30 offset:38912\n"
"v_mfma_f32_16x16x32_bf16 v[64:67], v[56:59], a[160:163], v[64:67]\n"
"buffer_load_dwordx4 v16, s[48:51], s65 offen offset:0 lds\n"
"v_mfma_f32_16x16x32_bf16 v[68:71], v[56:59], a[164:167], v[68:71]\n"
"s_cmp_ge_u32 s28, 0x40000000\n"
"s_cselect_b64 vcc, -1, 0\n"
"v_mfma_f32_16x16x32_bf16 v[72:75], v[56:59], a[168:171], v[72:75]\n"
"v_add_f32 v226, 1.0, v226\n"
"v_add_f32 v227, 1.0, v227\n"
"v_mfma_f32_16x16x32_bf16 v[76:79], v[56:59], a[172:175], v[76:79]\n"
"buffer_load_dwordx4 v17, s[48:51], s65 offen offset:1024 lds\n"
"v_mfma_f32_16x16x32_bf16 v[64:67], v[60:63], a[176:179], v[64:67]\n"
"ds_read_b64_tr_b16 v[56:57], v30 offset:40960\n"
"ds_read_b64_tr_b16 v[58:59], v30 offset:43008\n"
"v_mfma_f32_16x16x32_bf16 v[68:71], v[60:63], a[180:183], v[68:71]\n"
"v_rcp_f32 v225, v225\n"
"v_mfma_f32_16x16x32_bf16 v[72:75], v[60:63], a[184:187], v[72:75]\n"
"buffer_load_dwordx4 v18, s[48:51], s65 offen offset:2048 lds\n"
"v_mfma_f32_16x16x32_bf16 v[76:79], v[60:63], a[188:191], v[76:79]\n"
"ds_read_b64_tr_b16 v[60:61], v30 offset:45056\n"
"ds_read_b64_tr_b16 v[62:63], v30 offset:47104\n"
"v_mfma_f32_16x16x32_bf16 v[64:67], v[32:35], a[192:195], v[64:67]\n"
"v_rcp_f32 v224, v224\n"
"v_mfma_f32_16x16x32_bf16 v[68:71], v[32:35], a[196:199], v[68:71]\n"
"buffer_load_dwordx4 v19, s[48:51], s65 offen offset:3072 lds\n"
"v_mfma_f32_16x16x32_bf16 v[72:75], v[32:35], a[200:203], v[72:75]\n"
"s_sub_u32 s65, s65, 343936\n"
"s_add_u32 m0, s61, 131072\n"
"v_mfma_f32_16x16x32_bf16 v[76:79], v[32:35], a[204:207], v[76:79]\n"
"v_mul_f32 v203, 0x41317218, v203\n"
"s_waitcnt lgkmcnt(4)\n"
"v_mfma_f32_16x16x32_bf16 v[64:67], v[36:39], a[208:211], v[64:67]\n"
"v_rcp_f32 v227, v227\n"
"v_mfma_f32_16x16x32_bf16 v[68:71], v[36:39], a[212:215], v[68:71]\n"
"buffer_load_dwordx4 v16, s[48:51], s65 offen offset:0 lds\n"
"v_mfma_f32_16x16x32_bf16 v[72:75], v[36:39], a[216:219], v[72:75]\n"
"v_rcp_f32 v226, v226\n"
"v_mfma_f32_16x16x32_bf16 v[76:79], v[36:39], a[220:223], v[76:79]\n"
"v_mul_f32 v201, v201, v225\n"
"v_mul_f32 v202, 0x41317218, v202\n"
"v_mfma_f32_16x16x32_bf16 v[64:67], v[40:43], a[224:227], v[64:67]\n"
"buffer_load_dwordx4 v17, s[48:51], s65 offen offset:1024 lds\n"
"v_mfma_f32_16x16x32_bf16 v[68:71], v[40:43], a[228:231], v[68:71]\n"
"s_waitcnt vmcnt(12)\n"
"s_barrier\n"
"ds_read_b64_tr_b16 v[32:33], v30 offset:49152\n"
"ds_read_b64_tr_b16 v[34:35], v30 offset:51200\n"
"v_mfma_f32_16x16x32_bf16 v[72:75], v[40:43], a[232:235], v[72:75]\n"
"ds_read_b64_tr_b16 v[36:37], v30 offset:53248\n"
"ds_read_b64_tr_b16 v[38:39], v30 offset:55296\n"
"v_mfma_f32_16x16x32_bf16 v[76:79], v[40:43], a[236:239], v[76:79]\n"
"buffer_load_dwordx4 v18, s[48:51], s65 offen offset:2048 lds\n"
"v_mfma_f32_16x16x32_bf16 v[64:67], v[44:47], a[240:243], v[64:67]\n"
"ds_read_b64_tr_b16 v[40:41], v30 offset:57344\n"
"ds_read_b64_tr_b16 v[42:43], v30 offset:59392\n"
"v_mfma_f32_16x16x32_bf16 v[68:71], v[44:47], a[244:247], v[68:71]\n"
"v_mul_f32 v200, v200, v224\n"
"v_mul_f32 v203, v203, v227\n"
"v_mfma_f32_16x16x32_bf16 v[72:75], v[44:47], a[248:251], v[72:75]\n"
"buffer_load_dwordx4 v19, s[48:51], s65 offen offset:3072 lds\n"
"s_add_u32 s65, s65, 229376\n"
"s_add_u32 m0, s61, 147456\n"
"v_mfma_f32_16x16x32_bf16 v[76:79], v[44:47], a[252:255], v[76:79]\n"
"ds_read_b64_tr_b16 v[44:45], v30 offset:61440\n"
"ds_read_b64_tr_b16 v[46:47], v30 offset:63488\n"
"v_mfma_f32_16x16x32_bf16 v[64:67], v[48:51], a[0:3], v[64:67]\n"
"v_mul_f32 v202, v202, v226\n"
"v_mul_f32 v225, 0xc1800000, v205\n"
"v_mfma_f32_16x16x32_bf16 v[68:71], v[48:51], a[4:7], v[68:71]\n"
"buffer_load_dwordx4 v16, s[48:51], s65 offen offset:0 lds\n"
"v_mfma_f32_16x16x32_bf16 v[72:75], v[48:51], a[8:11], v[72:75]\n"
"v_mul_f32 v224, 0xc1800000, v204\n"
"v_mul_f32 v227, 0xc1800000, v207\n"
"v_mfma_f32_16x16x32_bf16 v[76:79], v[48:51], a[12:15], v[76:79]\n"
"ds_read_b64_tr_b16 v[48:49], v30 offset:0\n"
"ds_read_b64_tr_b16 v[50:51], v30 offset:2048\n"
"v_mfma_f32_16x16x32_bf16 v[64:67], v[52:55], a[16:19], v[64:67]\n"
"buffer_load_dwordx4 v17, s[48:51], s65 offen offset:1024 lds\n"
"v_mfma_f32_16x16x32_bf16 v[68:71], v[52:55], a[20:23], v[68:71]\n"
"v_mul_f32 v226, 0xc1800000, v206\n"
"v_mul_f32 v205, 0x41317218, v205\n"
"v_mfma_f32_16x16x32_bf16 v[72:75], v[52:55], a[24:27], v[72:75]\n"
"v_exp_f32 v225, v225\n"
"v_mfma_f32_16x16x32_bf16 v[76:79], v[52:55], a[28:31], v[76:79]\n"
"buffer_load_dwordx4 v18, s[48:51], s65 offen offset:2048 lds\n"
"s_waitcnt lgkmcnt(4)\n"
"v_mfma_f32_16x16x32_bf16 v[64:67], v[56:59], a[32:35], v[64:67]\n"
"ds_read_b64_tr_b16 v[52:53], v30 offset:4096\n"
"ds_read_b64_tr_b16 v[54:55], v30 offset:6144\n"
"v_mfma_f32_16x16x32_bf16 v[68:71], v[56:59], a[36:39], v[68:71]\n"
"v_exp_f32 v227, v227\n"
"v_mfma_f32_16x16x32_bf16 v[72:75], v[56:59], a[40:43], v[72:75]\n"
"buffer_load_dwordx4 v19, s[48:51], s65 offen offset:3072 lds\n"
"s_sub_u32 s65, s65, 114688\n"
"s_add_u32 m0, s61, 65536\n"
"v_mfma_f32_16x16x32_bf16 v[76:79], v[56:59], a[44:47], v[76:79]\n"
"ds_read_b64_tr_b16 v[56:57], v30 offset:8192\n"
"ds_read_b64_tr_b16 v[58:59], v30 offset:10240\n"
"v_mfma_f32_16x16x32_bf16 v[64:67], v[60:63], a[48:51], v[64:67]\n"
"v_exp_f32 v224, v224\n"
"v_mfma_f32_16x16x32_bf16 v[68:71], v[60:63], a[52:55], v[68:71]\n"
"buffer_load_dwordx4 v16, s[48:51], s65 offen offset:0 lds\n"
"v_mfma_f32_16x16x32_bf16 v[72:75], v[60:63], a[56:59], v[72:75]\n"
"v_exp_f32 v226, v226\n"
"v_mfma_f32_16x16x32_bf16 v[76:79], v[60:63], a[60:63], v[76:79]\n"
"ds_read_b64_tr_b16 v[60:61], v30 offset:12288\n"
"ds_read_b64_tr_b16 v[62:63], v30 offset:14336\n"
"v_mfma_f32_16x16x32_bf16 v[80:83], v[32:35], a[64:67], 0\n"
"buffer_load_dwordx4 v17, s[48:51], s65 offen offset:1024 lds\n"
"v_cvt_pk_bf16_f32 v64, v64, v65\n"
"v_mfma_f32_16x16x32_bf16 v[84:87], v[32:35], a[68:71], 0\n"
"v_cvt_pk_bf16_f32 v65, v66, v67\n"
"v_cvt_pk_bf16_f32 v72, v72, v73\n"
"v_mfma_f32_16x16x32_bf16 v[88:91], v[32:35], a[72:75], 0\n"
"v_cvt_pk_bf16_f32 v66, v68, v69\n"
"v_cvt_pk_bf16_f32 v67, v70, v71\n"
"v_mfma_f32_16x16x32_bf16 v[92:95], v[32:35], a[76:79], 0\n"
"buffer_load_dwordx4 v18, s[48:51], s65 offen offset:2048 lds\n"
"v_mfma_f32_16x16x32_bf16 v[80:83], v[36:39], a[80:83], v[80:83]\n"
"v_cvt_pk_bf16_f32 v73, v74, v75\n"
"v_cvt_pk_bf16_f32 v74, v76, v77\n"
"v_mfma_f32_16x16x32_bf16 v[84:87], v[36:39], a[84:87], v[84:87]\n"
"v_cvt_pk_bf16_f32 v75, v78, v79\n"
"v_cndmask_b32 v212, v212, v64, vcc\n"
"v_mfma_f32_16x16x32_bf16 v[88:91], v[36:39], a[88:91], v[88:91]\n"
"buffer_load_dwordx4 v19, s[48:51], s65 offen offset:3072 lds\n"
"v_mfma_f32_16x16x32_bf16 v[92:95], v[36:39], a[92:95], v[92:95]\n"
"s_add_u32 s65, s65, 229376\n"
"s_add_u32 m0, s61, 81920\n"
"v_mfma_f32_16x16x32_bf16 v[80:83], v[40:43], a[96:99], v[80:83]\n"
"v_cndmask_b32 v213, v213, v65, vcc\n"
"v_cndmask_b32 v214, v214, v66, vcc\n"
"v_mfma_f32_16x16x32_bf16 v[84:87], v[40:43], a[100:103], v[84:87]\n"
"buffer_store_dwordx4 v[64:67], v1, s[20:23], s68 offen sc1\n"
"s_waitcnt vmcnt(13)\n"
"s_barrier\n"
"ds_read_b64_tr_b16 v[32:33], v31 offset:32768\n"
"ds_read_b64_tr_b16 v[34:35], v31 offset:34816\n"
"v_mfma_f32_16x16x32_bf16 v[88:91], v[40:43], a[104:107], v[88:91]\n"
"ds_read_b64_tr_b16 v[36:37], v31 offset:36864\n"
"ds_read_b64_tr_b16 v[38:39], v31 offset:38912\n"
"v_mfma_f32_16x16x32_bf16 v[92:95], v[40:43], a[108:111], v[92:95]\n"
"ds_read_b64_tr_b16 v[40:41], v31 offset:40960\n"
"s_waitcnt lgkmcnt(5)\n"
"v_mfma_f32_16x16x32_bf16 v[80:83], v[44:47], a[112:115], v[80:83]\n"
"buffer_load_dwordx2 v[238:239], v244, s[20:23], s52 offen offset:256 sc1\n"
"v_mfma_f32_16x16x32_bf16 v[84:87], v[44:47], a[116:119], v[84:87]\n"
"ds_read_b64_tr_b16 v[42:43], v31 offset:43008\n"
"v_cndmask_b32 v215, v215, v67, vcc\n"
"v_mfma_f32_16x16x32_bf16 v[88:91], v[44:47], a[120:123], v[88:91]\n"
"s_cmp_ge_u32 s57, 0x40000000\n"
"s_cselect_b64 vcc, -1, 0\n"
"v_mfma_f32_16x16x32_bf16 v[92:95], v[44:47], a[124:127], v[92:95]\n"
"buffer_load_dwordx4 v16, s[48:51], s65 offen offset:0 lds\n"
"v_mfma_f32_16x16x32_bf16 v[80:83], v[48:51], a[128:131], v[80:83]\n"
"ds_read_b64_tr_b16 v[44:45], v31 offset:45056\n"
"ds_read_b64_tr_b16 v[46:47], v31 offset:47104\n"
"v_mfma_f32_16x16x32_bf16 v[84:87], v[48:51], a[132:135], v[84:87]\n"
"buffer_store_dwordx4 v[72:75], v1, s[20:23], s69 offen sc1\n"
"v_mfma_f32_16x16x32_bf16 v[88:91], v[48:51], a[136:139], v[88:91]\n"
"v_cndmask_b32 v212, v212, v72, vcc\n"
"v_cndmask_b32 v213, v213, v73, vcc\n"
"v_mfma_f32_16x16x32_bf16 v[92:95], v[48:51], a[140:143], v[92:95]\n"
"ds_read_b64_tr_b16 v[48:49], v31 offset:49152\n"
"ds_read_b64_tr_b16 v[50:51], v31 offset:51200\n"
"v_mfma_f32_16x16x32_bf16 v[80:83], v[52:55], a[144:147], v[80:83]\n"
"buffer_load_dwordx4 v17, s[48:51], s65 offen offset:1024 lds\n"
"v_mfma_f32_16x16x32_bf16 v[84:87], v[52:55], a[148:151], v[84:87]\n"
"v_cndmask_b32 v214, v214, v74, vcc\n"
"v_cndmask_b32 v215, v215, v75, vcc\n"
"v_mfma_f32_16x16x32_bf16 v[88:91], v[52:55], a[152:155], v[88:91]\n"
"s_add_u32 s68, s29, 15081472\n"
"s_add_u32 s69, s58, 15081472\n"
"v_mfma_f32_16x16x32_bf16 v[92:95], v[52:55], a[156:159], v[92:95]\n"
"buffer_load_dwordx4 v18, s[48:51], s65 offen offset:2048 lds\n"
"v_mfma_f32_16x16x32_bf16 v[80:83], v[56:59], a[160:163], v[80:83]\n"
"ds_read_b64_tr_b16 v[52:53], v31 offset:53248\n"
"ds_read_b64_tr_b16 v[54:55], v31 offset:55296\n"
"v_mfma_f32_16x16x32_bf16 v[84:87], v[56:59], a[164:167], v[84:87]\n"
"v_add_f32 v225, 1.0, v225\n"
"v_add_f32 v227, 1.0, v227\n"
"v_mfma_f32_16x16x32_bf16 v[88:91], v[56:59], a[168:171], v[88:91]\n"
"buffer_load_dwordx4 v19, s[48:51], s65 offen offset:3072 lds\n"
"v_mfma_f32_16x16x32_bf16 v[92:95], v[56:59], a[172:175], v[92:95]\n"
"ds_read_b64_tr_b16 v[56:57], v31 offset:57344\n"
"ds_read_b64_tr_b16 v[58:59], v31 offset:59392\n"
"v_mfma_f32_16x16x32_bf16 v[80:83], v[60:63], a[176:179], v[80:83]\n"
"v_add_f32 v224, 1.0, v224\n"
"v_add_f32 v226, 1.0, v226\n"
"v_mfma_f32_16x16x32_bf16 v[84:87], v[60:63], a[180:183], v[84:87]\n"
"v_rcp_f32 v225, v225\n"
"v_mfma_f32_16x16x32_bf16 v[88:91], v[60:63], a[184:187], v[88:91]\n"
"v_rcp_f32 v227, v227\n"
"v_mfma_f32_16x16x32_bf16 v[92:95], v[60:63], a[188:191], v[92:95]\n"
"ds_read_b64_tr_b16 v[60:61], v31 offset:61440\n"
"s_waitcnt lgkmcnt(5)\n"
"v_mfma_f32_16x16x32_bf16 v[80:83], v[32:35], a[192:195], v[80:83]\n"
"ds_read_b64_tr_b16 v[62:63], v31 offset:63488\n"
"v_mul_f32 v204, 0x41317218, v204\n"
"v_mfma_f32_16x16x32_bf16 v[84:87], v[32:35], a[196:199], v[84:87]\n"
"v_rcp_f32 v224, v224\n"
"v_mfma_f32_16x16x32_bf16 v[88:91], v[32:35], a[200:203], v[88:91]\n"
"v_mul_f32 v207, 0x41317218, v207\n"
"v_mul_f32 v205, v205, v225\n"
"v_mfma_f32_16x16x32_bf16 v[92:95], v[32:35], a[204:207], v[92:95]\n"
"v_rcp_f32 v226, v226\n"
"v_mfma_f32_16x16x32_bf16 v[80:83], v[36:39], a[208:211], v[80:83]\n"
"v_mul_f32 v206, 0x41317218, v206\n"
"v_mul_f32 v207, v207, v227\n"
"v_mfma_f32_16x16x32_bf16 v[84:87], v[36:39], a[212:215], v[84:87]\n"
"v_mul_f32 v204, v204, v224\n"
"v_mul_f32 v206, v206, v226\n"
"v_mfma_f32_16x16x32_bf16 v[88:91], v[36:39], a[216:219], v[88:91]\n"
"v_mov_b32 v225, s66\n"
"v_mov_b32 v227, s66\n"
"v_mfma_f32_16x16x32_bf16 v[92:95], v[36:39], a[220:223], v[92:95]\n"
"v_mfma_f32_16x16x32_bf16 v[80:83], v[40:43], a[224:227], v[80:83]\n"
"v_mfma_f32_16x16x32_bf16 v[84:87], v[40:43], a[228:231], v[84:87]\n"
"s_waitcnt vmcnt(11)\n"
"s_barrier\n"
"ds_read_b64_tr_b16 v[32:33], v3 offset:0\n"
"ds_read_b64_tr_b16 v[34:35], v3 offset:2048\n"
"v_mfma_f32_16x16x32_bf16 v[88:91], v[40:43], a[232:235], v[88:91]\n"
"ds_read_b64_tr_b16 v[36:37], v3 offset:4096\n"
"ds_read_b64_tr_b16 v[38:39], v3 offset:6144\n"
"v_mfma_f32_16x16x32_bf16 v[92:95], v[40:43], a[236:239], v[92:95]\n"
"ds_read_b64_tr_b16 v[40:41], v3 offset:8192\n"
"ds_read_b64_tr_b16 v[42:43], v3 offset:10240\n"
"v_mfma_f32_16x16x32_bf16 v[80:83], v[44:47], a[240:243], v[80:83]\n"
"v_mfma_f32_16x16x32_bf16 v[84:87], v[44:47], a[244:247], v[84:87]\n"
"v_mfma_f32_16x16x32_bf16 v[88:91], v[44:47], a[248:251], v[88:91]\n"
"v_mfma_f32_16x16x32_bf16 v[92:95], v[44:47], a[252:255], v[92:95]\n"
"ds_read_b64_tr_b16 v[44:45], v3 offset:12288\n"
"ds_read_b64_tr_b16 v[46:47], v3 offset:14336\n"
"v_mfma_f32_16x16x32_bf16 v[80:83], v[48:51], a[0:3], v[80:83]\n"
"s_waitcnt vmcnt(5)\n"
"v_cmp_ne_u64 vcc, s[70:71], v[238:239]\n"
"v_mfma_f32_16x16x32_bf16 v[84:87], v[48:51], a[4:7], v[84:87]\n"
"v_mfma_f32_16x16x32_bf16 v[88:91], v[48:51], a[8:11], v[88:91]\n"
"s_cbranch_vccnz L_spinsA_%=\n"
"L_spinretsA_%=:\n"
"v_mfma_f32_16x16x32_bf16 v[92:95], v[48:51], a[12:15], v[92:95]\n"
"ds_read_b64_tr_b16 v[48:49], v3 offset:16384\n"
"ds_read_b64_tr_b16 v[50:51], v3 offset:18432\n"
"s_waitcnt lgkmcnt(8)\n"
"v_mfma_f32_16x16x32_bf16 v[80:83], v[52:55], a[16:19], v[80:83]\n"
"buffer_load_dwordx4 v[100:103], v1, s[20:23], s80 offen offset:1024 sc1\n"
"v_mfma_f32_16x16x32_bf16 v[84:87], v[52:55], a[20:23], v[84:87]\n"
"s_cmp_ge_u32 s29, 0x40000000\n"
"s_cselect_b64 vcc, -1, 0\n"
"v_mfma_f32_16x16x32_bf16 v[88:91], v[52:55], a[24:27], v[88:91]\n"
"v_mfma_f32_16x16x32_bf16 v[92:95], v[52:55], a[28:31], v[92:95]\n"
"ds_read_b64_tr_b16 v[54:55], v3 offset:22528\n"
"ds_read_b64_tr_b16 v[52:53], v3 offset:20480\n"
"v_mfma_f32_16x16x32_bf16 v[80:83], v[56:59], a[32:35], v[80:83]\n"
"v_mfma_f32_16x16x32_bf16 v[84:87], v[56:59], a[36:39], v[84:87]\n"
"buffer_load_dwordx4 v[104:107], v1, s[20:23], s80 offen offset:2048 sc1\n"
"v_mfma_f32_16x16x32_bf16 v[88:91], v[56:59], a[40:43], v[88:91]\n"
"v_mfma_f32_16x16x32_bf16 v[92:95], v[56:59], a[44:47], v[92:95]\n"
"ds_read_b64_tr_b16 v[56:57], v3 offset:24576\n"
"ds_read_b64_tr_b16 v[58:59], v3 offset:26624\n"
"v_mfma_f32_16x16x32_bf16 v[80:83], v[60:63], a[48:51], v[80:83]\n"
"v_mfma_f32_16x16x32_bf16 v[84:87], v[60:63], a[52:55], v[84:87]\n"
"v_mfma_f32_16x16x32_bf16 v[88:91], v[60:63], a[56:59], v[88:91]\n"
"v_mfma_f32_16x16x32_bf16 v[92:95], v[60:63], a[60:63], v[92:95]\n"
"buffer_load_dwordx4 v[108:111], v1, s[20:23], s80 offen offset:3072 sc1\n"
"v_mfma_f32_16x16x32_bf16 v[64:67], v[32:35], a[64:67], 0\n"
"ds_read_b64_tr_b16 v[60:61], v3 offset:28672\n"
"ds_read_b64_tr_b16 v[62:63], v3 offset:30720\n"
"v_mfma_f32_16x16x32_bf16 v[68:71], v[32:35], a[68:71], 0\n"
"v_cvt_pk_bf16_f32 v80, v80, v81\n"
"v_cvt_pk_bf16_f32 v81, v82, v83\n"
"v_cvt_pk_bf16_f32 v88, v88, v89\n"
"v_cvt_pk_bf16_f32 v89, v90, v91\n"
"v_mfma_f32_16x16x32_bf16 v[72:75], v[32:35], a[72:75], 0\n"
"v_cvt_pk_bf16_f32 v82, v84, v85\n"
"v_cvt_pk_bf16_f32 v83, v86, v87\n"
"v_mfma_f32_16x16x32_bf16 v[76:79], v[32:35], a[76:79], 0\n"
"v_cvt_pk_bf16_f32 v90, v92, v93\n"
"s_waitcnt lgkmcnt(4)\n"
"v_mfma_f32_16x16x32_bf16 v[64:67], v[36:39], a[80:83], v[64:67]\n"
"buffer_store_dwordx4 v[80:83], v1, s[20:23], s68 offen sc1\n"
"v_mfma_f32_16x16x32_bf16 v[68:71], v[36:39], a[84:87], v[68:71]\n"
"v_cvt_pk_bf16_f32 v91, v94, v95\n"
"v_cndmask_b32 v212, v212, v80, vcc\n"
"v_mfma_f32_16x16x32_bf16 v[72:75], v[36:39], a[88:91], v[72:75]\n"
"v_cndmask_b32 v213, v213, v81, vcc\n"
"v_cndmask_b32 v214, v214, v82, vcc\n"
"v_mfma_f32_16x16x32_bf16 v[76:79], v[36:39], a[92:95], v[76:79]\n"
"v_cndmask_b32 v215, v215, v83, vcc\n"
"v_mfma_f32_16x16x32_bf16 v[64:67], v[40:43], a[96:99], v[64:67]\n"
"buffer_load_dwordx4 v[112:115], v1, s[20:23], s81 offen offset:0 sc1\n"
"v_mfma_f32_16x16x32_bf16 v[68:71], v[40:43], a[100:103], v[68:71]\n"
"s_waitcnt vmcnt(5)\n"
"s_barrier\n"
"ds_read_b64_tr_b16 v[32:33], v31 offset:0\n"
"ds_read_b64_tr_b16 v[34:35], v31 offset:2048\n"
"v_mfma_f32_16x16x32_bf16 v[72:75], v[40:43], a[104:107], v[72:75]\n"
"ds_read_b64_tr_b16 v[36:37], v31 offset:4096\n"
"ds_read_b64_tr_b16 v[38:39], v31 offset:6144\n"
"v_mfma_f32_16x16x32_bf16 v[76:79], v[40:43], a[108:111], v[76:79]\n"
"buffer_load_dwordx4 v[116:119], v1, s[20:23], s81 offen offset:1024 sc1\n"
"ds_read_b64_tr_b16 v[40:41], v31 offset:8192\n"
"v_mfma_f32_16x16x32_bf16 v[64:67], v[44:47], a[112:115], v[64:67]\n"
"ds_read_b64_tr_b16 v[42:43], v31 offset:10240\n"
"v_mfma_f32_16x16x32_bf16 v[68:71], v[44:47], a[116:119], v[68:71]\n"
"s_cmp_ge_u32 s58, 0x40000000\n"
"s_cselect_b64 vcc, -1, 0\n"
"v_mfma_f32_16x16x32_bf16 v[72:75], v[44:47], a[120:123], v[72:75]\n"
"buffer_store_dwordx4 v[88:91], v1, s[20:23], s69 offen sc1\n"
"v_mfma_f32_16x16x32_bf16 v[76:79], v[44:47], a[124:127], v[76:79]\n"
"ds_read_b64_tr_b16 v[44:45], v31 offset:12288\n"
"ds_read_b64_tr_b16 v[46:47], v31 offset:14336\n"
"v_mfma_f32_16x16x32_bf16 v[64:67], v[48:51], a[128:131], v[64:67]\n"
"v_cndmask_b32 v212, v212, v88, vcc\n"
"v_cndmask_b32 v213, v213, v89, vcc\n"
"v_mfma_f32_16x16x32_bf16 v[68:71], v[48:51], a[132:135], v[68:71]\n"
"buffer_load_dwordx4 v[120:123], v1, s[20:23], s81 offen offset:2048 sc1\n"
"v_mfma_f32_16x16x32_bf16 v[72:75], v[48:51], a[136:139], v[72:75]\n"
"v_cndmask_b32 v214, v214, v90, vcc\n"
"v_cndmask_b32 v215, v215, v91, vcc\n"
"v_mfma_f32_16x16x32_bf16 v[76:79], v[48:51], a[140:143], v[76:79]\n"
"ds_read_b64_tr_b16 v[48:49], v31 offset:16384\n"
"ds_read_b64_tr_b16 v[50:51], v31 offset:18432\n"
"v_mfma_f32_16x16x32_bf16 v[64:67], v[52:55], a[144:147], v[64:67]\n"
"v_mfma_f32_16x16x32_bf16 v[68:71], v[52:55], a[148:151], v[68:71]\n"
"v_mfma_f32_16x16x32_bf16 v[72:75], v[52:55], a[152:155], v[72:75]\n"
"v_mfma_f32_16x16x32_bf16 v[76:79], v[52:55], a[156:159], v[76:79]\n"
"ds_read_b64_tr_b16 v[52:53], v31 offset:20480\n"
"s_waitcnt lgkmcnt(5)\n"
"v_mfma_f32_16x16x32_bf16 v[64:67], v[56:59], a[160:163], v[64:67]\n"
"buffer_load_dwordx4 v[124:127], v1, s[20:23], s81 offen offset:3072 sc1\n"
"v_mfma_f32_16x16x32_bf16 v[68:71], v[56:59], a[164:167], v[68:71]\n"
"ds_read_b64_tr_b16 v[54:55], v31 offset:22528\n"
"v_mfma_f32_16x16x32_bf16 v[72:75], v[56:59], a[168:171], v[72:75]\n"
"v_mfma_f32_16x16x32_bf16 v[76:79], v[56:59], a[172:175], v[76:79]\n"
"ds_read_b64_tr_b16 v[56:57], v31 offset:24576\n"
"ds_read_b64_tr_b16 v[58:59], v31 offset:26624\n"
"v_mfma_f32_16x16x32_bf16 v[64:67], v[60:63], a[176:179], v[64:67]\n"
"v_mfma_f32_16x16x32_bf16 v[68:71], v[60:63], a[180:183], v[68:71]\n"
"v_mfma_f32_16x16x32_bf16 v[72:75], v[60:63], a[184:187], v[72:75]\n"
"v_mfma_f32_16x16x32_bf16 v[76:79], v[60:63], a[188:191], v[76:79]\n"
"ds_read_b64_tr_b16 v[60:61], v31 offset:28672\n"
"ds_read_b64_tr_b16 v[62:63], v31 offset:30720\n"
"v_mfma_f32_16x16x32_bf16 v[64:67], v[32:35], a[192:195], v[64:67]\n"
"v_mfma_f32_16x16x32_bf16 v[68:71], v[32:35], a[196:199], v[68:71]\n"
"v_mfma_f32_16x16x32_bf16 v[72:75], v[32:35], a[200:203], v[72:75]\n"
"v_mfma_f32_16x16x32_bf16 v[76:79], v[32:35], a[204:207], v[76:79]\n"
"v_mfma_f32_16x16x32_bf16 v[64:67], v[36:39], a[208:211], v[64:67]\n"
"v_mfma_f32_16x16x32_bf16 v[68:71], v[36:39], a[212:215], v[68:71]\n"
"v_mfma_f32_16x16x32_bf16 v[72:75], v[36:39], a[216:219], v[72:75]\n"
"v_mfma_f32_16x16x32_bf16 v[76:79], v[36:39], a[220:223], v[76:79]\n"
"v_mfma_f32_16x16x32_bf16 v[64:67], v[40:43], a[224:227], v[64:67]\n"
"v_mfma_f32_16x16x32_bf16 v[68:71], v[40:43], a[228:231], v[68:71]\n"
"v_mfma_f32_16x16x32_bf16 v[72:75], v[40:43], a[232:235], v[72:75]\n"
"v_mfma_f32_16x16x32_bf16 v[76:79], v[40:43], a[236:239], v[76:79]\n"
"s_waitcnt lgkmcnt(0)\n"
"v_mfma_f32_16x16x32_bf16 v[64:67], v[44:47], a[240:243], v[64:67]\n"
"v_mfma_f32_16x16x32_bf16 v[68:71], v[44:47], a[244:247], v[68:71]\n"
"v_mfma_f32_16x16x32_bf16 v[72:75], v[44:47], a[248:251], v[72:75]\n"
"v_mfma_f32_16x16x32_bf16 v[76:79], v[44:47], a[252:255], v[76:79]\n"
"v_mfma_f32_16x16x32_bf16 v[64:67], v[48:51], a[0:3], v[64:67]\n"
"v_mfma_f32_16x16x32_bf16 v[68:71], v[48:51], a[4:7], v[68:71]\n"
"v_mfma_f32_16x16x32_bf16 v[72:75], v[48:51], a[8:11], v[72:75]\n"
"v_mfma_f32_16x16x32_bf16 v[76:79], v[48:51], a[12:15], v[76:79]\n"
"v_mfma_f32_16x16x32_bf16 v[64:67], v[52:55], a[16:19], v[64:67]\n"
"v_mfma_f32_16x16x32_bf16 v[68:71], v[52:55], a[20:23], v[68:71]\n"
"v_mfma_f32_16x16x32_bf16 v[72:75], v[52:55], a[24:27], v[72:75]\n"
"v_mfma_f32_16x16x32_bf16 v[76:79], v[52:55], a[28:31], v[76:79]\n"
"v_mfma_f32_16x16x32_bf16 v[64:67], v[56:59], a[32:35], v[64:67]\n"
"v_mfma_f32_16x16x32_bf16 v[68:71], v[56:59], a[36:39], v[68:71]\n"
"v_mfma_f32_16x16x32_bf16 v[72:75], v[56:59], a[40:43], v[72:75]\n"
"v_mfma_f32_16x16x32_bf16 v[76:79], v[56:59], a[44:47], v[76:79]\n"
"v_mfma_f32_16x16x32_bf16 v[64:67], v[60:63], a[48:51], v[64:67]\n"
"v_mfma_f32_16x16x32_bf16 v[68:71], v[60:63], a[52:55], v[68:71]\n"
"v_mfma_f32_16x16x32_bf16 v[72:75], v[60:63], a[56:59], v[72:75]\n"
"v_mfma_f32_16x16x32_bf16 v[76:79], v[60:63], a[60:63], v[76:79]\n"
"s_waitcnt vmcnt(2)\n"
"buffer_store_dwordx2 v[246:247], v4, s[20:23], s78 offen offset:512 sc1\n"
"s_nop 2\n"
"v_cvt_pk_bf16_f32 v64, v64, v65\n"
"v_cvt_pk_bf16_f32 v65, v66, v67\n"
"v_cvt_pk_bf16_f32 v66, v68, v69\n"
"v_cvt_pk_bf16_f32 v67, v70, v71\n"
"s_add_u32 s68, s30, 15089664\n"
"buffer_store_dwordx4 v[64:67], v1, s[20:23], s68 offen sc1\n"
"s_cmp_ge_u32 s30, 0x40000000\n"
"s_cselect_b64 vcc, -1, 0\n"
"v_cndmask_b32 v212, v212, v64, vcc\n"
"v_cndmask_b32 v213, v213, v65, vcc\n"
"v_cndmask_b32 v214, v214, v66, vcc\n"
"v_cndmask_b32 v215, v215, v67, vcc\n"
"v_cvt_pk_bf16_f32 v72, v72, v73\n"
"v_cvt_pk_bf16_f32 v73, v74, v75\n"
"v_cvt_pk_bf16_f32 v74, v76, v77\n"
"v_cvt_pk_bf16_f32 v75, v78, v79\n"
"s_add_u32 s69, s59, 15089664\n"
"buffer_store_dwordx4 v[72:75], v1, s[20:23], s69 offen sc1\n"
"s_cmp_ge_u32 s59, 0x40000000\n"
"s_cselect_b64 vcc, -1, 0\n"
"v_cndmask_b32 v212, v212, v72, vcc\n"
"v_cndmask_b32 v213, v213, v73, vcc\n"
"v_cndmask_b32 v214, v214, v74, vcc\n"
"v_cndmask_b32 v215, v215, v75, vcc\n"
"s_cmp_eq_u32 s34, 2\n"
"s_cselect_b32 s80, 256, 0\n"
"v_add_u32 v244, s80, v244\n"
"s_cmp_eq_u32 s34, 3\n"
"s_cselect_b32 s80, 0, 32768\n"
"s_add_u32 s74, s74, s80\n"
"s_add_u32 s75, s75, s80\n"
"buffer_load_dwordx2 v[238:239], v244, s[20:23], s52 offen offset:512 sc1\n"
"s_waitcnt vmcnt(1)\n"
"buffer_store_dwordx2 v[246:247], v4, s[20:23], s78 offen offset:768 sc1\n"
"s_waitcnt vmcnt(1)\n"
"v_cmp_ne_u64 vcc, s[70:71], v[238:239]\n"
"s_nop 1\n"
"s_cbranch_vccnz L_spinsB_%=\n"
"L_spinretsB_%=:\n"
"s_cmp_eq_u32 s34, 3\n"
"s_cbranch_scc1 L_only1_%=\n"
"buffer_load_dwordx4 v[132:135], v1, s[20:23], s74 offen offset:1024 sc1\n"
"buffer_load_dwordx4 v[136:139], v1, s[20:23], s74 offen offset:2048 sc1\n"
"buffer_load_dwordx4 v[140:143], v1, s[20:23], s74 offen offset:3072 sc1\n"
"buffer_load_dwordx4 v[144:147], v1, s[20:23], s75 offen offset:0 sc1\n"
"buffer_load_dwordx4 v[148:151], v1, s[20:23], s75 offen offset:1024 sc1\n"
"buffer_load_dwordx4 v[152:155], v1, s[20:23], s75 offen offset:2048 sc1\n"
"buffer_load_dwordx4 v[156:159], v1, s[20:23], s75 offen offset:3072 sc1\n"
"v_mfma_f32_16x16x32_bf16 v[168:171], v[234:237], v[208:211], 0\n"
"v_mfma_f32_16x16x32_bf16 v[172:175], v[232:235], v[208:211], 0\n"
"v_mfma_f32_16x16x32_bf16 v[168:171], v[234:237], v[100:103], v[168:171]\n"
"v_mfma_f32_16x16x32_bf16 v[172:175], v[232:235], v[100:103], v[172:175]\n"
"v_mfma_f32_16x16x32_bf16 v[168:171], v[234:237], v[104:107], v[168:171]\n"
"v_mfma_f32_16x16x32_bf16 v[172:175], v[232:235], v[104:107], v[172:175]\n"
"v_mfma_f32_16x16x32_bf16 v[168:171], v[234:237], v[108:111], v[168:171]\n"
"v_mfma_f32_16x16x32_bf16 v[172:175], v[232:235], v[108:111], v[172:175]\n"
"v_mfma_f32_16x16x32_bf16 v[168:171], v[234:237], v[112:115], v[168:171]\n"
"v_mfma_f32_16x16x32_bf16 v[172:175], v[232:235], v[112:115], v[172:175]\n"
"v_mfma_f32_16x16x32_bf16 v[168:171], v[234:237], v[116:119], v[168:171]\n"
"v_mfma_f32_16x16x32_bf16 v[172:175], v[232:235], v[116:119], v[172:175]\n"
"v_mfma_f32_16x16x32_bf16 v[168:171], v[234:237], v[120:123], v[168:171]\n"
"v_mfma_f32_16x16x32_bf16 v[172:175], v[232:235], v[120:123], v[172:175]\n"
"v_mfma_f32_16x16x32_bf16 v[168:171], v[234:237], v[124:127], v[168:171]\n"
"v_mfma_f32_16x16x32_bf16 v[172:175], v[232:235], v[124:127], v[172:175]\n"
"v_lshlrev_b32 v184, 16, v160\n"
"v_and_b32 v185, 0xffff0000, v160\n"
"s_nop 4\n"
"v_fmac_f32 v184, v168, v192\n"
"v_fmac_f32 v185, v169, v193\n"
"v_cvt_pk_bf16_f32 v160, v184, v185\n"
"v_lshlrev_b32 v186, 16, v161\n"
"v_and_b32 v187, 0xffff0000, v161\n"
"v_fmac_f32 v186, v170, v194\n"
"v_fmac_f32 v187, v171, v195\n"
"v_cvt_pk_bf16_f32 v161, v186, v187\n"
"buffer_store_dwordx2 v[160:161], v248, s[24:27], s76 offen offset:0 sc1\n"
"v_lshlrev_b32 v184, 16, v162\n"
"v_and_b32 v185, 0xffff0000, v162\n"
"v_fmac_f32 v184, v172, v196\n"
"v_fmac_f32 v185, v173, v197\n"
"v_cvt_pk_bf16_f32 v162, v184, v185\n"
"v_lshlrev_b32 v186, 16, v163\n"
"v_and_b32 v187, 0xffff0000, v163\n"
"v_fmac_f32 v186, v174, v198\n"
"v_fmac_f32 v187, v175, v199\n"
"v_cvt_pk_bf16_f32 v163, v186, v187\n"
"buffer_store_dwordx2 v[162:163], v249, s[24:27], s76 offen offset:0 sc1\n"
"v_mfma_f32_16x16x32_bf16 v[176:179], v[234:237], v[212:215], 0\n"
"v_mfma_f32_16x16x32_bf16 v[180:183], v[232:235], v[212:215], 0\n"
"s_waitcnt vmcnt(8)\n"
"v_mfma_f32_16x16x32_bf16 v[176:179], v[234:237], v[132:135], v[176:179]\n"
"v_mfma_f32_16x16x32_bf16 v[180:183], v[232:235], v[132:135], v[180:183]\n"
"s_waitcnt vmcnt(7)\n"
"v_mfma_f32_16x16x32_bf16 v[176:179], v[234:237], v[136:139], v[176:179]\n"
"v_mfma_f32_16x16x32_bf16 v[180:183], v[232:235], v[136:139], v[180:183]\n"
"s_waitcnt vmcnt(6)\n"
"v_mfma_f32_16x16x32_bf16 v[176:179], v[234:237], v[140:143], v[176:179]\n"
"v_mfma_f32_16x16x32_bf16 v[180:183], v[232:235], v[140:143], v[180:183]\n"
"s_waitcnt vmcnt(5)\n"
"v_mfma_f32_16x16x32_bf16 v[176:179], v[234:237], v[144:147], v[176:179]\n"
"v_mfma_f32_16x16x32_bf16 v[180:183], v[232:235], v[144:147], v[180:183]\n"
"s_waitcnt vmcnt(4)\n"
"v_mfma_f32_16x16x32_bf16 v[176:179], v[234:237], v[148:151], v[176:179]\n"
"v_mfma_f32_16x16x32_bf16 v[180:183], v[232:235], v[148:151], v[180:183]\n"
"s_waitcnt vmcnt(3)\n"
"v_mfma_f32_16x16x32_bf16 v[176:179], v[234:237], v[152:155], v[176:179]\n"
"v_mfma_f32_16x16x32_bf16 v[180:183], v[232:235], v[152:155], v[180:183]\n"
"s_waitcnt vmcnt(2)\n"
"v_mfma_f32_16x16x32_bf16 v[176:179], v[234:237], v[156:159], v[176:179]\n"
"v_mfma_f32_16x16x32_bf16 v[180:183], v[232:235], v[156:159], v[180:183]\n"
"v_lshlrev_b32 v184, 16, v164\n"
"v_and_b32 v185, 0xffff0000, v164\n"
"s_nop 4\n"
"v_fmac_f32 v184, v176, v200\n"
"v_fmac_f32 v185, v177, v201\n"
"v_cvt_pk_bf16_f32 v164, v184, v185\n"
"v_lshlrev_b32 v186, 16, v165\n"
"v_and_b32 v187, 0xffff0000, v165\n"
"v_fmac_f32 v186, v178, v202\n"
"v_fmac_f32 v187, v179, v203\n"
"v_cvt_pk_bf16_f32 v165, v186, v187\n"
"buffer_store_dwordx2 v[164:165], v248, s[24:27], s76 offen offset:512 sc1\n"
"v_lshlrev_b32 v184, 16, v166\n"
"v_and_b32 v185, 0xffff0000, v166\n"
"v_fmac_f32 v184, v180, v204\n"
"v_fmac_f32 v185, v181, v205\n"
"v_cvt_pk_bf16_f32 v166, v184, v185\n"
"v_lshlrev_b32 v186, 16, v167\n"
"v_and_b32 v187, 0xffff0000, v167\n"
"v_fmac_f32 v186, v182, v206\n"
"v_fmac_f32 v187, v183, v207\n"
"v_cvt_pk_bf16_f32 v167, v186, v187\n"
"buffer_store_dwordx2 v[166:167], v249, s[24:27], s76 offen offset:512 sc1\n"
"s_branch L_fin_%=\n"
"L_only1_%=:\n"
"buffer_load_dwordx4 v[100:103], v1, s[20:23], s74 offen offset:1024 sc1\n"
"buffer_load_dwordx4 v[104:107], v1, s[20:23], s74 offen offset:2048 sc1\n"
"buffer_load_dwordx4 v[108:111], v1, s[20:23], s74 offen offset:3072 sc1\n"
"buffer_load_dwordx4 v[112:115], v1, s[20:23], s75 offen offset:0 sc1\n"
"buffer_load_dwordx4 v[116:119], v1, s[20:23], s75 offen offset:1024 sc1\n"
"buffer_load_dwordx4 v[120:123], v1, s[20:23], s75 offen offset:2048 sc1\n"
"buffer_load_dwordx4 v[124:127], v1, s[20:23], s75 offen offset:3072 sc1\n"
"buffer_store_dwordx2 v[246:247], v4, s[20:23], s52 offen offset:1024 sc1\n"
"v_mfma_f32_16x16x32_bf16 v[168:171], v[234:237], v[208:211], 0\n"
"v_mfma_f32_16x16x32_bf16 v[172:175], v[232:235], v[208:211], 0\n"
"s_waitcnt vmcnt(7)\n"
"v_mfma_f32_16x16x32_bf16 v[168:171], v[234:237], v[100:103], v[168:171]\n"
"v_mfma_f32_16x16x32_bf16 v[172:175], v[232:235], v[100:103], v[172:175]\n"
"s_waitcnt vmcnt(6)\n"
"v_mfma_f32_16x16x32_bf16 v[168:171], v[234:237], v[104:107], v[168:171]\n"
"v_mfma_f32_16x16x32_bf16 v[172:175], v[232:235], v[104:107], v[172:175]\n"
"s_waitcnt vmcnt(5)\n"
"v_mfma_f32_16x16x32_bf16 v[168:171], v[234:237], v[108:111], v[168:171]\n"
"v_mfma_f32_16x16x32_bf16 v[172:175], v[232:235], v[108:111], v[172:175]\n"
"s_waitcnt vmcnt(4)\n"
"v_mfma_f32_16x16x32_bf16 v[168:171], v[234:237], v[112:115], v[168:171]\n"
"v_mfma_f32_16x16x32_bf16 v[172:175], v[232:235], v[112:115], v[172:175]\n"
"s_waitcnt vmcnt(3)\n"
"v_mfma_f32_16x16x32_bf16 v[168:171], v[234:237], v[116:119], v[168:171]\n"
"v_mfma_f32_16x16x32_bf16 v[172:175], v[232:235], v[116:119], v[172:175]\n"
"s_waitcnt vmcnt(2)\n"
"v_mfma_f32_16x16x32_bf16 v[168:171], v[234:237], v[120:123], v[168:171]\n"
"v_mfma_f32_16x16x32_bf16 v[172:175], v[232:235], v[120:123], v[172:175]\n"
"s_waitcnt vmcnt(1)\n"
"v_mfma_f32_16x16x32_bf16 v[168:171], v[234:237], v[124:127], v[168:171]\n"
"v_mfma_f32_16x16x32_bf16 v[172:175], v[232:235], v[124:127], v[172:175]\n"
"v_lshlrev_b32 v184, 16, v160\n"
"v_and_b32 v185, 0xffff0000, v160\n"
"s_nop 4\n"
"v_fmac_f32 v184, v168, v192\n"
"v_fmac_f32 v185, v169, v193\n"
"v_cvt_pk_bf16_f32 v160, v184, v185\n"
"v_lshlrev_b32 v186, 16, v161\n"
"v_and_b32 v187, 0xffff0000, v161\n"
"v_fmac_f32 v186, v170, v194\n"
"v_fmac_f32 v187, v171, v195\n"
"v_cvt_pk_bf16_f32 v161, v186, v187\n"
"buffer_store_dwordx2 v[160:161], v248, s[24:27], s76 offen offset:0 sc1\n"
"v_lshlrev_b32 v184, 16, v162\n"
"v_and_b32 v185, 0xffff0000, v162\n"
"v_fmac_f32 v184, v172, v196\n"
"v_fmac_f32 v185, v173, v197\n"
"v_cvt_pk_bf16_f32 v162, v184, v185\n"
"v_lshlrev_b32 v186, 16, v163\n"
"v_and_b32 v187, 0xffff0000, v163\n"
"v_fmac_f32 v186, v174, v198\n"
"v_fmac_f32 v187, v175, v199\n"
"v_cvt_pk_bf16_f32 v163, v186, v187\n"
"buffer_store_dwordx2 v[162:163], v249, s[24:27], s76 offen offset:0 sc1\n"
"s_branch L_fin_%=\n"
"L_spiny_%=:\n"
"s_mov_b32 s79, 0x40000\n"
"L_spinly_%=:\n"
"s_sleep 1\n"
"buffer_load_dwordx2 v[238:239], v244, s[20:23], s52 offen offset:0 sc1\n"
"s_waitcnt vmcnt(0)\n"
"v_cmp_ne_u64 vcc, s[70:71], v[238:239]\n"
"s_nop 1\n"
"s_cbranch_vccz L_spinrety_%=\n"
"s_sub_u32 s79, s79, 1\n"
"s_cmp_lg_u32 s79, 0\n"
"s_cbranch_scc1 L_spinly_%=\n"
"s_branch L_spinrety_%=\n"
"L_spinsA_%=:\n"
"s_mov_b32 s79, 0x40000\n"
"L_spinlsA_%=:\n"
"s_sleep 1\n"
"buffer_load_dwordx2 v[238:239], v244, s[20:23], s52 offen offset:256 sc1\n"
"s_waitcnt vmcnt(0)\n"
"v_cmp_ne_u64 vcc, s[70:71], v[238:239]\n"
"s_nop 1\n"
"s_cbranch_vccz L_spinretsA_%=\n"
"s_sub_u32 s79, s79, 1\n"
"s_cmp_lg_u32 s79, 0\n"
"s_cbranch_scc1 L_spinlsA_%=\n"
"s_branch L_spinretsA_%=\n"
"L_spinsB_%=:\n"
"s_mov_b32 s79, 0x40000\n"
"L_spinlsB_%=:\n"
"s_sleep 1\n"
"buffer_load_dwordx2 v[238:239], v244, s[20:23], s52 offen offset:512 sc1\n"
"s_waitcnt vmcnt(0)\n"
"v_cmp_ne_u64 vcc, s[70:71], v[238:239]\n"
"s_nop 1\n"
"s_cbranch_vccz L_spinretsB_%=\n"
"s_sub_u32 s79, s79, 1\n"
"s_cmp_lg_u32 s79, 0\n"
"s_cbranch_scc1 L_spinlsB_%=\n"
"s_branch L_spinretsB_%=\n"
"L_slow_%=:\n"
"s_waitcnt vmcnt(0) lgkmcnt(0)\n"
"s_mov_b32 s79, m0\n"
"s_nop 15\n"
"v_mov_b32 v64, 0\n"
"v_mov_b32 v65, 0\n"
"v_mov_b32 v66, 0\n"
"v_mov_b32 v67, 0\n"
"v_mov_b32 v68, 0\n"
"v_mov_b32 v69, 0\n"
"v_mov_b32 v70, 0\n"
"v_mov_b32 v71, 0\n"
"v_mov_b32 v72, 0\n"
"v_mov_b32 v73, 0\n"
"v_mov_b32 v74, 0\n"
"v_mov_b32 v75, 0\n"
"v_mov_b32 v76, 0\n"
"v_mov_b32 v77, 0\n"
"v_mov_b32 v78, 0\n"
"v_mov_b32 v79, 0\n"
"v_mov_b32 v80, 0\n"
"v_mov_b32 v81, 0\n"
"v_mov_b32 v82, 0\n"
"v_mov_b32 v83, 0\n"
"v_mov_b32 v84, 0\n"
"v_mov_b32 v85, 0\n"
"v_mov_b32 v86, 0\n"
"v_mov_b32 v87, 0\n"
"v_mov_b32 v88, 0\n"
"v_mov_b32 v89, 0\n"
"v_mov_b32 v90, 0\n"
"v_mov_b32 v91, 0\n"
"v_mov_b32 v92, 0\n"
"v_mov_b32 v93, 0\n"
"v_mov_b32 v94, 0\n"
"v_mov_b32 v95, 0\n"
"v_mov_b32 v96, 0\n"
"v_mov_b32 v97, 0\n"
"v_mov_b32 v98, 0\n"
"v_mov_b32 v99, 0\n"
"v_mov_b32 v100, 0\n"
"v_mov_b32 v101, 0\n"
"v_mov_b32 v102, 0\n"
"v_mov_b32 v103, 0\n"
"v_mov_b32 v104, 0\n"
"v_mov_b32 v105, 0\n"
"v_mov_b32 v106, 0\n"
"v_mov_b32 v107, 0\n"
"v_mov_b32 v108, 0\n"
"v_mov_b32 v109, 0\n"
"v_mov_b32 v110, 0\n"
"v_mov_b32 v111, 0\n"
"v_mov_b32 v112, 0\n"
"v_mov_b32 v113, 0\n"
"v_mov_b32 v114, 0\n"
"v_mov_b32 v115, 0\n"
"v_mov_b32 v116, 0\n"
"v_mov_b32 v117, 0\n"
"v_mov_b32 v118, 0\n"
"v_mov_b32 v119, 0\n"
"v_mov_b32 v120, 0\n"
"v_mov_b32 v121, 0\n"
"v_mov_b32 v122, 0\n"
"v_mov_b32 v123, 0\n"
"v_mov_b32 v124, 0\n"
"v_mov_b32 v125, 0\n"
"v_mov_b32 v126, 0\n"
"v_mov_b32 v127, 0\n"
"v_mov_b32 v128, 0\n"
"v_mov_b32 v129, 0\n"
"v_mov_b32 v130, 0\n"
"v_mov_b32 v131, 0\n"
"v_mov_b32 v132, 0\n"
"v_mov_b32 v133, 0\n"
"v_mov_b32 v134, 0\n"
"v_mov_b32 v135, 0\n"
"v_mov_b32 v136, 0\n"
"v_mov_b32 v137, 0\n"
"v_mov_b32 v138, 0\n"
"v_mov_b32 v139, 0\n"
"v_mov_b32 v140, 0\n"
"v_mov_b32 v141, 0\n"
"v_mov_b32 v142, 0\n"
"v_mov_b32 v143, 0\n"
"v_mov_b32 v144, 0\n"
"v_mov_b32 v145, 0\n"
"v_mov_b32 v146, 0\n"
"v_mov_b32 v147, 0\n"
"v_mov_b32 v148, 0\n"
"v_mov_b32 v149, 0\n"
"v_mov_b32 v150, 0\n"
"v_mov_b32 v151, 0\n"
"v_mov_b32 v152, 0\n"
"v_mov_b32 v153, 0\n"
"v_mov_b32 v154, 0\n"
"v_mov_b32 v155, 0\n"
"v_mov_b32 v156, 0\n"
"v_mov_b32 v157, 0\n"
"v_mov_b32 v158, 0\n"
"v_mov_b32 v159, 0\n"
"v_mov_b32 v160, 0\n"
"v_mov_b32 v161, 0\n"
"v_mov_b32 v162, 0\n"
"v_mov_b32 v163, 0\n"
"v_mov_b32 v164, 0\n"
"v_mov_b32 v165, 0\n"
"v_mov_b32 v166, 0\n"
"v_mov_b32 v167, 0\n"
"v_mov_b32 v168, 0\n"
"v_mov_b32 v169, 0\n"
"v_mov_b32 v170, 0\n"
"v_mov_b32 v171, 0\n"
"v_mov_b32 v172, 0\n"
"v_mov_b32 v173, 0\n"
"v_mov_b32 v174, 0\n"
"v_mov_b32 v175, 0\n"
"v_mov_b32 v176, 0\n"
"v_mov_b32 v177, 0\n"
"v_mov_b32 v178, 0\n"
"v_mov_b32 v179, 0\n"
"v_mov_b32 v180, 0\n"
"v_mov_b32 v181, 0\n"
"v_mov_b32 v182, 0\n"
"v_mov_b32 v183, 0\n"
"v_mov_b32 v184, 0\n"
"v_mov_b32 v185, 0\n"
"v_mov_b32 v186, 0\n"
"v_mov_b32 v187, 0\n"
"v_mov_b32 v188, 0\n"
"v_mov_b32 v189, 0\n"
"v_mov_b32 v190, 0\n"
"v_mov_b32 v191, 0\n"
"v_and_b32 v224, 15, v7\n"
"v_lshlrev_b32 v224, 9, v224\n"
"v_lshrrev_b32 v225, 4, v7\n"
"v_lshl_add_u32 v224, v225, 4, v224\n"
"s_lshr_b32 s83, s60, 1\n"
"s_lshl_b32 s83, s83, 14\n"
"s_and_b32 s82, s60, 1\n"
"s_lshl_b32 s82, s82, 11\n"
"s_add_u32 s83, s83, s82\n"
"s_add_u32 s83, s83, 98304\n"
"s_sub_u32 s82, s83, 65536\n"
"v_add_u32 v232, s82, v8\n"
"v_add_u32 v233, s82, v9\n"
"v_add_u32 v234, s82, v10\n"
"v_add_u32 v235, s82, v11\n"
"v_mov_b32 v250, 0\n"
"v_mov_b32 v251, 0\n"
"s_mov_b32 s80, s53\n"
"L_slowm_%=:\n"
"s_add_u32 s81, s80, 0\n"
"buffer_load_dwordx4 v[32:35], v224, s[36:39], s81 offen offset:0\n"
"buffer_load_dwordx4 v[36:39], v224, s[36:39], s81 offen offset:64\n"
"buffer_load_dwordx4 v[40:43], v224, s[36:39], s81 offen offset:128\n"
"buffer_load_dwordx4 v[44:47], v224, s[36:39], s81 offen offset:192\n"
"buffer_load_dwordx4 v[48:51], v224, s[36:39], s81 offen offset:256\n"
"buffer_load_dwordx4 v[52:55], v224, s[36:39], s81 offen offset:320\n"
"buffer_load_dwordx4 v[56:59], v224, s[36:39], s81 offen offset:384\n"
"buffer_load_dwordx4 v[60:63], v224, s[36:39], s81 offen offset:448\n"
"s_waitcnt vmcnt(7)\n"
"v_mfma_f32_16x16x32_bf16 v[192:195], v[32:35], a[112:115], v[208:211]\n"
"v_mfma_f32_16x16x32_bf16 v[200:203], v[32:35], a[116:119], v[212:215]\n"
"s_waitcnt vmcnt(6)\n"
"v_mfma_f32_16x16x32_bf16 v[192:195], v[36:39], a[120:123], v[192:195]\n"
"v_mfma_f32_16x16x32_bf16 v[200:203], v[36:39], a[124:127], v[200:203]\n"
"s_waitcnt vmcnt(5)\n"
"v_mfma_f32_16x16x32_bf16 v[192:195], v[40:43], a[128:131], v[192:195]\n"
"v_mfma_f32_16x16x32_bf16 v[200:203], v[40:43], a[132:135], v[200:203]\n"
"s_waitcnt vmcnt(4)\n"
"v_mfma_f32_16x16x32_bf16 v[192:195], v[44:47], a[136:139], v[192:195]\n"
"v_mfma_f32_16x16x32_bf16 v[200:203], v[44:47], a[140:143], v[200:203]\n"
"s_waitcnt vmcnt(3)\n"
"v_mfma_f32_16x16x32_bf16 v[192:195], v[48:51], a[144:147], v[192:195]\n"
"v_mfma_f32_16x16x32_bf16 v[200:203], v[48:51], a[148:151], v[200:203]\n"
"s_waitcnt vmcnt(2)\n"
"v_mfma_f32_16x16x32_bf16 v[192:195], v[52:55], a[152:155], v[192:195]\n"
"v_mfma_f32_16x16x32_bf16 v[200:203], v[52:55], a[156:159], v[200:203]\n"
"s_waitcnt vmcnt(1)\n"
"v_mfma_f32_16x16x32_bf16 v[192:195], v[56:59], a[160:163], v[192:195]\n"
"v_mfma_f32_16x16x32_bf16 v[200:203], v[56:59], a[164:167], v[200:203]\n"
"s_waitcnt vmcnt(0)\n"
"v_mfma_f32_16x16x32_bf16 v[192:195], v[60:63], a[168:171], v[192:195]\n"
"v_mfma_f32_16x16x32_bf16 v[200:203], v[60:63], a[172:175], v[200:203]\n"
"s_nop 7\n"
"s_add_u32 s81, s80, 8192\n"
"buffer_load_dwordx4 v[32:35], v224, s[36:39], s81 offen offset:0\n"
"buffer_load_dwordx4 v[36:39], v224, s[36:39], s81 offen offset:64\n"
"buffer_load_dwordx4 v[40:43], v224, s[36:39], s81 offen offset:128\n"
"buffer_load_dwordx4 v[44:47], v224, s[36:39], s81 offen offset:192\n"
"buffer_load_dwordx4 v[48:51], v224, s[36:39], s81 offen offset:256\n"
"buffer_load_dwordx4 v[52:55], v224, s[36:39], s81 offen offset:320\n"
"buffer_load_dwordx4 v[56:59], v224, s[36:39], s81 offen offset:384\n"
"buffer_load_dwordx4 v[60:63], v224, s[36:39], s81 offen offset:448\n"
"s_waitcnt vmcnt(7)\n"
"v_mfma_f32_16x16x32_bf16 v[196:199], v[32:35], a[112:115], v[208:211]\n"
"v_mfma_f32_16x16x32_bf16 v[204:207], v[32:35], a[116:119], v[212:215]\n"
"s_waitcnt vmcnt(6)\n"
"v_mfma_f32_16x16x32_bf16 v[196:199], v[36:39], a[120:123], v[196:199]\n"
"v_mfma_f32_16x16x32_bf16 v[204:207], v[36:39], a[124:127], v[204:207]\n"
"s_waitcnt vmcnt(5)\n"
"v_mfma_f32_16x16x32_bf16 v[196:199], v[40:43], a[128:131], v[196:199]\n"
"v_mfma_f32_16x16x32_bf16 v[204:207], v[40:43], a[132:135], v[204:207]\n"
"s_waitcnt vmcnt(4)\n"
"v_mfma_f32_16x16x32_bf16 v[196:199], v[44:47], a[136:139], v[196:199]\n"
"v_mfma_f32_16x16x32_bf16 v[204:207], v[44:47], a[140:143], v[204:207]\n"
"s_waitcnt vmcnt(3)\n"
"v_mfma_f32_16x16x32_bf16 v[196:199], v[48:51], a[144:147], v[196:199]\n"
"v_mfma_f32_16x16x32_bf16 v[204:207], v[48:51], a[148:151], v[204:207]\n"
"s_waitcnt vmcnt(2)\n"
"v_mfma_f32_16x16x32_bf16 v[196:199], v[52:55], a[152:155], v[196:199]\n"
"v_mfma_f32_16x16x32_bf16 v[204:207], v[52:55], a[156:159], v[204:207]\n"
"s_waitcnt vmcnt(1)\n"
"v_mfma_f32_16x16x32_bf16 v[196:199], v[56:59], a[160:163], v[196:199]\n"
"v_mfma_f32_16x16x32_bf16 v[204:207], v[56:59], a[164:167], v[204:207]\n"
"s_waitcnt vmcnt(0)\n"
"v_mfma_f32_16x16x32_bf16 v[196:199], v[60:63], a[168:171], v[196:199]\n"
"v_mfma_f32_16x16x32_bf16 v[204:207], v[60:63], a[172:175], v[204:207]\n"
"s_nop 7\n"
"v_max3_f32 v250, v192, v193, v250\n"
"v_max3_f32 v250, v194, v195, v250\n"
"v_max3_f32 v250, v196, v197, v250\n"
"v_max3_f32 v250, v198, v199, v250\n"
"v_max3_f32 v251, v200, v201, v251\n"
"v_max3_f32 v251, v202, v203, v251\n"
"v_max3_f32 v251, v204, v205, v251\n"
"v_max3_f32 v251, v206, v207, v251\n"
"s_add_u32 s80, s80, 16384\n"
"s_sub_u32 s81, s80, s53\n"
"s_cmp_lg_u32 s81, 262144\n"
"s_nop 7\n"
"s_cbranch_scc1 L_slowm_%=\n"
"v_mov_b32 v225, v250\n"
"s_nop 1\n"
"v_permlane32_swap_b32 v250, v225\n"
"v_max_f32 v250, v250, v225\n"
"v_mov_b32 v225, v250\n"
"s_nop 1\n"
"v_permlane16_swap_b32 v250, v225\n"
"v_max_f32 v250, v250, v225\n"
"v_sub_f32 v208, v208, v250\n"
"v_mov_b32 v216, 0\n"
"v_sub_f32 v209, v209, v250\n"
"v_mov_b32 v217, 0\n"
"v_sub_f32 v210, v210, v250\n"
"v_mov_b32 v218, 0\n"
"v_sub_f32 v211, v211, v250\n"
"v_mov_b32 v219, 0\n"
"v_mov_b32 v225, v251\n"
"s_nop 1\n"
"v_permlane32_swap_b32 v251, v225\n"
"v_max_f32 v251, v251, v225\n"
"v_mov_b32 v225, v251\n"
"s_nop 1\n"
"v_permlane16_swap_b32 v251, v225\n"
"v_max_f32 v251, v251, v225\n"
"v_sub_f32 v212, v212, v251\n"
"v_mov_b32 v220, 0\n"
"v_sub_f32 v213, v213, v251\n"
"v_mov_b32 v221, 0\n"
"v_sub_f32 v214, v214, v251\n"
"v_mov_b32 v222, 0\n"
"v_sub_f32 v215, v215, v251\n"
"v_mov_b32 v223, 0\n"
"s_mov_b32 s80, s53\n"
"s_nop 3\n"
"L_slowl_%=:\n"
"s_add_u32 s81, s80, 0\n"
"buffer_load_dwordx4 v[32:35], v224, s[36:39], s81 offen offset:0\n"
"buffer_load_dwordx4 v[36:39], v224, s[36:39], s81 offen offset:64\n"
"buffer_load_dwordx4 v[40:43], v224, s[36:39], s81 offen offset:128\n"
"buffer_load_dwordx4 v[44:47], v224, s[36:39], s81 offen offset:192\n"
"buffer_load_dwordx4 v[48:51], v224, s[36:39], s81 offen offset:256\n"
"buffer_load_dwordx4 v[52:55], v224, s[36:39], s81 offen offset:320\n"
"buffer_load_dwordx4 v[56:59], v224, s[36:39], s81 offen offset:384\n"
"buffer_load_dwordx4 v[60:63], v224, s[36:39], s81 offen offset:448\n"
"s_waitcnt vmcnt(7)\n"
"v_mfma_f32_16x16x32_bf16 v[192:195], v[32:35], a[112:115], v[208:211]\n"
"v_mfma_f32_16x16x32_bf16 v[200:203], v[32:35], a[116:119], v[212:215]\n"
"s_waitcnt vmcnt(6)\n"
"v_mfma_f32_16x16x32_bf16 v[192:195], v[36:39], a[120:123], v[192:195]\n"
"v_mfma_f32_16x16x32_bf16 v[200:203], v[36:39], a[124:127], v[200:203]\n"
"s_waitcnt vmcnt(5)\n"
"v_mfma_f32_16x16x32_bf16 v[192:195], v[40:43], a[128:131], v[192:195]\n"
"v_mfma_f32_16x16x32_bf16 v[200:203], v[40:43], a[132:135], v[200:203]\n"
"s_waitcnt vmcnt(4)\n"
"v_mfma_f32_16x16x32_bf16 v[192:195], v[44:47], a[136:139], v[192:195]\n"
"v_mfma_f32_16x16x32_bf16 v[200:203], v[44:47], a[140:143], v[200:203]\n"
"s_waitcnt vmcnt(3)\n"
"v_mfma_f32_16x16x32_bf16 v[192:195], v[48:51], a[144:147], v[192:195]\n"
"v_mfma_f32_16x16x32_bf16 v[200:203], v[48:51], a[148:151], v[200:203]\n"
"s_waitcnt vmcnt(2)\n"
"v_mfma_f32_16x16x32_bf16 v[192:195], v[52:55], a[152:155], v[192:195]\n"
"v_mfma_f32_16x16x32_bf16 v[200:203], v[52:55], a[156:159], v[200:203]\n"
"s_waitcnt vmcnt(1)\n"
"v_mfma_f32_16x16x32_bf16 v[192:195], v[56:59], a[160:163], v[192:195]\n"
"v_mfma_f32_16x16x32_bf16 v[200:203], v[56:59], a[164:167], v[200:203]\n"
"s_waitcnt vmcnt(0)\n"
"v_mfma_f32_16x16x32_bf16 v[192:195], v[60:63], a[168:171], v[192:195]\n"
"v_mfma_f32_16x16x32_bf16 v[200:203], v[60:63], a[172:175], v[200:203]\n"
"s_nop 7\n"
"s_add_u32 s81, s80, 8192\n"
"buffer_load_dwordx4 v[32:35], v224, s[36:39], s81 offen offset:0\n"
"buffer_load_dwordx4 v[36:39], v224, s[36:39], s81 offen offset:64\n"
"buffer_load_dwordx4 v[40:43], v224, s[36:39], s81 offen offset:128\n"
"buffer_load_dwordx4 v[44:47], v224, s[36:39], s81 offen offset:192\n"
"buffer_load_dwordx4 v[48:51], v224, s[36:39], s81 offen offset:256\n"
"buffer_load_dwordx4 v[52:55], v224, s[36:39], s81 offen offset:320\n"
"buffer_load_dwordx4 v[56:59], v224, s[36:39], s81 offen offset:384\n"
"buffer_load_dwordx4 v[60:63], v224, s[36:39], s81 offen offset:448\n"
"s_waitcnt vmcnt(7)\n"
"v_mfma_f32_16x16x32_bf16 v[196:199], v[32:35], a[112:115], v[208:211]\n"
"v_mfma_f32_16x16x32_bf16 v[204:207], v[32:35], a[116:119], v[212:215]\n"
"s_waitcnt vmcnt(6)\n"
"v_mfma_f32_16x16x32_bf16 v[196:199], v[36:39], a[120:123], v[196:199]\n"
"v_mfma_f32_16x16x32_bf16 v[204:207], v[36:39], a[124:127], v[204:207]\n"
"s_waitcnt vmcnt(5)\n"
"v_mfma_f32_16x16x32_bf16 v[196:199], v[40:43], a[128:131], v[196:199]\n"
"v_mfma_f32_16x16x32_bf16 v[204:207], v[40:43], a[132:135], v[204:207]\n"
"s_waitcnt vmcnt(4)\n"
"v_mfma_f32_16x16x32_bf16 v[196:199], v[44:47], a[136:139], v[196:199]\n"
"v_mfma_f32_16x16x32_bf16 v[204:207], v[44:47], a[140:143], v[204:207]\n"
"s_waitcnt vmcnt(3)\n"
"v_mfma_f32_16x16x32_bf16 v[196:199], v[48:51], a[144:147], v[196:199]\n"
"v_mfma_f32_16x16x32_bf16 v[204:207], v[48:51], a[148:151], v[204:207]\n"
"s_waitcnt vmcnt(2)\n"
"v_mfma_f32_16x16x32_bf16 v[196:199], v[52:55], a[152:155], v[196:199]\n"
"v_mfma_f32_16x16x32_bf16 v[204:207], v[52:55], a[156:159], v[204:207]\n"
"s_waitcnt vmcnt(1)\n"
"v_mfma_f32_16x16x32_bf16 v[196:199], v[56:59], a[160:163], v[196:199]\n"
"v_mfma_f32_16x16x32_bf16 v[204:207], v[56:59], a[164:167], v[204:207]\n"
"s_waitcnt vmcnt(0)\n"
"v_mfma_f32_16x16x32_bf16 v[196:199], v[60:63], a[168:171], v[196:199]\n"
"v_mfma_f32_16x16x32_bf16 v[204:207], v[60:63], a[172:175], v[204:207]\n"
"s_nop 7\n"
"v_exp_f32 v192, v192\n"
"v_exp_f32 v193, v193\n"
"v_exp_f32 v194, v194\n"
"v_exp_f32 v195, v195\n"
"v_exp_f32 v196, v196\n"
"v_exp_f32 v197, v197\n"
"v_exp_f32 v198, v198\n"
"v_exp_f32 v199, v199\n"
"v_cvt_pk_bf16_f32 v192, v192, v193\n"
"v_cvt_pk_bf16_f32 v193, v194, v195\n"
"v_cvt_pk_bf16_f32 v194, v196, v197\n"
"v_cvt_pk_bf16_f32 v195, v198, v199\n"
"v_exp_f32 v200, v200\n"
"v_exp_f32 v201, v201\n"
"v_exp_f32 v202, v202\n"
"v_exp_f32 v203, v203\n"
"v_exp_f32 v204, v204\n"
"v_exp_f32 v205, v205\n"
"v_exp_f32 v206, v206\n"
"v_exp_f32 v207, v207\n"
"v_cvt_pk_bf16_f32 v200, v200, v201\n"
"v_cvt_pk_bf16_f32 v201, v202, v203\n"
"v_cvt_pk_bf16_f32 v202, v204, v205\n"
"v_cvt_pk_bf16_f32 v203, v206, v207\n"
"v_mfma_f32_16x16x32_bf16 v[216:219], v[240:243], v[192:195], v[216:219]\n"
"s_nop 0\n"
"v_mfma_f32_16x16x32_bf16 v[220:223], v[240:243], v[200:203], v[220:223]\n"
"s_sub_u32 s81, s80, s53\n"
"s_cmp_eq_u32 s81, 245760\n"
"s_cbranch_scc1 L_slowe_%=\n"
"s_add_u32 s81, s80, 0\n"
"s_mov_b32 m0, s83\n"
"s_nop 0\n"
"buffer_load_dwordx4 v20, s[40:43], s81 offen offset:0 lds\n"
"buffer_load_dwordx4 v21, s[40:43], s81 offen offset:1024 lds\n"
"s_add_u32 m0, s83, 2048\n"
"s_nop 0\n"
"buffer_load_dwordx4 v22, s[40:43], s81 offen offset:2048 lds\n"
"buffer_load_dwordx4 v23, s[40:43], s81 offen offset:3072 lds\n"
"s_waitcnt vmcnt(0)\n"
"ds_read_b64_tr_b16 v[32:33], v232 offset:0\n"
"ds_read_b64_tr_b16 v[34:35], v232 offset:4096\n"
"ds_read_b64_tr_b16 v[36:37], v233 offset:0\n"
"ds_read_b64_tr_b16 v[38:39], v233 offset:4096\n"
"ds_read_b64_tr_b16 v[40:41], v234 offset:0\n"
"ds_read_b64_tr_b16 v[42:43], v234 offset:4096\n"
"ds_read_b64_tr_b16 v[44:45], v235 offset:0\n"
"ds_read_b64_tr_b16 v[46:47], v235 offset:4096\n"
"s_waitcnt lgkmcnt(0)\n"
"v_mfma_f32_16x16x32_bf16 v[64:67], v[32:35], v[192:195], v[64:67]\n"
"v_mfma_f32_16x16x32_bf16 v[72:75], v[32:35], v[200:203], v[72:75]\n"
"v_mfma_f32_16x16x32_bf16 v[68:71], v[36:39], v[192:195], v[68:71]\n"
"v_mfma_f32_16x16x32_bf16 v[76:79], v[36:39], v[200:203], v[76:79]\n"
"v_mfma_f32_16x16x32_bf16 v[80:83], v[40:43], v[192:195], v[80:83]\n"
"v_mfma_f32_16x16x32_bf16 v[88:91], v[40:43], v[200:203], v[88:91]\n"
"v_mfma_f32_16x16x32_bf16 v[84:87], v[44:47], v[192:195], v[84:87]\n"
"v_mfma_f32_16x16x32_bf16 v[92:95], v[44:47], v[200:203], v[92:95]\n"
"s_add_u32 s81, s80, 128\n"
"s_mov_b32 m0, s83\n"
"s_nop 0\n"
"buffer_load_dwordx4 v20, s[40:43], s81 offen offset:0 lds\n"
"buffer_load_dwordx4 v21, s[40:43], s81 offen offset:1024 lds\n"
"s_add_u32 m0, s83, 2048\n"
"s_nop 0\n"
"buffer_load_dwordx4 v22, s[40:43], s81 offen offset:2048 lds\n"
"buffer_load_dwordx4 v23, s[40:43], s81 offen offset:3072 lds\n"
"s_waitcnt vmcnt(0)\n"
"ds_read_b64_tr_b16 v[32:33], v232 offset:0\n"
"ds_read_b64_tr_b16 v[34:35], v232 offset:4096\n"
"ds_read_b64_tr_b16 v[36:37], v233 offset:0\n"
"ds_read_b64_tr_b16 v[38:39], v233 offset:4096\n"
"ds_read_b64_tr_b16 v[40:41], v234 offset:0\n"
"ds_read_b64_tr_b16 v[42:43], v234 offset:4096\n"
"ds_read_b64_tr_b16 v[44:45], v235 offset:0\n"
"ds_read_b64_tr_b16 v[46:47], v235 offset:4096\n"
"s_waitcnt lgkmcnt(0)\n"
"v_mfma_f32_16x16x32_bf16 v[96:99], v[32:35], v[192:195], v[96:99]\n"
"v_mfma_f32_16x16x32_bf16 v[104:107], v[32:35], v[200:203], v[104:107]\n"
"v_mfma_f32_16x16x32_bf16 v[100:103], v[36:39], v[192:195], v[100:103]\n"
"v_mfma_f32_16x16x32_bf16 v[108:111], v[36:39], v[200:203], v[108:111]\n"
"v_mfma_f32_16x16x32_bf16 v[112:115], v[40:43], v[192:195], v[112:115]\n"
"v_mfma_f32_16x16x32_bf16 v[120:123], v[40:43], v[200:203], v[120:123]\n"
"v_mfma_f32_16x16x32_bf16 v[116:119], v[44:47], v[192:195], v[116:119]\n"
"v_mfma_f32_16x16x32_bf16 v[124:127], v[44:47], v[200:203], v[124:127]\n"
"s_add_u32 s81, s80, 256\n"
"s_mov_b32 m0, s83\n"
"s_nop 0\n"
"buffer_load_dwordx4 v20, s[40:43], s81 offen offset:0 lds\n"
"buffer_load_dwordx4 v21, s[40:43], s81 offen offset:1024 lds\n"
"s_add_u32 m0, s83, 2048\n"
"s_nop 0\n"
"buffer_load_dwordx4 v22, s[40:43], s81 offen offset:2048 lds\n"
"buffer_load_dwordx4 v23, s[40:43], s81 offen offset:3072 lds\n"
"s_waitcnt vmcnt(0)\n"
"ds_read_b64_tr_b16 v[32:33], v232 offset:0\n"
"ds_read_b64_tr_b16 v[34:35], v232 offset:4096\n"
"ds_read_b64_tr_b16 v[36:37], v233 offset:0\n"
"ds_read_b64_tr_b16 v[38:39], v233 offset:4096\n"
"ds_read_b64_tr_b16 v[40:41], v234 offset:0\n"
"ds_read_b64_tr_b16 v[42:43], v234 offset:4096\n"
"ds_read_b64_tr_b16 v[44:45], v235 offset:0\n"
"ds_read_b64_tr_b16 v[46:47], v235 offset:4096\n"
"s_waitcnt lgkmcnt(0)\n"
"v_mfma_f32_16x16x32_bf16 v[128:131], v[32:35], v[192:195], v[128:131]\n"
"v_mfma_f32_16x16x32_bf16 v[136:139], v[32:35], v[200:203], v[136:139]\n"
"v_mfma_f32_16x16x32_bf16 v[132:135], v[36:39], v[192:195], v[132:135]\n"
"v_mfma_f32_16x16x32_bf16 v[140:143], v[36:39], v[200:203], v[140:143]\n"
"v_mfma_f32_16x16x32_bf16 v[144:147], v[40:43], v[192:195], v[144:147]\n"
"v_mfma_f32_16x16x32_bf16 v[152:155], v[40:43], v[200:203], v[152:155]\n"
"v_mfma_f32_16x16x32_bf16 v[148:151], v[44:47], v[192:195], v[148:151]\n"
"v_mfma_f32_16x16x32_bf16 v[156:159], v[44:47], v[200:203], v[156:159]\n"
"s_add_u32 s81, s80, 384\n"
"s_mov_b32 m0, s83\n"
"s_nop 0\n"
"buffer_load_dwordx4 v20, s[40:43], s81 offen offset:0 lds\n"
"buffer_load_dwordx4 v21, s[40:43], s81 offen offset:1024 lds\n"
"s_add_u32 m0, s83, 2048\n"
"s_nop 0\n"
"buffer_load_dwordx4 v22, s[40:43], s81 offen offset:2048 lds\n"
"buffer_load_dwordx4 v23, s[40:43], s81 offen offset:3072 lds\n"
"s_waitcnt vmcnt(0)\n"
"ds_read_b64_tr_b16 v[32:33], v232 offset:0\n"
"ds_read_b64_tr_b16 v[34:35], v232 offset:4096\n"
"ds_read_b64_tr_b16 v[36:37], v233 offset:0\n"
"ds_read_b64_tr_b16 v[38:39], v233 offset:4096\n"
"ds_read_b64_tr_b16 v[40:41], v234 offset:0\n"
"ds_read_b64_tr_b16 v[42:43], v234 offset:4096\n"
"ds_read_b64_tr_b16 v[44:45], v235 offset:0\n"
"ds_read_b64_tr_b16 v[46:47], v235 offset:4096\n"
"s_waitcnt lgkmcnt(0)\n"
"v_mfma_f32_16x16x32_bf16 v[160:163], v[32:35], v[192:195], v[160:163]\n"
"v_mfma_f32_16x16x32_bf16 v[168:171], v[32:35], v[200:203], v[168:171]\n"
"v_mfma_f32_16x16x32_bf16 v[164:167], v[36:39], v[192:195], v[164:167]\n"
"v_mfma_f32_16x16x32_bf16 v[172:175], v[36:39], v[200:203], v[172:175]\n"
"v_mfma_f32_16x16x32_bf16 v[176:179], v[40:43], v[192:195], v[176:179]\n"
"v_mfma_f32_16x16x32_bf16 v[184:187], v[40:43], v[200:203], v[184:187]\n"
"v_mfma_f32_16x16x32_bf16 v[180:183], v[44:47], v[192:195], v[180:183]\n"
"v_mfma_f32_16x16x32_bf16 v[188:191], v[44:47], v[200:203], v[188:191]\n"
"s_add_u32 s80, s80, 16384\n"
"s_nop 15\n"
"s_branch L_slowl_%=\n"
"L_slowe_%=:\n"
"s_waitcnt vmcnt(0) lgkmcnt(0)\n"
"s_mov_b32 m0, s79\n"
"s_nop 15\n"
"s_branch L_slowret_%=\n"
"L_fin_%=:\n"
"L_end_%=:\n"
      : "+{v0}"(tid)
      : [karg] "s"(__builtin_amdgcn_kernarg_segment_ptr()), [bid] "s"((unsigned)blockIdx.x)
      : "memory", "vcc", "scc", "v1","v2","v3","v4","v5","v6","v7","v8","v9","v10","v11","v12","v13","v14","v15","v16","v17","v18","v19","v20","v21","v22","v23","v24","v25","v26","v27","v28","v29","v30","v31","v32","v33","v34","v35","v36","v37","v38","v39","v40","v41","v42","v43","v44","v45","v46","v47","v48","v49","v50","v51","v52","v53","v54","v55","v56","v57","v58","v59","v60","v61","v62","v63","v64","v65","v66","v67","v68","v69","v70","v71","v72","v73","v74","v75","v76","v77","v78","v79","v80","v81","v82","v83","v84","v85","v86","v87","v88","v89","v90","v91","v92","v93","v94","v95","v96","v97","v98","v99","v100","v101","v102","v103","v104","v105","v106","v107","v108","v109","v110","v111","v112","v113","v114","v115","v116","v117","v118","v119","v120","v121","v122","v123","v124","v125","v126","v127","v128","v129","v130","v131","v132","v133","v134","v135","v136","v137","v138","v139","v140","v141","v142","v143","v144","v145","v146","v147","v148","v149","v150","v151","v152","v153","v154","v155","v156","v157","v158","v159","v160","v161","v162","v163","v164","v165","v166","v167","v168","v169","v170","v171","v172","v173","v174","v175","v176","v177","v178","v179","v180","v181","v182","v183","v184","v185","v186","v187","v188","v189","v190","v191","v192","v193","v194","v195","v196","v197","v198","v199","v200","v201","v202","v203","v204","v205","v206","v207","v208","v209","v210","v211","v212","v213","v214","v215","v216","v217","v218","v219","v220","v221","v222","v223","v224","v225","v226","v227","v228","v229","v230","v231","v232","v233","v234","v235","v236","v237","v238","v239","v240","v241","v242","v243","v244","v245","v246","v247","v248","v249","v250","v251","v252","v253","v254","v255","a0","a1","a2","a3","a4","a5","a6","a7","a8","a9","a10","a11","a12","a13","a14","a15","a16","a17","a18","a19","a20","a21","a22","a23","a24","a25","a26","a27","a28","a29","a30","a31","a32","a33","a34","a35","a36","a37","a38","a39","a40","a41","a42","a43","a44","a45","a46","a47","a48","a49","a50","a51","a52","a53","a54","a55","a56","a57","a58","a59","a60","a61","a62","a63","a64","a65","a66","a67","a68","a69","a70","a71","a72","a73","a74","a75","a76","a77","a78","a79","a80","a81","a82","a83","a84","a85","a86","a87","a88","a89","a90","a91","a92","a93","a94","a95","a96","a97","a98","a99","a100","a101","a102","a103","a104","a105","a106","a107","a108","a109","a110","a111","a112","a113","a114","a115","a116","a117","a118","a119","a120","a121","a122","a123","a124","a125","a126","a127","a128","a129","a130","a131","a132","a133","a134","a135","a136","a137","a138","a139","a140","a141","a142","a143","a144","a145","a146","a147","a148","a149","a150","a151","a152","a153","a154","a155","a156","a157","a158","a159","a160","a161","a162","a163","a164","a165","a166","a167","a168","a169","a170","a171","a172","a173","a174","a175","a176","a177","a178","a179","a180","a181","a182","a183","a184","a185","a186","a187","a188","a189","a190","a191","a192","a193","a194","a195","a196","a197","a198","a199","a200","a201","a202","a203","a204","a205","a206","a207","a208","a209","a210","a211","a212","a213","a214","a215","a216","a217","a218","a219","a220","a221","a222","a223","a224","a225","a226","a227","a228","a229","a230","a231","a232","a233","a234","a235","a236","a237","a238","a239","a240","a241","a242","a243","a244","a245","a246","a247","a248","a249","a250","a251","a252","a253","a254","a255","s4","s5","s6","s7","s8","s9","s10","s11","s12","s13","s14","s15","s16","s17","s18","s19","s20","s21","s22","s23","s24","s25","s26","s27","s28","s29","s30","s31","s33","s34","s35","s36","s37","s38","s39","s40","s41","s42","s43","s44","s45","s46","s47","s48","s49","s50","s51","s52","s53","s54","s55","s56","s57","s58","s59","s60","s61","s62","s63","s64","s65","s66","s67","s68","s69","s70","s71","s72","s73","s74","s75","s76","s77","s78","s79","s80","s81","s82","s83","s84","s85","s86","s87","s88","s89","s90","s91","s92","s93","s94","s95","s96","s97","s98","s99");
}

extern "C" void kernel_launch(void* const* d_in, const int* in_sizes, int n_in, void* d_out, int out_size, void* d_ws,
                              size_t ws_size, hipStream_t stream) {
  (void)in_sizes; (void)n_in; (void)out_size; (void)ws_size;
  (void)hipFuncSetAttribute((const void*)fused_block_kernel, hipFuncAttributeMaxDynamicSharedMemorySize, 163840);
  const unsigned F = 0x20000u;
  hipLaunchKernelGGL(fused_block_kernel, dim3(256), dim3(256), 163840, stream,
                     (const void*)d_in[0], 2048u * 448u * 2u, F, (const void*)d_in[3], 448u * 4096u * 2u, F,
                     (const void*)d_in[6], 448u * 1792u * 2u, F, d_ws, 30146560u, F,
                     (const void*)d_in[1], 8u * 2048u * 256u * 2u, F, (const void*)d_in[2], 8u * 2048u * 256u * 2u, F,
                     (const void*)d_in[7], 1792u * 448u * 2u, F, (const void*)d_in[4], 4096u * 448u * 2u, F,
                     d_out, 2048u * 448u * 2u, F);
}
```
